```python
import torch

def _softmax(x, axis=-1):
    return torch.softmax(x, dim=axis)
def _gelu(x):
    return torch.nn.functional.gelu(x, approximate="tanh")
def _silu(x):
    return torch.nn.functional.silu(x)
def _sigmoid(x):
    return torch.sigmoid(x)
def _layernorm(x, axis=-1, eps=1e-5):
    m = x.mean(dim=axis, keepdim=True)
    v = x.var(dim=axis, keepdim=True, unbiased=False)
    return (x - m) / torch.sqrt(v + eps)
def _scatter_add(tgt, idx, src, axis=0):
    return tgt.clone().index_add_(axis, idx, src)
def _segment_sum(src, seg, n):
    out = torch.zeros((n,) + src.shape[1:], dtype=src.dtype)
    return out.index_add_(0, seg, src)
def _pad(x, pw):
    flat = tuple(v for pair in reversed(pw) for v in pair)
    return torch.nn.functional.pad(x, flat)

kernel_name = 'standalone__mha_block_cached__near_ridge'

def setup_inputs():

    _w = (lambda a: (lambda t: t.to(torch.bfloat16) if t.is_floating_point() else t)(torch.as_tensor(a)))
    B, Sq, D = 8, 96, 512
    H, Sk, Dh = 32, 1024, 128
    return {
        "x": _w((lambda *s: torch.randn(*s, dtype=torch.bfloat16) * 0.8)(B, Sq, D)),
        "k_cache": _w((lambda *s: torch.randn(*s, dtype=torch.bfloat16) * 0.8)(B, H, Sk, Dh)),
        "v_cache": _w((lambda *s: torch.randn(*s, dtype=torch.bfloat16) * 0.05)(B, H, Sk, Dh)),
        "wq": _w((lambda *s: torch.randn(*s, dtype=torch.bfloat16) * 0.8)(D, H * Dh) * (D ** -0.5)),
        "wo": _w((lambda *s: torch.randn(*s, dtype=torch.bfloat16) * 0.05)(H * Dh, D) * ((H * Dh) ** -0.5)),
    }

def reference(x, k_cache, v_cache, wq, wo):
    B, Sq, D = x.shape
    H, Dh = 32, 128
    q = (lambda x, *d: x.permute(*d))((x @ wq).reshape(B, Sq, H, Dh), 0, 2, 1, 3)
    scores = torch.einsum("bhqd,bhkd->bhqk", q, k_cache) * (Dh ** -0.5)
    p = _softmax(scores, axis=-1)
    o = torch.einsum("bhqk,bhkd->bhqd", p, v_cache)
    return (lambda x, *d: x.permute(*d))(o, 0, 2, 1, 3).reshape(B, Sq, H * Dh) @ wo
```

```cpp
#include <hip/hip_runtime.h>
#include <hip/hip_bf16.h>

constexpr int B = 8, SQ = 96, D = 512, H = 32, SK = 1024, DH = 128;
typedef unsigned short u16;
__device__ __forceinline__ float b2f(u16 v) { return __uint_as_float(((unsigned)v) << 16); }
__device__ __forceinline__ u16 f2b(float f) { __hip_bfloat16 h = __float2bfloat16(f); return *reinterpret_cast<u16*>(&h); }

__global__ void gemm_naive(const u16* A, const u16* Bm, u16* C, int M, int N, int K) {
  int n = blockIdx.x * blockDim.x + threadIdx.x, m = blockIdx.y;
  if (n >= N) return;
  float acc = 0.f;
  for (int k = 0; k < K; ++k) acc += b2f(A[(size_t)m * K + k]) * b2f(Bm[(size_t)k * N + n]);
  C[(size_t)m * N + n] = f2b(acc);
}

__global__ void attn_naive(const u16* Q, const u16* Kc, const u16* Vc, u16* O) {
  __shared__ float s[SK];
  __shared__ float q[DH];
  __shared__ float red[256];
  int qi = blockIdx.x, h = blockIdx.y, b = blockIdx.z, t = threadIdx.x;
  if (t < DH) q[t] = b2f(Q[((size_t)b * SQ + qi) * (H * DH) + h * DH + t]);
  __syncthreads();
  const u16* Kb = Kc + ((size_t)b * H + h) * SK * DH;
  const u16* Vb = Vc + ((size_t)b * H + h) * SK * DH;
  float mx = -1e30f;
  for (int k = t; k < SK; k += 256) {
    float a = 0.f;
    for (int d = 0; d < DH; ++d) a += q[d] * b2f(Kb[(size_t)k * DH + d]);
    a *= 0.08838834764831845f; s[k] = a; mx = fmaxf(mx, a);
  }
  red[t] = mx; __syncthreads();
  for (int o = 128; o > 0; o >>= 1) { if (t < o) red[t] = fmaxf(red[t], red[t + o]); __syncthreads(); }
  mx = red[0]; __syncthreads();
  float sum = 0.f;
  for (int k = t; k < SK; k += 256) { float e = __expf(s[k] - mx); s[k] = e; sum += e; }
  red[t] = sum; __syncthreads();
  for (int o = 128; o > 0; o >>= 1) { if (t < o) red[t] += red[t + o]; __syncthreads(); }
  sum = red[0];
  if (t < DH) {
    float a = 0.f;
    for (int k = 0; k < SK; ++k) a += s[k] * b2f(Vb[(size_t)k * DH + t]);
    O[((size_t)b * SQ + qi) * (H * DH) + h * DH + t] = f2b(a / sum);
  }
}

extern "C" void kernel_launch(void* const* d_in, const int* in_sizes, int n_in,
                              void* d_out, int out_size, void* d_ws, size_t ws_size,
                              hipStream_t stream) {
  const u16* x = (const u16*)d_in[0]; const u16* k = (const u16*)d_in[1]; const u16* v = (const u16*)d_in[2];
  const u16* wq = (const u16*)d_in[3]; const u16* wo = (const u16*)d_in[4];
  u16* q = (u16*)d_ws; u16* o = q + (size_t)B * SQ * H * DH;
  gemm_naive<<<dim3(H * DH / 256, B * SQ), 256, 0, stream>>>(x, wq, q, B * SQ, H * DH, D);
  attn_naive<<<dim3(SQ, H, B), 256, 0, stream>>>(q, k, v, o);
  gemm_naive<<<dim3(D / 256, B * SQ), 256, 0, stream>>>(o, wo, (u16*)d_out, B * SQ, D, H * DH);
}
```

```cpp
#include <hip/hip_runtime.h>
#include <stdint.h>

__global__ __launch_bounds__(256, 1) void mha_block_cached_kernel(const void* x, const void* k, const void* v, const void* wq,
                                                                  const void* wo, void* out, void* ws) {
  __shared__ __attribute__((aligned(16))) char lds[163840];
  asm volatile(
      ".Lstart_%=:\n"
      "s_load_dwordx8 s[4:11], %0, 0x0\n"
      "s_load_dwordx4 s[12:15], %0, 0x20\n"
      "s_load_dwordx2 s[16:17], %0, 0x30\n"
      "v_lshrrev_b32 v2, 6, %2\n"
      "v_and_b32 v1, 63, %2\n"
      "s_and_b32 s21, %1, 7\n"
      "s_lshr_b32 s28, %1, 3\n"
      "v_readfirstlane_b32 s20, v2\n"
      "s_nop 1\n"
      "s_getpc_b64 s[30:31]\n"
      ".Lanchor0_%=:\n"
      "s_sub_u32 s30, s30, .Lanchor0_%=-.Lstart_%=\n"
      "s_subb_u32 s31, s31, 0\n"
      "s_and_b32 s29, s28, 7\n"
      "s_lshl_b32 s29, s29, 2\n"
      "s_add_u32 s29, s29, s20\n"
      "s_mul_i32 s29, s29, 1536\n"
      "v_min_u32 v2, 11, v1\n"
      "v_lshlrev_b32 v2, 7, v2\n"
      "v_add_u32 v2, s29, v2\n"
      "v_min_u32 v2, .Lend_%=-.Lstart_%=-4, v2\n"
      "global_load_dword v223, v2, s[30:31]\n"
      "s_and_b32 s22, s28, 3\n"
      "s_lshr_b32 s23, s28, 2\n"
      "s_lshl_b32 s24, s21, 2\n"
      "s_add_u32 s24, s24, s22\n"
      "s_lshl_b32 s25, s23, 5\n"
      "s_add_u32 s25, s25, s24\n"
      "s_xor_b32 s29, s22, 2\n"
      "s_and_b32 s26, s29, 1\n"
      "s_lshr_b32 s27, s29, 1\n"
      "v_bfe_u32 v2, v1, 2, 3\n"
      "v_lshrrev_b32 v5, 2, v2\n"
      "v_and_b32 v3, 3, v1\n"
      "v_xor_b32 v3, v3, v5\n"
      "v_lshrrev_b32 v5, 5, v1\n"
      "v_lshlrev_b32 v3, 4, v3\n"
      "v_lshl_add_u32 v3, v5, 6, v3\n"
      "s_and_b32 s29, s20, 1\n"
      "s_lshl_b32 s29, s29, 5\n"
      "s_lshl_b32 s94, s20, 11\n"
      "s_mul_i32 s95, s20, 3072\n"
      "v_xor_b32 v4, s29, v3\n"
      "s_mul_i32 s29, s20, 24576\n"
      "v_lshl_add_u32 v5, v2, 10, v4\n"
      "v_add_u32 v212, s29, v5\n"
      "v_xor_b32 v5, 32, v212\n"
      "v_add_u32 v213, 7168, v5\n"
      "v_add_u32 v214, 14336, v212\n"
      "s_lshl_b32 s29, s20, 16\n"
      "s_add_u32 s29, s29, 1024\n"
      "v_lshl_add_u32 v5, v2, 13, v4\n"
      "v_add_u32 v215, s29, v5\n"
      "v_subrev_u32 v216, 896, v215\n"
      "s_add_u32 s29, s94, 1024\n"
      "v_lshl_add_u32 v5, v2, 8, v4\n"
      "v_add_u32 v217, s29, v5\n"
      "v_subrev_u32 v218, 896, v217\n"
      "s_mul_i32 s29, s25, 0\n"
      "s_and_b32 s29, s29, 31\n"
      "s_lshl_b32 s77, s29, 13\n"
      "s_xor_b32 s29, s22, 0\n"
      "s_lshl_b32 s29, s29, 1\n"
      "s_add_u32 s29, s29, s23\n"
      "s_and_b32 s29, s29, 7\n"
      "s_lshl_b32 s18, s29, 7\n"
      "s_lshl_b32 s19, s29, 19\n"
      "s_mul_i32 s29, s23, 98304\n"
      "s_lshl_b32 s30, s24, 8\n"
      "s_lshl_b32 s71, s25, 18\n"
      "s_sub_u32 s31, 4195328, s30\n"
      "s_waitcnt lgkmcnt(0)\n"
      "s_add_u32 s96, s4, s29\n"
      "s_addc_u32 s97, s5, 0\n"
      "s_mov_b32 s98, 98304\n"
      "s_mov_b32 s99, 0x20000\n"
      "s_add_u32 s36, s10, s30\n"
      "s_addc_u32 s37, s11, 0\n"
      "s_mov_b32 s38, s31\n"
      "s_mov_b32 s39, 0x20000\n"
      "s_sub_u32 s36, s36, 1024\n"
      "s_subb_u32 s37, s37, 0\n"
      "s_add_u32 m0, s95, 139264\n"
      "s_nop 0\n"
      "buffer_load_dwordx4 v212, s[96:99], s18 offen lds\n"
      "buffer_load_dwordx4 v213, s[96:99], s18 offen offset:1024 lds\n"
      "buffer_load_dwordx4 v214, s[96:99], s18 offen offset:2048 lds\n"
      "s_add_u32 s18, s18, 128\n"
      "s_and_b32 s18, s18, 1023\n"
      "s_add_u32 m0, s94, 98304\n"
      "s_nop 0\n"
      "buffer_load_dwordx4 v215, s[36:39], s19 offen lds\n"
      "buffer_load_dwordx4 v216, s[36:39], s19 offen offset:1024 lds\n"
      "s_add_u32 s19, s19, 0x40000\n"
      "s_and_b32 s19, s19, 0x3fffff\n"
      "s_add_u32 s40, s6, s71\n"
      "s_addc_u32 s41, s7, 0\n"
      "s_mov_b32 s42, 263168\n"
      "s_mov_b32 s43, 0x20000\n"
      "s_sub_u32 s40, s40, 1024\n"
      "s_subb_u32 s41, s41, 0\n"
      "s_add_u32 s44, s8, s71\n"
      "s_addc_u32 s45, s9, 0\n"
      "s_mov_b32 s46, 263168\n"
      "s_mov_b32 s47, 0x20000\n"
      "s_sub_u32 s44, s44, 1024\n"
      "s_subb_u32 s45, s45, 0\n"
      "s_mov_b32 s52, s16\n"
      "s_mov_b32 s53, s17\n"
      "s_mov_b32 s54, 27262976\n"
      "s_mov_b32 s55, 0x20000\n"
      "s_mov_b32 s56, s14\n"
      "s_mov_b32 s57, s15\n"
      "s_mov_b32 s58, 786432\n"
      "s_mov_b32 s59, 0x20000\n"
      "s_lshl_b32 s29, s23, 7\n"
      "v_mov_b32 v5, 0\n"
      "buffer_load_dwordx2 v[220:221], v5, s[52:55], s29 offen sc1\n"
      "s_add_u32 m0, s94, 106496\n"
      "s_nop 0\n"
      "buffer_load_dwordx4 v215, s[36:39], s19 offen lds\n"
      "buffer_load_dwordx4 v216, s[36:39], s19 offen offset:1024 lds\n"
      "s_add_u32 s19, s19, 0x40000\n"
      "s_and_b32 s19, s19, 0x3fffff\n"
      "s_add_u32 m0, s95, 151552\n"
      "s_nop 0\n"
      "buffer_load_dwordx4 v212, s[96:99], s18 offen lds\n"
      "buffer_load_dwordx4 v213, s[96:99], s18 offen offset:1024 lds\n"
      "buffer_load_dwordx4 v214, s[96:99], s18 offen offset:2048 lds\n"
      "s_add_u32 s18, s18, 128\n"
      "s_and_b32 s18, s18, 1023\n"
      "s_add_u32 m0, s94, 0\n"
      "s_add_u32 s66, s77, 0\n"
      "s_and_b32 s66, s66, 0x3ffff\n"
      "buffer_load_dwordx4 v217, s[40:43], s66 offen nt lds\n"
      "buffer_load_dwordx4 v218, s[40:43], s66 offen offset:1024 nt lds\n"
      "s_add_u32 m0, s94, 114688\n"
      "s_nop 0\n"
      "buffer_load_dwordx4 v215, s[36:39], s19 offen lds\n"
      "buffer_load_dwordx4 v216, s[36:39], s19 offen offset:1024 lds\n"
      "s_add_u32 s19, s19, 0x40000\n"
      "s_and_b32 s19, s19, 0x3fffff\n"
      "s_add_u32 m0, s94, 122880\n"
      "s_nop 0\n"
      "buffer_load_dwordx4 v215, s[36:39], s19 offen lds\n"
      "buffer_load_dwordx4 v216, s[36:39], s19 offen offset:1024 lds\n"
      "s_add_u32 s19, s19, 0x40000\n"
      "s_and_b32 s19, s19, 0x3fffff\n"
      "s_add_u32 m0, s94, 131072\n"
      "s_nop 0\n"
      "buffer_load_dwordx4 v215, s[36:39], s19 offen lds\n"
      "buffer_load_dwordx4 v216, s[36:39], s19 offen offset:1024 lds\n"
      "s_add_u32 s19, s19, 0x40000\n"
      "s_and_b32 s19, s19, 0x3fffff\n"
      "s_add_u32 m0, s94, 8192\n"
      "s_add_u32 s66, s77, 8192\n"
      "s_and_b32 s66, s66, 0x3ffff\n"
      "buffer_load_dwordx4 v217, s[40:43], s66 offen nt lds\n"
      "buffer_load_dwordx4 v218, s[40:43], s66 offen offset:1024 nt lds\n"
      "s_add_u32 m0, s94, 49152\n"
      "s_add_u32 s66, s77, 0\n"
      "s_and_b32 s66, s66, 0x3ffff\n"
      "buffer_load_dwordx4 v217, s[44:47], s66 offen nt lds\n"
      "buffer_load_dwordx4 v218, s[44:47], s66 offen offset:1024 nt lds\n"
      "s_add_u32 m0, s94, 16384\n"
      "s_add_u32 s66, s77, 16384\n"
      "s_and_b32 s66, s66, 0x3ffff\n"
      "buffer_load_dwordx4 v217, s[40:43], s66 offen nt lds\n"
      "buffer_load_dwordx4 v218, s[40:43], s66 offen offset:1024 nt lds\n"
      "s_cmp_lg_u32 s20, 3\n"
      "s_cbranch_scc1 .Lcompute_%=\n"
      "v_xor_b32 v4, 32, v3\n"
      "v_lshl_add_u32 v6, v2, 8, v3\n"
      "v_lshl_add_u32 v7, v2, 8, v4\n"
      "v_add_u32 v84, 1024, v6\n"
      "v_add_u32 v85, 128, v6\n"
      "v_add_u32 v86, 1024, v7\n"
      "v_add_u32 v87, 128, v7\n"
      "v_add_u32 v88, 5120, v6\n"
      "v_add_u32 v89, 4224, v6\n"
      "v_add_u32 v90, 5120, v7\n"
      "v_add_u32 v91, 4224, v7\n"
      "v_lshl_add_u32 v6, v2, 10, v3\n"
      "v_lshl_add_u32 v7, v2, 10, v4\n"
      "v_add_u32 v64, 0, v6\n"
      "v_add_u32 v65, 7168, v7\n"
      "v_add_u32 v66, 14336, v6\n"
      "v_add_u32 v67, 21504, v7\n"
      "v_add_u32 v68, 32768, v6\n"
      "v_add_u32 v69, 39936, v7\n"
      "v_add_u32 v70, 47104, v6\n"
      "v_add_u32 v71, 54272, v7\n"
      "v_add_u32 v72, 65536, v6\n"
      "v_add_u32 v73, 72704, v7\n"
      "v_add_u32 v74, 79872, v6\n"
      "v_add_u32 v75, 87040, v7\n"
      "s_lshl_b32 s29, s21, 19\n"
      "s_lshl_b32 s30, s27, 9\n"
      "s_add_u32 s29, s29, s30\n"
      "s_sub_u32 s30, 524288, s30\n"
      "s_add_u32 s48, s12, s29\n"
      "s_addc_u32 s49, s13, 0\n"
      "s_mov_b32 s50, s30\n"
      "s_mov_b32 s51, 0x20000\n"
      "v_lshrrev_b32 v5, 5, v1\n"
      "v_and_b32 v6, 31, v1\n"
      "v_lshlrev_b32 v7, 2, v5\n"
      "v_xor_b32 v6, v6, v7\n"
      "v_lshlrev_b32 v6, 4, v6\n"
      "v_lshl_add_u32 v6, v5, 10, v6\n"
      "v_xor_b32 v7, 128, v6\n"
      "v_add_u32 v92, 0, v6\n"
      "v_add_u32 v93, 1024, v7\n"
      "v_add_u32 v94, 2048, v6\n"
      "v_add_u32 v95, 3072, v7\n"
      "v_add_u32 v96, 16384, v6\n"
      "v_add_u32 v97, 17408, v7\n"
      "v_add_u32 v98, 18432, v6\n"
      "v_add_u32 v99, 19456, v7\n"
      "v_lshrrev_b32 v2, 2, v1\n"
      "v_and_b32 v3, 3, v1\n"
      "v_and_b32 v4, 3, v2\n"
      "s_lshl_b32 s29, s20, 3\n"
      "v_lshlrev_b32 v4, 2, v4\n"
      "v_or_b32 v3, s29, v3\n"
      "v_xor_b32 v3, v3, v4\n"
      "v_lshlrev_b32 v3, 4, v3\n"
      "v_lshl_add_u32 v3, v2, 9, v3\n"
      "v_add_u32 v14, 98304, v3\n"
      "v_xor_b32 v15, 64, v14\n"
      "v_lshlrev_b32 v21, 4, v1\n"
      "s_lshl_b32 s29, s26, 1\n"
      "s_add_u32 s29, s29, s23\n"
      "s_and_b32 s29, s29, 3\n"
      "s_lshl_b32 s74, s29, 17\n"
      "s_sub_u32 s29, s22, s29\n"
      "s_and_b32 s76, s29, 3\n"
      "s_waitcnt vmcnt(20)\n"
      "s_barrier\n"
      "s_waitcnt vmcnt(17)\n"
      "s_barrier\n"
      "s_add_u32 m0, s95, 139264\n"
      "s_nop 0\n"
      "buffer_load_dwordx4 v212, s[96:99], s18 offen lds\n"
      "buffer_load_dwordx4 v213, s[96:99], s18 offen offset:1024 lds\n"
      "buffer_load_dwordx4 v214, s[96:99], s18 offen offset:2048 lds\n"
      "s_add_u32 s18, s18, 128\n"
      "s_and_b32 s18, s18, 1023\n"
      "s_add_u32 m0, s94, 98304\n"
      "s_nop 0\n"
      "buffer_load_dwordx4 v215, s[36:39], s19 offen lds\n"
      "buffer_load_dwordx4 v216, s[36:39], s19 offen offset:1024 lds\n"
      "s_add_u32 s19, s19, 0x40000\n"
      "s_and_b32 s19, s19, 0x3fffff\n"
      "s_waitcnt vmcnt(15)\n"
      "s_barrier\n"
      "s_add_u32 m0, s94, 106496\n"
      "s_nop 0\n"
      "buffer_load_dwordx4 v215, s[36:39], s19 offen lds\n"
      "buffer_load_dwordx4 v216, s[36:39], s19 offen offset:1024 lds\n"
      "s_add_u32 s19, s19, 0x40000\n"
      "s_and_b32 s19, s19, 0x3fffff\n"
      "s_waitcnt vmcnt(15)\n"
      "s_barrier\n"
      "s_add_u32 m0, s95, 151552\n"
      "s_nop 0\n"
      "buffer_load_dwordx4 v212, s[96:99], s18 offen lds\n"
      "buffer_load_dwordx4 v213, s[96:99], s18 offen offset:1024 lds\n"
      "buffer_load_dwordx4 v214, s[96:99], s18 offen offset:2048 lds\n"
      "s_add_u32 s18, s18, 128\n"
      "s_and_b32 s18, s18, 1023\n"
      "s_add_u32 m0, s94, 114688\n"
      "s_nop 0\n"
      "buffer_load_dwordx4 v215, s[36:39], s19 offen lds\n"
      "buffer_load_dwordx4 v216, s[36:39], s19 offen offset:1024 lds\n"
      "s_add_u32 s19, s19, 0x40000\n"
      "s_and_b32 s19, s19, 0x3fffff\n"
      "s_waitcnt vmcnt(9)\n"
      "s_barrier\n"
      "s_add_u32 m0, s94, 122880\n"
      "s_nop 0\n"
      "buffer_load_dwordx4 v215, s[36:39], s19 offen lds\n"
      "buffer_load_dwordx4 v216, s[36:39], s19 offen offset:1024 lds\n"
      "s_add_u32 s19, s19, 0x40000\n"
      "s_and_b32 s19, s19, 0x3fffff\n"
      "s_add_u32 m0, s94, 57344\n"
      "s_add_u32 s66, s77, 8192\n"
      "s_and_b32 s66, s66, 0x3ffff\n"
      "buffer_load_dwordx4 v217, s[44:47], s66 offen nt lds\n"
      "buffer_load_dwordx4 v218, s[44:47], s66 offen offset:1024 nt lds\n"
      "s_add_u32 m0, s94, 24576\n"
      "s_add_u32 s66, s77, 24576\n"
      "s_and_b32 s66, s66, 0x3ffff\n"
      "buffer_load_dwordx4 v217, s[40:43], s66 offen nt lds\n"
      "buffer_load_dwordx4 v218, s[40:43], s66 offen offset:1024 nt lds\n"
      "s_add_u32 m0, s94, 65536\n"
      "s_add_u32 s66, s77, 16384\n"
      "s_and_b32 s66, s66, 0x3ffff\n"
      "buffer_load_dwordx4 v217, s[44:47], s66 offen nt lds\n"
      "buffer_load_dwordx4 v218, s[44:47], s66 offen offset:1024 nt lds\n"
      "s_waitcnt vmcnt(15)\n"
      "s_barrier\n"
      "s_add_u32 m0, s95, 139264\n"
      "s_nop 0\n"
      "buffer_load_dwordx4 v212, s[96:99], s18 offen lds\n"
      "buffer_load_dwordx4 v213, s[96:99], s18 offen offset:1024 lds\n"
      "buffer_load_dwordx4 v214, s[96:99], s18 offen offset:2048 lds\n"
      "s_add_u32 s18, s18, 128\n"
      "s_and_b32 s18, s18, 1023\n"
      "s_add_u32 m0, s94, 131072\n"
      "s_nop 0\n"
      "buffer_load_dwordx4 v215, s[36:39], s19 offen lds\n"
      "buffer_load_dwordx4 v216, s[36:39], s19 offen offset:1024 lds\n"
      "s_add_u32 s19, s19, 0x40000\n"
      "s_and_b32 s19, s19, 0x3fffff\n"
      "s_waitcnt vmcnt(15)\n"
      "s_barrier\n"
      "s_add_u32 m0, s94, 98304\n"
      "s_nop 0\n"
      "buffer_load_dwordx4 v215, s[36:39], s19 offen lds\n"
      "buffer_load_dwordx4 v216, s[36:39], s19 offen offset:1024 lds\n"
      "s_add_u32 s19, s19, 0x40000\n"
      "s_and_b32 s19, s19, 0x3fffff\n"
      "s_waitcnt vmcnt(15)\n"
      "s_barrier\n"
      "s_add_u32 m0, s95, 151552\n"
      "s_nop 0\n"
      "buffer_load_dwordx4 v212, s[96:99], s18 offen lds\n"
      "buffer_load_dwordx4 v213, s[96:99], s18 offen offset:1024 lds\n"
      "buffer_load_dwordx4 v214, s[96:99], s18 offen offset:2048 lds\n"
      "s_add_u32 s18, s18, 128\n"
      "s_and_b32 s18, s18, 1023\n"
      "s_add_u32 m0, s94, 106496\n"
      "s_nop 0\n"
      "buffer_load_dwordx4 v215, s[36:39], s19 offen lds\n"
      "buffer_load_dwordx4 v216, s[36:39], s19 offen offset:1024 lds\n"
      "s_add_u32 s19, s19, 0x40000\n"
      "s_and_b32 s19, s19, 0x3fffff\n"
      "s_waitcnt vmcnt(9)\n"
      "s_barrier\n"
      "s_add_u32 m0, s94, 114688\n"
      "s_nop 0\n"
      "buffer_load_dwordx4 v215, s[36:39], s19 offen lds\n"
      "buffer_load_dwordx4 v216, s[36:39], s19 offen offset:1024 lds\n"
      "s_add_u32 s19, s19, 0x40000\n"
      "s_and_b32 s19, s19, 0x3fffff\n"
      "s_add_u32 m0, s94, 32768\n"
      "s_add_u32 s66, s77, 32768\n"
      "s_and_b32 s66, s66, 0x3ffff\n"
      "buffer_load_dwordx4 v217, s[40:43], s66 offen nt lds\n"
      "buffer_load_dwordx4 v218, s[40:43], s66 offen offset:1024 nt lds\n"
      "s_add_u32 m0, s94, 73728\n"
      "s_add_u32 s66, s77, 24576\n"
      "s_and_b32 s66, s66, 0x3ffff\n"
      "buffer_load_dwordx4 v217, s[44:47], s66 offen nt lds\n"
      "buffer_load_dwordx4 v218, s[44:47], s66 offen offset:1024 nt lds\n"
      "s_waitcnt vmcnt(13)\n"
      "s_barrier\n"
      "s_add_u32 m0, s95, 139264\n"
      "s_nop 0\n"
      "buffer_load_dwordx4 v212, s[96:99], s18 offen lds\n"
      "buffer_load_dwordx4 v213, s[96:99], s18 offen offset:1024 lds\n"
      "buffer_load_dwordx4 v214, s[96:99], s18 offen offset:2048 lds\n"
      "s_add_u32 s18, s18, 128\n"
      "s_and_b32 s18, s18, 1023\n"
      "s_add_u32 m0, s94, 122880\n"
      "s_nop 0\n"
      "buffer_load_dwordx4 v215, s[36:39], s19 offen lds\n"
      "buffer_load_dwordx4 v216, s[36:39], s19 offen offset:1024 lds\n"
      "s_add_u32 s19, s19, 0x40000\n"
      "s_and_b32 s19, s19, 0x3fffff\n"
      "s_waitcnt vmcnt(13)\n"
      "s_barrier\n"
      "s_add_u32 m0, s94, 131072\n"
      "s_nop 0\n"
      "buffer_load_dwordx4 v215, s[36:39], s19 offen lds\n"
      "buffer_load_dwordx4 v216, s[36:39], s19 offen offset:1024 lds\n"
      "s_add_u32 s19, s19, 0x40000\n"
      "s_and_b32 s19, s19, 0x3fffff\n"
      "s_waitcnt vmcnt(13)\n"
      "s_barrier\n"
      "s_add_u32 m0, s95, 151552\n"
      "s_nop 0\n"
      "buffer_load_dwordx4 v212, s[96:99], s18 offen lds\n"
      "buffer_load_dwordx4 v213, s[96:99], s18 offen offset:1024 lds\n"
      "buffer_load_dwordx4 v214, s[96:99], s18 offen offset:2048 lds\n"
      "s_add_u32 s18, s18, 128\n"
      "s_and_b32 s18, s18, 1023\n"
      "s_add_u32 m0, s94, 98304\n"
      "s_nop 0\n"
      "buffer_load_dwordx4 v215, s[36:39], s19 offen lds\n"
      "buffer_load_dwordx4 v216, s[36:39], s19 offen offset:1024 lds\n"
      "s_add_u32 s19, s19, 0x40000\n"
      "s_and_b32 s19, s19, 0x3fffff\n"
      "s_add_u32 m0, s94, 40960\n"
      "s_add_u32 s66, s77, 40960\n"
      "s_and_b32 s66, s66, 0x3ffff\n"
      "buffer_load_dwordx4 v217, s[40:43], s66 offen nt lds\n"
      "buffer_load_dwordx4 v218, s[40:43], s66 offen offset:1024 nt lds\n"
      "s_add_u32 m0, s94, 81920\n"
      "s_add_u32 s66, s77, 32768\n"
      "s_and_b32 s66, s66, 0x3ffff\n"
      "buffer_load_dwordx4 v217, s[44:47], s66 offen nt lds\n"
      "buffer_load_dwordx4 v218, s[44:47], s66 offen offset:1024 nt lds\n"
      "s_waitcnt vmcnt(13)\n"
      "s_barrier\n"
      "s_mov_b32 m0, 106496\n"
      "s_add_u32 s66, s74, 0\n"
      "buffer_load_dwordx4 v92, s[48:51], s66 offen lds\n"
      "buffer_load_dwordx4 v93, s[48:51], s66 offen offset:1024 lds\n"
      "buffer_load_dwordx4 v94, s[48:51], s66 offen offset:2048 lds\n"
      "buffer_load_dwordx4 v95, s[48:51], s66 offen offset:3072 lds\n"
      "s_mov_b32 m0, 110592\n"
      "s_nop 0\n"
      "buffer_load_dwordx4 v96, s[48:51], s66 offen lds\n"
      "buffer_load_dwordx4 v97, s[48:51], s66 offen offset:1024 lds\n"
      "buffer_load_dwordx4 v98, s[48:51], s66 offen offset:2048 lds\n"
      "buffer_load_dwordx4 v99, s[48:51], s66 offen offset:3072 lds\n"
      "s_add_u32 s74, s74, 8192\n"
      "s_and_b32 s74, s74, 0x7ffff\n"
      "s_mov_b32 m0, 90112\n"
      "s_add_u32 s66, s77, 40960\n"
      "s_and_b32 s66, s66, 0x3ffff\n"
      "buffer_load_dwordx4 v84, s[44:47], s66 offen nt lds\n"
      "buffer_load_dwordx4 v85, s[44:47], s66 offen offset:1024 nt lds\n"
      "buffer_load_dwordx4 v86, s[44:47], s66 offen offset:2048 nt lds\n"
      "buffer_load_dwordx4 v87, s[44:47], s66 offen offset:3072 nt lds\n"
      "s_mov_b32 m0, 94208\n"
      "s_nop 0\n"
      "buffer_load_dwordx4 v88, s[44:47], s66 offen nt lds\n"
      "buffer_load_dwordx4 v89, s[44:47], s66 offen offset:1024 nt lds\n"
      "buffer_load_dwordx4 v90, s[44:47], s66 offen offset:2048 nt lds\n"
      "buffer_load_dwordx4 v91, s[44:47], s66 offen offset:3072 nt lds\n"
      "s_waitcnt vmcnt(27)\n"
      "s_barrier\n"
      "s_mov_b32 m0, 114688\n"
      "s_add_u32 s66, s74, 0\n"
      "buffer_load_dwordx4 v92, s[48:51], s66 offen lds\n"
      "buffer_load_dwordx4 v93, s[48:51], s66 offen offset:1024 lds\n"
      "buffer_load_dwordx4 v94, s[48:51], s66 offen offset:2048 lds\n"
      "buffer_load_dwordx4 v95, s[48:51], s66 offen offset:3072 lds\n"
      "s_mov_b32 m0, 118784\n"
      "s_nop 0\n"
      "buffer_load_dwordx4 v96, s[48:51], s66 offen lds\n"
      "buffer_load_dwordx4 v97, s[48:51], s66 offen offset:1024 lds\n"
      "buffer_load_dwordx4 v98, s[48:51], s66 offen offset:2048 lds\n"
      "buffer_load_dwordx4 v99, s[48:51], s66 offen offset:3072 lds\n"
      "s_add_u32 s74, s74, 24576\n"
      "s_and_b32 s74, s74, 0x7ffff\n"
      "s_waitcnt vmcnt(30)\n"
      "s_barrier\n"
      "s_mov_b32 m0, 139264\n"
      "s_add_u32 s66, s74, 0\n"
      "buffer_load_dwordx4 v92, s[48:51], s66 offen lds\n"
      "buffer_load_dwordx4 v93, s[48:51], s66 offen offset:1024 lds\n"
      "buffer_load_dwordx4 v94, s[48:51], s66 offen offset:2048 lds\n"
      "buffer_load_dwordx4 v95, s[48:51], s66 offen offset:3072 lds\n"
      "s_mov_b32 m0, 143360\n"
      "s_nop 0\n"
      "buffer_load_dwordx4 v96, s[48:51], s66 offen lds\n"
      "buffer_load_dwordx4 v97, s[48:51], s66 offen offset:1024 lds\n"
      "buffer_load_dwordx4 v98, s[48:51], s66 offen offset:2048 lds\n"
      "buffer_load_dwordx4 v99, s[48:51], s66 offen offset:3072 lds\n"
      "s_add_u32 s74, s74, 8192\n"
      "s_and_b32 s74, s74, 0x7ffff\n"
      "s_waitcnt vmcnt(36)\n"
      "s_barrier\n"
      "s_mov_b32 m0, 122880\n"
      "s_add_u32 s66, s74, 0\n"
      "buffer_load_dwordx4 v92, s[48:51], s66 offen lds\n"
      "buffer_load_dwordx4 v93, s[48:51], s66 offen offset:1024 lds\n"
      "buffer_load_dwordx4 v94, s[48:51], s66 offen offset:2048 lds\n"
      "buffer_load_dwordx4 v95, s[48:51], s66 offen offset:3072 lds\n"
      "s_mov_b32 m0, 126976\n"
      "s_nop 0\n"
      "buffer_load_dwordx4 v96, s[48:51], s66 offen lds\n"
      "buffer_load_dwordx4 v97, s[48:51], s66 offen offset:1024 lds\n"
      "buffer_load_dwordx4 v98, s[48:51], s66 offen offset:2048 lds\n"
      "buffer_load_dwordx4 v99, s[48:51], s66 offen offset:3072 lds\n"
      "s_add_u32 s74, s74, 24576\n"
      "s_and_b32 s74, s74, 0x7ffff\n"
      "s_barrier\n"
      "s_mov_b32 m0, 0\n"
      "s_add_u32 s66, s77, 49152\n"
      "s_and_b32 s66, s66, 0x3ffff\n"
      "buffer_load_dwordx4 v84, s[40:43], s66 offen nt lds\n"
      "buffer_load_dwordx4 v85, s[40:43], s66 offen offset:1024 nt lds\n"
      "buffer_load_dwordx4 v86, s[40:43], s66 offen offset:2048 nt lds\n"
      "buffer_load_dwordx4 v87, s[40:43], s66 offen offset:3072 nt lds\n"
      "s_mov_b32 m0, 4096\n"
      "s_nop 0\n"
      "buffer_load_dwordx4 v88, s[40:43], s66 offen nt lds\n"
      "buffer_load_dwordx4 v89, s[40:43], s66 offen offset:1024 nt lds\n"
      "buffer_load_dwordx4 v90, s[40:43], s66 offen offset:2048 nt lds\n"
      "buffer_load_dwordx4 v91, s[40:43], s66 offen offset:3072 nt lds\n"
      "s_mov_b32 m0, 131072\n"
      "s_add_u32 s66, s74, 0\n"
      "buffer_load_dwordx4 v92, s[48:51], s66 offen lds\n"
      "buffer_load_dwordx4 v93, s[48:51], s66 offen offset:1024 lds\n"
      "buffer_load_dwordx4 v94, s[48:51], s66 offen offset:2048 lds\n"
      "buffer_load_dwordx4 v95, s[48:51], s66 offen offset:3072 lds\n"
      "s_mov_b32 m0, 135168\n"
      "s_nop 0\n"
      "buffer_load_dwordx4 v96, s[48:51], s66 offen lds\n"
      "buffer_load_dwordx4 v97, s[48:51], s66 offen offset:1024 lds\n"
      "buffer_load_dwordx4 v98, s[48:51], s66 offen offset:2048 lds\n"
      "buffer_load_dwordx4 v99, s[48:51], s66 offen offset:3072 lds\n"
      "s_add_u32 s74, s74, 8192\n"
      "s_and_b32 s74, s74, 0x7ffff\n"
      "v_readfirstlane_b32 s60, v220\n"
      "v_readfirstlane_b32 s61, v221\n"
      "s_nop 1\n"
      "s_add_u32 s60, s60, 0x7f4a7c15\n"
      "s_addc_u32 s61, s61, 0x9e3779b9\n"
      "s_mov_b32 s75, 0\n"
      "s_mov_b32 s68, 0\n"
      "s_waitcnt vmcnt(32)\n"
      "s_barrier\n"
      "ds_read_b64_tr_b16 a[0:1], v14 offset:8192\n"
      "ds_read_b64_tr_b16 a[4:5], v14 offset:8200\n"
      "ds_read_b64_tr_b16 a[8:9], v15 offset:8192\n"
      "ds_read_b64_tr_b16 a[12:13], v15 offset:8200\n"
      "ds_read_b64_tr_b16 a[2:3], v14 offset:16384\n"
      "ds_read_b64_tr_b16 a[6:7], v14 offset:16392\n"
      "ds_read_b64_tr_b16 a[10:11], v15 offset:16384\n"
      "ds_read_b64_tr_b16 a[14:15], v15 offset:16392\n"
      "s_mov_b32 m0, 147456\n"
      "s_add_u32 s66, s74, 0\n"
      "buffer_load_dwordx4 v92, s[48:51], s66 offen lds\n"
      "buffer_load_dwordx4 v93, s[48:51], s66 offen offset:1024 lds\n"
      "buffer_load_dwordx4 v94, s[48:51], s66 offen offset:2048 lds\n"
      "buffer_load_dwordx4 v95, s[48:51], s66 offen offset:3072 lds\n"
      "s_mov_b32 m0, 151552\n"
      "s_nop 0\n"
      "buffer_load_dwordx4 v96, s[48:51], s66 offen lds\n"
      "buffer_load_dwordx4 v97, s[48:51], s66 offen offset:1024 lds\n"
      "buffer_load_dwordx4 v98, s[48:51], s66 offen offset:2048 lds\n"
      "buffer_load_dwordx4 v99, s[48:51], s66 offen offset:3072 lds\n"
      "s_add_u32 s74, s74, 24576\n"
      "s_and_b32 s74, s74, 0x7ffff\n"
      "s_waitcnt lgkmcnt(0)\n"
      "s_waitcnt vmcnt(32)\n"
      "s_barrier\n"
      "ds_read_b64_tr_b16 a[16:17], v14 offset:40960\n"
      "ds_read_b64_tr_b16 a[20:21], v14 offset:40968\n"
      "ds_read_b64_tr_b16 a[24:25], v15 offset:40960\n"
      "ds_read_b64_tr_b16 a[28:29], v15 offset:40968\n"
      "s_mov_b32 m0, 8192\n"
      "s_add_u32 s66, s77, 57344\n"
      "s_and_b32 s66, s66, 0x3ffff\n"
      "buffer_load_dwordx4 v84, s[40:43], s66 offen nt lds\n"
      "buffer_load_dwordx4 v85, s[40:43], s66 offen offset:1024 nt lds\n"
      "buffer_load_dwordx4 v86, s[40:43], s66 offen offset:2048 nt lds\n"
      "buffer_load_dwordx4 v87, s[40:43], s66 offen offset:3072 nt lds\n"
      "s_mov_b32 m0, 12288\n"
      "s_nop 0\n"
      "buffer_load_dwordx4 v88, s[40:43], s66 offen nt lds\n"
      "buffer_load_dwordx4 v89, s[40:43], s66 offen offset:1024 nt lds\n"
      "buffer_load_dwordx4 v90, s[40:43], s66 offen offset:2048 nt lds\n"
      "buffer_load_dwordx4 v91, s[40:43], s66 offen offset:3072 nt lds\n"
      "s_mov_b32 m0, 155648\n"
      "s_add_u32 s66, s74, 0\n"
      "buffer_load_dwordx4 v92, s[48:51], s66 offen lds\n"
      "buffer_load_dwordx4 v93, s[48:51], s66 offen offset:1024 lds\n"
      "buffer_load_dwordx4 v94, s[48:51], s66 offen offset:2048 lds\n"
      "buffer_load_dwordx4 v95, s[48:51], s66 offen offset:3072 lds\n"
      "s_mov_b32 m0, 159744\n"
      "s_nop 0\n"
      "buffer_load_dwordx4 v96, s[48:51], s66 offen lds\n"
      "buffer_load_dwordx4 v97, s[48:51], s66 offen offset:1024 lds\n"
      "buffer_load_dwordx4 v98, s[48:51], s66 offen offset:2048 lds\n"
      "buffer_load_dwordx4 v99, s[48:51], s66 offen offset:3072 lds\n"
      "s_add_u32 s74, s74, 8192\n"
      "s_and_b32 s74, s74, 0x7ffff\n"
      "s_mov_b32 m0, 49152\n"
      "s_add_u32 s66, s77, 49152\n"
      "s_and_b32 s66, s66, 0x3ffff\n"
      "buffer_load_dwordx4 v84, s[44:47], s66 offen nt lds\n"
      "buffer_load_dwordx4 v85, s[44:47], s66 offen offset:1024 nt lds\n"
      "buffer_load_dwordx4 v86, s[44:47], s66 offen offset:2048 nt lds\n"
      "buffer_load_dwordx4 v87, s[44:47], s66 offen offset:3072 nt lds\n"
      "s_mov_b32 m0, 53248\n"
      "s_nop 0\n"
      "buffer_load_dwordx4 v88, s[44:47], s66 offen nt lds\n"
      "buffer_load_dwordx4 v89, s[44:47], s66 offen offset:1024 nt lds\n"
      "buffer_load_dwordx4 v90, s[44:47], s66 offen offset:2048 nt lds\n"
      "buffer_load_dwordx4 v91, s[44:47], s66 offen offset:3072 nt lds\n"
      "s_waitcnt lgkmcnt(0)\n"
      "s_waitcnt vmcnt(48)\n"
      "s_barrier\n"
      "ds_read_b64_tr_b16 a[18:19], v14 offset:24576\n"
      "ds_read_b64_tr_b16 a[22:23], v14 offset:24584\n"
      "ds_read_b64_tr_b16 a[26:27], v15 offset:24576\n"
      "ds_read_b64_tr_b16 a[30:31], v15 offset:24584\n"
      "s_mov_b32 m0, 16384\n"
      "s_add_u32 s66, s77, 65536\n"
      "s_and_b32 s66, s66, 0x3ffff\n"
      "buffer_load_dwordx4 v84, s[40:43], s66 offen nt lds\n"
      "buffer_load_dwordx4 v85, s[40:43], s66 offen offset:1024 nt lds\n"
      "buffer_load_dwordx4 v86, s[40:43], s66 offen offset:2048 nt lds\n"
      "buffer_load_dwordx4 v87, s[40:43], s66 offen offset:3072 nt lds\n"
      "s_mov_b32 m0, 20480\n"
      "s_nop 0\n"
      "buffer_load_dwordx4 v88, s[40:43], s66 offen nt lds\n"
      "buffer_load_dwordx4 v89, s[40:43], s66 offen offset:1024 nt lds\n"
      "buffer_load_dwordx4 v90, s[40:43], s66 offen offset:2048 nt lds\n"
      "buffer_load_dwordx4 v91, s[40:43], s66 offen offset:3072 nt lds\n"
      "s_mov_b32 m0, 98304\n"
      "s_add_u32 s66, s74, 0\n"
      "buffer_load_dwordx4 v92, s[48:51], s66 offen lds\n"
      "buffer_load_dwordx4 v93, s[48:51], s66 offen offset:1024 lds\n"
      "buffer_load_dwordx4 v94, s[48:51], s66 offen offset:2048 lds\n"
      "buffer_load_dwordx4 v95, s[48:51], s66 offen offset:3072 lds\n"
      "s_mov_b32 m0, 102400\n"
      "s_nop 0\n"
      "buffer_load_dwordx4 v96, s[48:51], s66 offen lds\n"
      "buffer_load_dwordx4 v97, s[48:51], s66 offen offset:1024 lds\n"
      "buffer_load_dwordx4 v98, s[48:51], s66 offen offset:2048 lds\n"
      "buffer_load_dwordx4 v99, s[48:51], s66 offen offset:3072 lds\n"
      "s_add_u32 s74, s74, 24576\n"
      "s_and_b32 s74, s74, 0x7ffff\n"
      "s_mov_b32 m0, 57344\n"
      "s_add_u32 s66, s77, 57344\n"
      "s_and_b32 s66, s66, 0x3ffff\n"
      "buffer_load_dwordx4 v84, s[44:47], s66 offen nt lds\n"
      "buffer_load_dwordx4 v85, s[44:47], s66 offen offset:1024 nt lds\n"
      "buffer_load_dwordx4 v86, s[44:47], s66 offen offset:2048 nt lds\n"
      "buffer_load_dwordx4 v87, s[44:47], s66 offen offset:3072 nt lds\n"
      "s_mov_b32 m0, 61440\n"
      "s_nop 0\n"
      "buffer_load_dwordx4 v88, s[44:47], s66 offen nt lds\n"
      "buffer_load_dwordx4 v89, s[44:47], s66 offen offset:1024 nt lds\n"
      "buffer_load_dwordx4 v90, s[44:47], s66 offen offset:2048 nt lds\n"
      "buffer_load_dwordx4 v91, s[44:47], s66 offen offset:3072 nt lds\n"
      "s_waitcnt lgkmcnt(0)\n"
      "s_waitcnt vmcnt(56)\n"
      "s_barrier\n"
      "ds_read_b64_tr_b16 a[32:33], v14 offset:32768\n"
      "ds_read_b64_tr_b16 a[36:37], v14 offset:32776\n"
      "ds_read_b64_tr_b16 a[40:41], v15 offset:32768\n"
      "ds_read_b64_tr_b16 a[44:45], v15 offset:32776\n"
      "s_mov_b32 m0, 24576\n"
      "s_add_u32 s66, s77, 73728\n"
      "s_and_b32 s66, s66, 0x3ffff\n"
      "buffer_load_dwordx4 v84, s[40:43], s66 offen nt lds\n"
      "buffer_load_dwordx4 v85, s[40:43], s66 offen offset:1024 nt lds\n"
      "buffer_load_dwordx4 v86, s[40:43], s66 offen offset:2048 nt lds\n"
      "buffer_load_dwordx4 v87, s[40:43], s66 offen offset:3072 nt lds\n"
      "s_mov_b32 m0, 28672\n"
      "s_nop 0\n"
      "buffer_load_dwordx4 v88, s[40:43], s66 offen nt lds\n"
      "buffer_load_dwordx4 v89, s[40:43], s66 offen offset:1024 nt lds\n"
      "buffer_load_dwordx4 v90, s[40:43], s66 offen offset:2048 nt lds\n"
      "buffer_load_dwordx4 v91, s[40:43], s66 offen offset:3072 nt lds\n"
      "s_mov_b32 m0, 106496\n"
      "s_add_u32 s66, s74, 0\n"
      "buffer_load_dwordx4 v92, s[48:51], s66 offen lds\n"
      "buffer_load_dwordx4 v93, s[48:51], s66 offen offset:1024 lds\n"
      "buffer_load_dwordx4 v94, s[48:51], s66 offen offset:2048 lds\n"
      "buffer_load_dwordx4 v95, s[48:51], s66 offen offset:3072 lds\n"
      "s_mov_b32 m0, 110592\n"
      "s_nop 0\n"
      "buffer_load_dwordx4 v96, s[48:51], s66 offen lds\n"
      "buffer_load_dwordx4 v97, s[48:51], s66 offen offset:1024 lds\n"
      "buffer_load_dwordx4 v98, s[48:51], s66 offen offset:2048 lds\n"
      "buffer_load_dwordx4 v99, s[48:51], s66 offen offset:3072 lds\n"
      "s_add_u32 s74, s74, 8192\n"
      "s_and_b32 s74, s74, 0x7ffff\n"
      "s_mov_b32 m0, 65536\n"
      "s_add_u32 s66, s77, 65536\n"
      "s_and_b32 s66, s66, 0x3ffff\n"
      "buffer_load_dwordx4 v84, s[44:47], s66 offen nt lds\n"
      "buffer_load_dwordx4 v85, s[44:47], s66 offen offset:1024 nt lds\n"
      "buffer_load_dwordx4 v86, s[44:47], s66 offen offset:2048 nt lds\n"
      "buffer_load_dwordx4 v87, s[44:47], s66 offen offset:3072 nt lds\n"
      "s_mov_b32 m0, 69632\n"
      "s_nop 0\n"
      "buffer_load_dwordx4 v88, s[44:47], s66 offen nt lds\n"
      "buffer_load_dwordx4 v89, s[44:47], s66 offen offset:1024 nt lds\n"
      "buffer_load_dwordx4 v90, s[44:47], s66 offen offset:2048 nt lds\n"
      "buffer_load_dwordx4 v91, s[44:47], s66 offen offset:3072 nt lds\n"
      "s_waitcnt lgkmcnt(0)\n"
      "s_waitcnt vmcnt(63)\n"
      "s_barrier\n"
      "ds_read_b64_tr_b16 a[34:35], v14 offset:49152\n"
      "ds_read_b64_tr_b16 a[38:39], v14 offset:49160\n"
      "ds_read_b64_tr_b16 a[42:43], v15 offset:49152\n"
      "ds_read_b64_tr_b16 a[46:47], v15 offset:49160\n"
      "s_mov_b32 m0, 32768\n"
      "s_add_u32 s66, s77, 81920\n"
      "s_and_b32 s66, s66, 0x3ffff\n"
      "buffer_load_dwordx4 v84, s[40:43], s66 offen nt lds\n"
      "buffer_load_dwordx4 v85, s[40:43], s66 offen offset:1024 nt lds\n"
      "buffer_load_dwordx4 v86, s[40:43], s66 offen offset:2048 nt lds\n"
      "buffer_load_dwordx4 v87, s[40:43], s66 offen offset:3072 nt lds\n"
      "s_mov_b32 m0, 36864\n"
      "s_nop 0\n"
      "buffer_load_dwordx4 v88, s[40:43], s66 offen nt lds\n"
      "buffer_load_dwordx4 v89, s[40:43], s66 offen offset:1024 nt lds\n"
      "buffer_load_dwordx4 v90, s[40:43], s66 offen offset:2048 nt lds\n"
      "buffer_load_dwordx4 v91, s[40:43], s66 offen offset:3072 nt lds\n"
      "s_mov_b32 m0, 114688\n"
      "s_add_u32 s66, s74, 0\n"
      "buffer_load_dwordx4 v92, s[48:51], s66 offen lds\n"
      "buffer_load_dwordx4 v93, s[48:51], s66 offen offset:1024 lds\n"
      "buffer_load_dwordx4 v94, s[48:51], s66 offen offset:2048 lds\n"
      "buffer_load_dwordx4 v95, s[48:51], s66 offen offset:3072 lds\n"
      "s_mov_b32 m0, 118784\n"
      "s_nop 0\n"
      "buffer_load_dwordx4 v96, s[48:51], s66 offen lds\n"
      "buffer_load_dwordx4 v97, s[48:51], s66 offen offset:1024 lds\n"
      "buffer_load_dwordx4 v98, s[48:51], s66 offen offset:2048 lds\n"
      "buffer_load_dwordx4 v99, s[48:51], s66 offen offset:3072 lds\n"
      "s_add_u32 s74, s74, 24576\n"
      "s_and_b32 s74, s74, 0x7ffff\n"
      "s_mov_b32 m0, 73728\n"
      "s_add_u32 s66, s77, 73728\n"
      "s_and_b32 s66, s66, 0x3ffff\n"
      "buffer_load_dwordx4 v84, s[44:47], s66 offen nt lds\n"
      "buffer_load_dwordx4 v85, s[44:47], s66 offen offset:1024 nt lds\n"
      "buffer_load_dwordx4 v86, s[44:47], s66 offen offset:2048 nt lds\n"
      "buffer_load_dwordx4 v87, s[44:47], s66 offen offset:3072 nt lds\n"
      "s_mov_b32 m0, 77824\n"
      "s_nop 0\n"
      "buffer_load_dwordx4 v88, s[44:47], s66 offen nt lds\n"
      "buffer_load_dwordx4 v89, s[44:47], s66 offen offset:1024 nt lds\n"
      "buffer_load_dwordx4 v90, s[44:47], s66 offen offset:2048 nt lds\n"
      "buffer_load_dwordx4 v91, s[44:47], s66 offen offset:3072 nt lds\n"
      "s_waitcnt lgkmcnt(0)\n"
      "s_waitcnt vmcnt(63)\n"
      "s_barrier\n"
      "ds_read_b64_tr_b16 a[48:49], v14 offset:57344\n"
      "ds_read_b64_tr_b16 a[52:53], v14 offset:57352\n"
      "ds_read_b64_tr_b16 a[56:57], v15 offset:57344\n"
      "ds_read_b64_tr_b16 a[60:61], v15 offset:57352\n"
      "s_mov_b32 m0, 40960\n"
      "s_add_u32 s66, s77, 90112\n"
      "s_and_b32 s66, s66, 0x3ffff\n"
      "buffer_load_dwordx4 v84, s[40:43], s66 offen nt lds\n"
      "buffer_load_dwordx4 v85, s[40:43], s66 offen offset:1024 nt lds\n"
      "buffer_load_dwordx4 v86, s[40:43], s66 offen offset:2048 nt lds\n"
      "buffer_load_dwordx4 v87, s[40:43], s66 offen offset:3072 nt lds\n"
      "s_mov_b32 m0, 45056\n"
      "s_nop 0\n"
      "buffer_load_dwordx4 v88, s[40:43], s66 offen nt lds\n"
      "buffer_load_dwordx4 v89, s[40:43], s66 offen offset:1024 nt lds\n"
      "buffer_load_dwordx4 v90, s[40:43], s66 offen offset:2048 nt lds\n"
      "buffer_load_dwordx4 v91, s[40:43], s66 offen offset:3072 nt lds\n"
      "s_mov_b32 m0, 139264\n"
      "s_add_u32 s66, s74, 0\n"
      "buffer_load_dwordx4 v92, s[48:51], s66 offen lds\n"
      "buffer_load_dwordx4 v93, s[48:51], s66 offen offset:1024 lds\n"
      "buffer_load_dwordx4 v94, s[48:51], s66 offen offset:2048 lds\n"
      "buffer_load_dwordx4 v95, s[48:51], s66 offen offset:3072 lds\n"
      "s_mov_b32 m0, 143360\n"
      "s_nop 0\n"
      "buffer_load_dwordx4 v96, s[48:51], s66 offen lds\n"
      "buffer_load_dwordx4 v97, s[48:51], s66 offen offset:1024 lds\n"
      "buffer_load_dwordx4 v98, s[48:51], s66 offen offset:2048 lds\n"
      "buffer_load_dwordx4 v99, s[48:51], s66 offen offset:3072 lds\n"
      "s_add_u32 s74, s74, 8192\n"
      "s_and_b32 s74, s74, 0x7ffff\n"
      "s_mov_b32 m0, 81920\n"
      "s_add_u32 s66, s77, 81920\n"
      "s_and_b32 s66, s66, 0x3ffff\n"
      "buffer_load_dwordx4 v84, s[44:47], s66 offen nt lds\n"
      "buffer_load_dwordx4 v85, s[44:47], s66 offen offset:1024 nt lds\n"
      "buffer_load_dwordx4 v86, s[44:47], s66 offen offset:2048 nt lds\n"
      "buffer_load_dwordx4 v87, s[44:47], s66 offen offset:3072 nt lds\n"
      "s_mov_b32 m0, 86016\n"
      "s_nop 0\n"
      "buffer_load_dwordx4 v88, s[44:47], s66 offen nt lds\n"
      "buffer_load_dwordx4 v89, s[44:47], s66 offen offset:1024 nt lds\n"
      "buffer_load_dwordx4 v90, s[44:47], s66 offen offset:2048 nt lds\n"
      "buffer_load_dwordx4 v91, s[44:47], s66 offen offset:3072 nt lds\n"
      "s_waitcnt lgkmcnt(0)\n"
      "s_waitcnt vmcnt(63)\n"
      "s_barrier\n"
      "ds_read_b64_tr_b16 a[50:51], v14 offset:0\n"
      "ds_read_b64_tr_b16 a[54:55], v14 offset:8\n"
      "ds_read_b64_tr_b16 a[58:59], v15 offset:0\n"
      "ds_read_b64_tr_b16 a[62:63], v15 offset:8\n"
      "s_mov_b32 m0, 0\n"
      "s_add_u32 s66, s77, 98304\n"
      "s_and_b32 s66, s66, 0x3ffff\n"
      "buffer_load_dwordx4 v84, s[40:43], s66 offen nt lds\n"
      "buffer_load_dwordx4 v85, s[40:43], s66 offen offset:1024 nt lds\n"
      "buffer_load_dwordx4 v86, s[40:43], s66 offen offset:2048 nt lds\n"
      "buffer_load_dwordx4 v87, s[40:43], s66 offen offset:3072 nt lds\n"
      "s_mov_b32 m0, 4096\n"
      "s_nop 0\n"
      "buffer_load_dwordx4 v88, s[40:43], s66 offen nt lds\n"
      "buffer_load_dwordx4 v89, s[40:43], s66 offen offset:1024 nt lds\n"
      "buffer_load_dwordx4 v90, s[40:43], s66 offen offset:2048 nt lds\n"
      "buffer_load_dwordx4 v91, s[40:43], s66 offen offset:3072 nt lds\n"
      "s_mov_b32 m0, 122880\n"
      "s_add_u32 s66, s74, 0\n"
      "buffer_load_dwordx4 v92, s[48:51], s66 offen lds\n"
      "buffer_load_dwordx4 v93, s[48:51], s66 offen offset:1024 lds\n"
      "buffer_load_dwordx4 v94, s[48:51], s66 offen offset:2048 lds\n"
      "buffer_load_dwordx4 v95, s[48:51], s66 offen offset:3072 lds\n"
      "s_mov_b32 m0, 126976\n"
      "s_nop 0\n"
      "buffer_load_dwordx4 v96, s[48:51], s66 offen lds\n"
      "buffer_load_dwordx4 v97, s[48:51], s66 offen offset:1024 lds\n"
      "buffer_load_dwordx4 v98, s[48:51], s66 offen offset:2048 lds\n"
      "buffer_load_dwordx4 v99, s[48:51], s66 offen offset:3072 lds\n"
      "s_add_u32 s74, s74, 24576\n"
      "s_and_b32 s74, s74, 0x7ffff\n"
      "s_mov_b32 m0, 90112\n"
      "s_add_u32 s66, s77, 90112\n"
      "s_and_b32 s66, s66, 0x3ffff\n"
      "buffer_load_dwordx4 v84, s[44:47], s66 offen nt lds\n"
      "buffer_load_dwordx4 v85, s[44:47], s66 offen offset:1024 nt lds\n"
      "buffer_load_dwordx4 v86, s[44:47], s66 offen offset:2048 nt lds\n"
      "buffer_load_dwordx4 v87, s[44:47], s66 offen offset:3072 nt lds\n"
      "s_mov_b32 m0, 94208\n"
      "s_nop 0\n"
      "buffer_load_dwordx4 v88, s[44:47], s66 offen nt lds\n"
      "buffer_load_dwordx4 v89, s[44:47], s66 offen offset:1024 nt lds\n"
      "buffer_load_dwordx4 v90, s[44:47], s66 offen offset:2048 nt lds\n"
      "buffer_load_dwordx4 v91, s[44:47], s66 offen offset:3072 nt lds\n"
      "s_waitcnt lgkmcnt(0)\n"
      "s_waitcnt vmcnt(63)\n"
      "s_barrier\n"
      "ds_read_b64_tr_b16 a[64:65], v14 offset:8192\n"
      "ds_read_b64_tr_b16 a[68:69], v14 offset:8200\n"
      "ds_read_b64_tr_b16 a[72:73], v15 offset:8192\n"
      "ds_read_b64_tr_b16 a[76:77], v15 offset:8200\n"
      "s_mov_b32 m0, 8192\n"
      "s_add_u32 s66, s77, 106496\n"
      "s_and_b32 s66, s66, 0x3ffff\n"
      "buffer_load_dwordx4 v84, s[40:43], s66 offen nt lds\n"
      "buffer_load_dwordx4 v85, s[40:43], s66 offen offset:1024 nt lds\n"
      "buffer_load_dwordx4 v86, s[40:43], s66 offen offset:2048 nt lds\n"
      "buffer_load_dwordx4 v87, s[40:43], s66 offen offset:3072 nt lds\n"
      "s_mov_b32 m0, 12288\n"
      "s_nop 0\n"
      "buffer_load_dwordx4 v88, s[40:43], s66 offen nt lds\n"
      "buffer_load_dwordx4 v89, s[40:43], s66 offen offset:1024 nt lds\n"
      "buffer_load_dwordx4 v90, s[40:43], s66 offen offset:2048 nt lds\n"
      "buffer_load_dwordx4 v91, s[40:43], s66 offen offset:3072 nt lds\n"
      "s_mov_b32 m0, 131072\n"
      "s_add_u32 s66, s74, 0\n"
      "buffer_load_dwordx4 v92, s[48:51], s66 offen lds\n"
      "buffer_load_dwordx4 v93, s[48:51], s66 offen offset:1024 lds\n"
      "buffer_load_dwordx4 v94, s[48:51], s66 offen offset:2048 lds\n"
      "buffer_load_dwordx4 v95, s[48:51], s66 offen offset:3072 lds\n"
      "s_mov_b32 m0, 135168\n"
      "s_nop 0\n"
      "buffer_load_dwordx4 v96, s[48:51], s66 offen lds\n"
      "buffer_load_dwordx4 v97, s[48:51], s66 offen offset:1024 lds\n"
      "buffer_load_dwordx4 v98, s[48:51], s66 offen offset:2048 lds\n"
      "buffer_load_dwordx4 v99, s[48:51], s66 offen offset:3072 lds\n"
      "s_add_u32 s74, s74, 8192\n"
      "s_and_b32 s74, s74, 0x7ffff\n"
      "s_mov_b32 m0, 49152\n"
      "s_add_u32 s66, s77, 98304\n"
      "s_and_b32 s66, s66, 0x3ffff\n"
      "buffer_load_dwordx4 v84, s[44:47], s66 offen nt lds\n"
      "buffer_load_dwordx4 v85, s[44:47], s66 offen offset:1024 nt lds\n"
      "buffer_load_dwordx4 v86, s[44:47], s66 offen offset:2048 nt lds\n"
      "buffer_load_dwordx4 v87, s[44:47], s66 offen offset:3072 nt lds\n"
      "s_mov_b32 m0, 53248\n"
      "s_nop 0\n"
      "buffer_load_dwordx4 v88, s[44:47], s66 offen nt lds\n"
      "buffer_load_dwordx4 v89, s[44:47], s66 offen offset:1024 nt lds\n"
      "buffer_load_dwordx4 v90, s[44:47], s66 offen offset:2048 nt lds\n"
      "buffer_load_dwordx4 v91, s[44:47], s66 offen offset:3072 nt lds\n"
      "s_waitcnt lgkmcnt(0)\n"
      "s_waitcnt vmcnt(63)\n"
      "s_barrier\n"
      "ds_read_b64_tr_b16 a[66:67], v14 offset:16384\n"
      "ds_read_b64_tr_b16 a[70:71], v14 offset:16392\n"
      "ds_read_b64_tr_b16 a[74:75], v15 offset:16384\n"
      "ds_read_b64_tr_b16 a[78:79], v15 offset:16392\n"
      "s_mov_b32 m0, 16384\n"
      "s_add_u32 s66, s77, 114688\n"
      "s_and_b32 s66, s66, 0x3ffff\n"
      "buffer_load_dwordx4 v84, s[40:43], s66 offen nt lds\n"
      "buffer_load_dwordx4 v85, s[40:43], s66 offen offset:1024 nt lds\n"
      "buffer_load_dwordx4 v86, s[40:43], s66 offen offset:2048 nt lds\n"
      "buffer_load_dwordx4 v87, s[40:43], s66 offen offset:3072 nt lds\n"
      "s_mov_b32 m0, 20480\n"
      "s_nop 0\n"
      "buffer_load_dwordx4 v88, s[40:43], s66 offen nt lds\n"
      "buffer_load_dwordx4 v89, s[40:43], s66 offen offset:1024 nt lds\n"
      "buffer_load_dwordx4 v90, s[40:43], s66 offen offset:2048 nt lds\n"
      "buffer_load_dwordx4 v91, s[40:43], s66 offen offset:3072 nt lds\n"
      "s_mov_b32 m0, 147456\n"
      "s_add_u32 s66, s74, 0\n"
      "buffer_load_dwordx4 v92, s[48:51], s66 offen lds\n"
      "buffer_load_dwordx4 v93, s[48:51], s66 offen offset:1024 lds\n"
      "buffer_load_dwordx4 v94, s[48:51], s66 offen offset:2048 lds\n"
      "buffer_load_dwordx4 v95, s[48:51], s66 offen offset:3072 lds\n"
      "s_mov_b32 m0, 151552\n"
      "s_nop 0\n"
      "buffer_load_dwordx4 v96, s[48:51], s66 offen lds\n"
      "buffer_load_dwordx4 v97, s[48:51], s66 offen offset:1024 lds\n"
      "buffer_load_dwordx4 v98, s[48:51], s66 offen offset:2048 lds\n"
      "buffer_load_dwordx4 v99, s[48:51], s66 offen offset:3072 lds\n"
      "s_add_u32 s74, s74, 24576\n"
      "s_and_b32 s74, s74, 0x7ffff\n"
      "s_mov_b32 m0, 57344\n"
      "s_add_u32 s66, s77, 106496\n"
      "s_and_b32 s66, s66, 0x3ffff\n"
      "buffer_load_dwordx4 v84, s[44:47], s66 offen nt lds\n"
      "buffer_load_dwordx4 v85, s[44:47], s66 offen offset:1024 nt lds\n"
      "buffer_load_dwordx4 v86, s[44:47], s66 offen offset:2048 nt lds\n"
      "buffer_load_dwordx4 v87, s[44:47], s66 offen offset:3072 nt lds\n"
      "s_mov_b32 m0, 61440\n"
      "s_nop 0\n"
      "buffer_load_dwordx4 v88, s[44:47], s66 offen nt lds\n"
      "buffer_load_dwordx4 v89, s[44:47], s66 offen offset:1024 nt lds\n"
      "buffer_load_dwordx4 v90, s[44:47], s66 offen offset:2048 nt lds\n"
      "buffer_load_dwordx4 v91, s[44:47], s66 offen offset:3072 nt lds\n"
      "s_waitcnt lgkmcnt(0)\n"
      "s_waitcnt vmcnt(63)\n"
      "s_barrier\n"
      "ds_read_b64_tr_b16 a[80:81], v14 offset:40960\n"
      "ds_read_b64_tr_b16 a[84:85], v14 offset:40968\n"
      "ds_read_b64_tr_b16 a[88:89], v15 offset:40960\n"
      "ds_read_b64_tr_b16 a[92:93], v15 offset:40968\n"
      "s_mov_b32 m0, 24576\n"
      "s_add_u32 s66, s77, 122880\n"
      "s_and_b32 s66, s66, 0x3ffff\n"
      "buffer_load_dwordx4 v84, s[40:43], s66 offen nt lds\n"
      "buffer_load_dwordx4 v85, s[40:43], s66 offen offset:1024 nt lds\n"
      "buffer_load_dwordx4 v86, s[40:43], s66 offen offset:2048 nt lds\n"
      "buffer_load_dwordx4 v87, s[40:43], s66 offen offset:3072 nt lds\n"
      "s_mov_b32 m0, 28672\n"
      "s_nop 0\n"
      "buffer_load_dwordx4 v88, s[40:43], s66 offen nt lds\n"
      "buffer_load_dwordx4 v89, s[40:43], s66 offen offset:1024 nt lds\n"
      "buffer_load_dwordx4 v90, s[40:43], s66 offen offset:2048 nt lds\n"
      "buffer_load_dwordx4 v91, s[40:43], s66 offen offset:3072 nt lds\n"
      "s_mov_b32 m0, 155648\n"
      "s_add_u32 s66, s74, 0\n"
      "buffer_load_dwordx4 v92, s[48:51], s66 offen lds\n"
      "buffer_load_dwordx4 v93, s[48:51], s66 offen offset:1024 lds\n"
      "buffer_load_dwordx4 v94, s[48:51], s66 offen offset:2048 lds\n"
      "buffer_load_dwordx4 v95, s[48:51], s66 offen offset:3072 lds\n"
      "s_mov_b32 m0, 159744\n"
      "s_nop 0\n"
      "buffer_load_dwordx4 v96, s[48:51], s66 offen lds\n"
      "buffer_load_dwordx4 v97, s[48:51], s66 offen offset:1024 lds\n"
      "buffer_load_dwordx4 v98, s[48:51], s66 offen offset:2048 lds\n"
      "buffer_load_dwordx4 v99, s[48:51], s66 offen offset:3072 lds\n"
      "s_add_u32 s74, s74, 8192\n"
      "s_and_b32 s74, s74, 0x7ffff\n"
      "s_mov_b32 m0, 65536\n"
      "s_add_u32 s66, s77, 114688\n"
      "s_and_b32 s66, s66, 0x3ffff\n"
      "buffer_load_dwordx4 v84, s[44:47], s66 offen nt lds\n"
      "buffer_load_dwordx4 v85, s[44:47], s66 offen offset:1024 nt lds\n"
      "buffer_load_dwordx4 v86, s[44:47], s66 offen offset:2048 nt lds\n"
      "buffer_load_dwordx4 v87, s[44:47], s66 offen offset:3072 nt lds\n"
      "s_mov_b32 m0, 69632\n"
      "s_nop 0\n"
      "buffer_load_dwordx4 v88, s[44:47], s66 offen nt lds\n"
      "buffer_load_dwordx4 v89, s[44:47], s66 offen offset:1024 nt lds\n"
      "buffer_load_dwordx4 v90, s[44:47], s66 offen offset:2048 nt lds\n"
      "buffer_load_dwordx4 v91, s[44:47], s66 offen offset:3072 nt lds\n"
      "s_waitcnt lgkmcnt(0)\n"
      "s_waitcnt vmcnt(63)\n"
      "s_barrier\n"
      "ds_read_b64_tr_b16 a[82:83], v14 offset:24576\n"
      "ds_read_b64_tr_b16 a[86:87], v14 offset:24584\n"
      "ds_read_b64_tr_b16 a[90:91], v15 offset:24576\n"
      "ds_read_b64_tr_b16 a[94:95], v15 offset:24584\n"
      "s_mov_b32 m0, 32768\n"
      "s_add_u32 s66, s77, 131072\n"
      "s_and_b32 s66, s66, 0x3ffff\n"
      "buffer_load_dwordx4 v84, s[40:43], s66 offen nt lds\n"
      "buffer_load_dwordx4 v85, s[40:43], s66 offen offset:1024 nt lds\n"
      "buffer_load_dwordx4 v86, s[40:43], s66 offen offset:2048 nt lds\n"
      "buffer_load_dwordx4 v87, s[40:43], s66 offen offset:3072 nt lds\n"
      "s_mov_b32 m0, 36864\n"
      "s_nop 0\n"
      "buffer_load_dwordx4 v88, s[40:43], s66 offen nt lds\n"
      "buffer_load_dwordx4 v89, s[40:43], s66 offen offset:1024 nt lds\n"
      "buffer_load_dwordx4 v90, s[40:43], s66 offen offset:2048 nt lds\n"
      "buffer_load_dwordx4 v91, s[40:43], s66 offen offset:3072 nt lds\n"
      "s_mov_b32 m0, 98304\n"
      "s_add_u32 s66, s74, 0\n"
      "buffer_load_dwordx4 v92, s[48:51], s66 offen lds\n"
      "buffer_load_dwordx4 v93, s[48:51], s66 offen offset:1024 lds\n"
      "buffer_load_dwordx4 v94, s[48:51], s66 offen offset:2048 lds\n"
      "buffer_load_dwordx4 v95, s[48:51], s66 offen offset:3072 lds\n"
      "s_mov_b32 m0, 102400\n"
      "s_nop 0\n"
      "buffer_load_dwordx4 v96, s[48:51], s66 offen lds\n"
      "buffer_load_dwordx4 v97, s[48:51], s66 offen offset:1024 lds\n"
      "buffer_load_dwordx4 v98, s[48:51], s66 offen offset:2048 lds\n"
      "buffer_load_dwordx4 v99, s[48:51], s66 offen offset:3072 lds\n"
      "s_add_u32 s74, s74, 24576\n"
      "s_and_b32 s74, s74, 0x7ffff\n"
      "s_mov_b32 m0, 73728\n"
      "s_add_u32 s66, s77, 122880\n"
      "s_and_b32 s66, s66, 0x3ffff\n"
      "buffer_load_dwordx4 v84, s[44:47], s66 offen nt lds\n"
      "buffer_load_dwordx4 v85, s[44:47], s66 offen offset:1024 nt lds\n"
      "buffer_load_dwordx4 v86, s[44:47], s66 offen offset:2048 nt lds\n"
      "buffer_load_dwordx4 v87, s[44:47], s66 offen offset:3072 nt lds\n"
      "s_mov_b32 m0, 77824\n"
      "s_nop 0\n"
      "buffer_load_dwordx4 v88, s[44:47], s66 offen nt lds\n"
      "buffer_load_dwordx4 v89, s[44:47], s66 offen offset:1024 nt lds\n"
      "buffer_load_dwordx4 v90, s[44:47], s66 offen offset:2048 nt lds\n"
      "buffer_load_dwordx4 v91, s[44:47], s66 offen offset:3072 nt lds\n"
      "s_waitcnt lgkmcnt(0)\n"
      "s_waitcnt vmcnt(63)\n"
      "s_barrier\n"
      "ds_read_b64_tr_b16 a[96:97], v14 offset:32768\n"
      "ds_read_b64_tr_b16 a[100:101], v14 offset:32776\n"
      "ds_read_b64_tr_b16 a[104:105], v15 offset:32768\n"
      "ds_read_b64_tr_b16 a[108:109], v15 offset:32776\n"
      "s_mov_b32 m0, 40960\n"
      "s_add_u32 s66, s77, 139264\n"
      "s_and_b32 s66, s66, 0x3ffff\n"
      "buffer_load_dwordx4 v84, s[40:43], s66 offen nt lds\n"
      "buffer_load_dwordx4 v85, s[40:43], s66 offen offset:1024 nt lds\n"
      "buffer_load_dwordx4 v86, s[40:43], s66 offen offset:2048 nt lds\n"
      "buffer_load_dwordx4 v87, s[40:43], s66 offen offset:3072 nt lds\n"
      "s_mov_b32 m0, 45056\n"
      "s_nop 0\n"
      "buffer_load_dwordx4 v88, s[40:43], s66 offen nt lds\n"
      "buffer_load_dwordx4 v89, s[40:43], s66 offen offset:1024 nt lds\n"
      "buffer_load_dwordx4 v90, s[40:43], s66 offen offset:2048 nt lds\n"
      "buffer_load_dwordx4 v91, s[40:43], s66 offen offset:3072 nt lds\n"
      "s_mov_b32 m0, 106496\n"
      "s_add_u32 s66, s74, 0\n"
      "buffer_load_dwordx4 v92, s[48:51], s66 offen lds\n"
      "buffer_load_dwordx4 v93, s[48:51], s66 offen offset:1024 lds\n"
      "buffer_load_dwordx4 v94, s[48:51], s66 offen offset:2048 lds\n"
      "buffer_load_dwordx4 v95, s[48:51], s66 offen offset:3072 lds\n"
      "s_mov_b32 m0, 110592\n"
      "s_nop 0\n"
      "buffer_load_dwordx4 v96, s[48:51], s66 offen lds\n"
      "buffer_load_dwordx4 v97, s[48:51], s66 offen offset:1024 lds\n"
      "buffer_load_dwordx4 v98, s[48:51], s66 offen offset:2048 lds\n"
      "buffer_load_dwordx4 v99, s[48:51], s66 offen offset:3072 lds\n"
      "s_add_u32 s74, s74, 8192\n"
      "s_and_b32 s74, s74, 0x7ffff\n"
      "s_mov_b32 m0, 81920\n"
      "s_add_u32 s66, s77, 131072\n"
      "s_and_b32 s66, s66, 0x3ffff\n"
      "buffer_load_dwordx4 v84, s[44:47], s66 offen nt lds\n"
      "buffer_load_dwordx4 v85, s[44:47], s66 offen offset:1024 nt lds\n"
      "buffer_load_dwordx4 v86, s[44:47], s66 offen offset:2048 nt lds\n"
      "buffer_load_dwordx4 v87, s[44:47], s66 offen offset:3072 nt lds\n"
      "s_mov_b32 m0, 86016\n"
      "s_nop 0\n"
      "buffer_load_dwordx4 v88, s[44:47], s66 offen nt lds\n"
      "buffer_load_dwordx4 v89, s[44:47], s66 offen offset:1024 nt lds\n"
      "buffer_load_dwordx4 v90, s[44:47], s66 offen offset:2048 nt lds\n"
      "buffer_load_dwordx4 v91, s[44:47], s66 offen offset:3072 nt lds\n"
      "s_waitcnt lgkmcnt(0)\n"
      "s_waitcnt vmcnt(63)\n"
      "s_barrier\n"
      "ds_read_b64_tr_b16 a[98:99], v14 offset:49152\n"
      "ds_read_b64_tr_b16 a[102:103], v14 offset:49160\n"
      "ds_read_b64_tr_b16 a[106:107], v15 offset:49152\n"
      "ds_read_b64_tr_b16 a[110:111], v15 offset:49160\n"
      "s_mov_b32 m0, 0\n"
      "s_add_u32 s66, s77, 147456\n"
      "s_and_b32 s66, s66, 0x3ffff\n"
      "buffer_load_dwordx4 v84, s[40:43], s66 offen nt lds\n"
      "buffer_load_dwordx4 v85, s[40:43], s66 offen offset:1024 nt lds\n"
      "buffer_load_dwordx4 v86, s[40:43], s66 offen offset:2048 nt lds\n"
      "buffer_load_dwordx4 v87, s[40:43], s66 offen offset:3072 nt lds\n"
      "s_mov_b32 m0, 4096\n"
      "s_nop 0\n"
      "buffer_load_dwordx4 v88, s[40:43], s66 offen nt lds\n"
      "buffer_load_dwordx4 v89, s[40:43], s66 offen offset:1024 nt lds\n"
      "buffer_load_dwordx4 v90, s[40:43], s66 offen offset:2048 nt lds\n"
      "buffer_load_dwordx4 v91, s[40:43], s66 offen offset:3072 nt lds\n"
      "s_mov_b32 m0, 114688\n"
      "s_add_u32 s66, s74, 0\n"
      "buffer_load_dwordx4 v92, s[48:51], s66 offen lds\n"
      "buffer_load_dwordx4 v93, s[48:51], s66 offen offset:1024 lds\n"
      "buffer_load_dwordx4 v94, s[48:51], s66 offen offset:2048 lds\n"
      "buffer_load_dwordx4 v95, s[48:51], s66 offen offset:3072 lds\n"
      "s_mov_b32 m0, 118784\n"
      "s_nop 0\n"
      "buffer_load_dwordx4 v96, s[48:51], s66 offen lds\n"
      "buffer_load_dwordx4 v97, s[48:51], s66 offen offset:1024 lds\n"
      "buffer_load_dwordx4 v98, s[48:51], s66 offen offset:2048 lds\n"
      "buffer_load_dwordx4 v99, s[48:51], s66 offen offset:3072 lds\n"
      "s_add_u32 s74, s74, 24576\n"
      "s_and_b32 s74, s74, 0x7ffff\n"
      "s_mov_b32 m0, 90112\n"
      "s_add_u32 s66, s77, 139264\n"
      "s_and_b32 s66, s66, 0x3ffff\n"
      "buffer_load_dwordx4 v84, s[44:47], s66 offen nt lds\n"
      "buffer_load_dwordx4 v85, s[44:47], s66 offen offset:1024 nt lds\n"
      "buffer_load_dwordx4 v86, s[44:47], s66 offen offset:2048 nt lds\n"
      "buffer_load_dwordx4 v87, s[44:47], s66 offen offset:3072 nt lds\n"
      "s_mov_b32 m0, 94208\n"
      "s_nop 0\n"
      "buffer_load_dwordx4 v88, s[44:47], s66 offen nt lds\n"
      "buffer_load_dwordx4 v89, s[44:47], s66 offen offset:1024 nt lds\n"
      "buffer_load_dwordx4 v90, s[44:47], s66 offen offset:2048 nt lds\n"
      "buffer_load_dwordx4 v91, s[44:47], s66 offen offset:3072 nt lds\n"
      "s_waitcnt lgkmcnt(0)\n"
      "s_waitcnt vmcnt(63)\n"
      "s_barrier\n"
      "ds_read_b64_tr_b16 a[112:113], v14 offset:57344\n"
      "ds_read_b64_tr_b16 a[116:117], v14 offset:57352\n"
      "ds_read_b64_tr_b16 a[120:121], v15 offset:57344\n"
      "ds_read_b64_tr_b16 a[124:125], v15 offset:57352\n"
      "s_mov_b32 m0, 139264\n"
      "s_add_u32 s66, s74, 0\n"
      "buffer_load_dwordx4 v92, s[48:51], s66 offen lds\n"
      "buffer_load_dwordx4 v93, s[48:51], s66 offen offset:1024 lds\n"
      "buffer_load_dwordx4 v94, s[48:51], s66 offen offset:2048 lds\n"
      "buffer_load_dwordx4 v95, s[48:51], s66 offen offset:3072 lds\n"
      "s_mov_b32 m0, 143360\n"
      "s_nop 0\n"
      "buffer_load_dwordx4 v96, s[48:51], s66 offen lds\n"
      "buffer_load_dwordx4 v97, s[48:51], s66 offen offset:1024 lds\n"
      "buffer_load_dwordx4 v98, s[48:51], s66 offen offset:2048 lds\n"
      "buffer_load_dwordx4 v99, s[48:51], s66 offen offset:3072 lds\n"
      "s_add_u32 s74, s74, 8192\n"
      "s_and_b32 s74, s74, 0x7ffff\n"
      "s_waitcnt lgkmcnt(0)\n"
      "s_waitcnt vmcnt(63)\n"
      "s_barrier\n"
      "ds_read_b64_tr_b16 a[114:115], v14 offset:0\n"
      "ds_read_b64_tr_b16 a[118:119], v14 offset:8\n"
      "ds_read_b64_tr_b16 a[122:123], v15 offset:0\n"
      "ds_read_b64_tr_b16 a[126:127], v15 offset:8\n"
      "s_mov_b32 m0, 8192\n"
      "s_add_u32 s66, s77, 155648\n"
      "s_and_b32 s66, s66, 0x3ffff\n"
      "buffer_load_dwordx4 v84, s[40:43], s66 offen nt lds\n"
      "buffer_load_dwordx4 v85, s[40:43], s66 offen offset:1024 nt lds\n"
      "buffer_load_dwordx4 v86, s[40:43], s66 offen offset:2048 nt lds\n"
      "buffer_load_dwordx4 v87, s[40:43], s66 offen offset:3072 nt lds\n"
      "s_mov_b32 m0, 12288\n"
      "s_nop 0\n"
      "buffer_load_dwordx4 v88, s[40:43], s66 offen nt lds\n"
      "buffer_load_dwordx4 v89, s[40:43], s66 offen offset:1024 nt lds\n"
      "buffer_load_dwordx4 v90, s[40:43], s66 offen offset:2048 nt lds\n"
      "buffer_load_dwordx4 v91, s[40:43], s66 offen offset:3072 nt lds\n"
      "s_mov_b32 m0, 122880\n"
      "s_add_u32 s66, s74, 0\n"
      "buffer_load_dwordx4 v92, s[48:51], s66 offen lds\n"
      "buffer_load_dwordx4 v93, s[48:51], s66 offen offset:1024 lds\n"
      "buffer_load_dwordx4 v94, s[48:51], s66 offen offset:2048 lds\n"
      "buffer_load_dwordx4 v95, s[48:51], s66 offen offset:3072 lds\n"
      "s_mov_b32 m0, 126976\n"
      "s_nop 0\n"
      "buffer_load_dwordx4 v96, s[48:51], s66 offen lds\n"
      "buffer_load_dwordx4 v97, s[48:51], s66 offen offset:1024 lds\n"
      "buffer_load_dwordx4 v98, s[48:51], s66 offen offset:2048 lds\n"
      "buffer_load_dwordx4 v99, s[48:51], s66 offen offset:3072 lds\n"
      "s_add_u32 s74, s74, 24576\n"
      "s_and_b32 s74, s74, 0x7ffff\n"
      "s_mov_b32 m0, 49152\n"
      "s_add_u32 s66, s77, 147456\n"
      "s_and_b32 s66, s66, 0x3ffff\n"
      "buffer_load_dwordx4 v84, s[44:47], s66 offen nt lds\n"
      "buffer_load_dwordx4 v85, s[44:47], s66 offen offset:1024 nt lds\n"
      "buffer_load_dwordx4 v86, s[44:47], s66 offen offset:2048 nt lds\n"
      "buffer_load_dwordx4 v87, s[44:47], s66 offen offset:3072 nt lds\n"
      "s_mov_b32 m0, 53248\n"
      "s_nop 0\n"
      "buffer_load_dwordx4 v88, s[44:47], s66 offen nt lds\n"
      "buffer_load_dwordx4 v89, s[44:47], s66 offen offset:1024 nt lds\n"
      "buffer_load_dwordx4 v90, s[44:47], s66 offen offset:2048 nt lds\n"
      "buffer_load_dwordx4 v91, s[44:47], s66 offen offset:3072 nt lds\n"
      "s_waitcnt lgkmcnt(0)\n"
      "s_waitcnt vmcnt(63)\n"
      "s_barrier\n"
      "ds_read_b64_tr_b16 a[128:129], v14 offset:8192\n"
      "ds_read_b64_tr_b16 a[132:133], v14 offset:8200\n"
      "ds_read_b64_tr_b16 a[136:137], v15 offset:8192\n"
      "ds_read_b64_tr_b16 a[140:141], v15 offset:8200\n"
      "s_mov_b32 m0, 16384\n"
      "s_add_u32 s66, s77, 163840\n"
      "s_and_b32 s66, s66, 0x3ffff\n"
      "buffer_load_dwordx4 v84, s[40:43], s66 offen nt lds\n"
      "buffer_load_dwordx4 v85, s[40:43], s66 offen offset:1024 nt lds\n"
      "buffer_load_dwordx4 v86, s[40:43], s66 offen offset:2048 nt lds\n"
      "buffer_load_dwordx4 v87, s[40:43], s66 offen offset:3072 nt lds\n"
      "s_mov_b32 m0, 20480\n"
      "s_nop 0\n"
      "buffer_load_dwordx4 v88, s[40:43], s66 offen nt lds\n"
      "buffer_load_dwordx4 v89, s[40:43], s66 offen offset:1024 nt lds\n"
      "buffer_load_dwordx4 v90, s[40:43], s66 offen offset:2048 nt lds\n"
      "buffer_load_dwordx4 v91, s[40:43], s66 offen offset:3072 nt lds\n"
      "s_mov_b32 m0, 131072\n"
      "s_add_u32 s66, s74, 0\n"
      "buffer_load_dwordx4 v92, s[48:51], s66 offen lds\n"
      "buffer_load_dwordx4 v93, s[48:51], s66 offen offset:1024 lds\n"
      "buffer_load_dwordx4 v94, s[48:51], s66 offen offset:2048 lds\n"
      "buffer_load_dwordx4 v95, s[48:51], s66 offen offset:3072 lds\n"
      "s_mov_b32 m0, 135168\n"
      "s_nop 0\n"
      "buffer_load_dwordx4 v96, s[48:51], s66 offen lds\n"
      "buffer_load_dwordx4 v97, s[48:51], s66 offen offset:1024 lds\n"
      "buffer_load_dwordx4 v98, s[48:51], s66 offen offset:2048 lds\n"
      "buffer_load_dwordx4 v99, s[48:51], s66 offen offset:3072 lds\n"
      "s_add_u32 s74, s74, 8192\n"
      "s_and_b32 s74, s74, 0x7ffff\n"
      "s_mov_b32 m0, 57344\n"
      "s_add_u32 s66, s77, 155648\n"
      "s_and_b32 s66, s66, 0x3ffff\n"
      "buffer_load_dwordx4 v84, s[44:47], s66 offen nt lds\n"
      "buffer_load_dwordx4 v85, s[44:47], s66 offen offset:1024 nt lds\n"
      "buffer_load_dwordx4 v86, s[44:47], s66 offen offset:2048 nt lds\n"
      "buffer_load_dwordx4 v87, s[44:47], s66 offen offset:3072 nt lds\n"
      "s_mov_b32 m0, 61440\n"
      "s_nop 0\n"
      "buffer_load_dwordx4 v88, s[44:47], s66 offen nt lds\n"
      "buffer_load_dwordx4 v89, s[44:47], s66 offen offset:1024 nt lds\n"
      "buffer_load_dwordx4 v90, s[44:47], s66 offen offset:2048 nt lds\n"
      "buffer_load_dwordx4 v91, s[44:47], s66 offen offset:3072 nt lds\n"
      "s_waitcnt lgkmcnt(0)\n"
      "s_waitcnt vmcnt(63)\n"
      "s_barrier\n"
      "ds_read_b64_tr_b16 a[130:131], v14 offset:16384\n"
      "ds_read_b64_tr_b16 a[134:135], v14 offset:16392\n"
      "ds_read_b64_tr_b16 a[138:139], v15 offset:16384\n"
      "ds_read_b64_tr_b16 a[142:143], v15 offset:16392\n"
      "s_mov_b32 m0, 24576\n"
      "s_add_u32 s66, s77, 172032\n"
      "s_and_b32 s66, s66, 0x3ffff\n"
      "buffer_load_dwordx4 v84, s[40:43], s66 offen nt lds\n"
      "buffer_load_dwordx4 v85, s[40:43], s66 offen offset:1024 nt lds\n"
      "buffer_load_dwordx4 v86, s[40:43], s66 offen offset:2048 nt lds\n"
      "buffer_load_dwordx4 v87, s[40:43], s66 offen offset:3072 nt lds\n"
      "s_mov_b32 m0, 28672\n"
      "s_nop 0\n"
      "buffer_load_dwordx4 v88, s[40:43], s66 offen nt lds\n"
      "buffer_load_dwordx4 v89, s[40:43], s66 offen offset:1024 nt lds\n"
      "buffer_load_dwordx4 v90, s[40:43], s66 offen offset:2048 nt lds\n"
      "buffer_load_dwordx4 v91, s[40:43], s66 offen offset:3072 nt lds\n"
      "s_mov_b32 m0, 147456\n"
      "s_add_u32 s66, s74, 0\n"
      "buffer_load_dwordx4 v92, s[48:51], s66 offen lds\n"
      "buffer_load_dwordx4 v93, s[48:51], s66 offen offset:1024 lds\n"
      "buffer_load_dwordx4 v94, s[48:51], s66 offen offset:2048 lds\n"
      "buffer_load_dwordx4 v95, s[48:51], s66 offen offset:3072 lds\n"
      "s_mov_b32 m0, 151552\n"
      "s_nop 0\n"
      "buffer_load_dwordx4 v96, s[48:51], s66 offen lds\n"
      "buffer_load_dwordx4 v97, s[48:51], s66 offen offset:1024 lds\n"
      "buffer_load_dwordx4 v98, s[48:51], s66 offen offset:2048 lds\n"
      "buffer_load_dwordx4 v99, s[48:51], s66 offen offset:3072 lds\n"
      "s_add_u32 s74, s74, 24576\n"
      "s_and_b32 s74, s74, 0x7ffff\n"
      "s_mov_b32 m0, 65536\n"
      "s_add_u32 s66, s77, 163840\n"
      "s_and_b32 s66, s66, 0x3ffff\n"
      "buffer_load_dwordx4 v84, s[44:47], s66 offen nt lds\n"
      "buffer_load_dwordx4 v85, s[44:47], s66 offen offset:1024 nt lds\n"
      "buffer_load_dwordx4 v86, s[44:47], s66 offen offset:2048 nt lds\n"
      "buffer_load_dwordx4 v87, s[44:47], s66 offen offset:3072 nt lds\n"
      "s_mov_b32 m0, 69632\n"
      "s_nop 0\n"
      "buffer_load_dwordx4 v88, s[44:47], s66 offen nt lds\n"
      "buffer_load_dwordx4 v89, s[44:47], s66 offen offset:1024 nt lds\n"
      "buffer_load_dwordx4 v90, s[44:47], s66 offen offset:2048 nt lds\n"
      "buffer_load_dwordx4 v91, s[44:47], s66 offen offset:3072 nt lds\n"
      "s_waitcnt lgkmcnt(0)\n"
      "s_waitcnt vmcnt(63)\n"
      "s_barrier\n"
      "ds_read_b64_tr_b16 a[144:145], v14 offset:40960\n"
      "ds_read_b64_tr_b16 a[148:149], v14 offset:40968\n"
      "ds_read_b64_tr_b16 a[152:153], v15 offset:40960\n"
      "ds_read_b64_tr_b16 a[156:157], v15 offset:40968\n"
      "s_mov_b32 m0, 32768\n"
      "s_add_u32 s66, s77, 180224\n"
      "s_and_b32 s66, s66, 0x3ffff\n"
      "buffer_load_dwordx4 v84, s[40:43], s66 offen nt lds\n"
      "buffer_load_dwordx4 v85, s[40:43], s66 offen offset:1024 nt lds\n"
      "buffer_load_dwordx4 v86, s[40:43], s66 offen offset:2048 nt lds\n"
      "buffer_load_dwordx4 v87, s[40:43], s66 offen offset:3072 nt lds\n"
      "s_mov_b32 m0, 36864\n"
      "s_nop 0\n"
      "buffer_load_dwordx4 v88, s[40:43], s66 offen nt lds\n"
      "buffer_load_dwordx4 v89, s[40:43], s66 offen offset:1024 nt lds\n"
      "buffer_load_dwordx4 v90, s[40:43], s66 offen offset:2048 nt lds\n"
      "buffer_load_dwordx4 v91, s[40:43], s66 offen offset:3072 nt lds\n"
      "s_mov_b32 m0, 155648\n"
      "s_add_u32 s66, s74, 0\n"
      "buffer_load_dwordx4 v92, s[48:51], s66 offen lds\n"
      "buffer_load_dwordx4 v93, s[48:51], s66 offen offset:1024 lds\n"
      "buffer_load_dwordx4 v94, s[48:51], s66 offen offset:2048 lds\n"
      "buffer_load_dwordx4 v95, s[48:51], s66 offen offset:3072 lds\n"
      "s_mov_b32 m0, 159744\n"
      "s_nop 0\n"
      "buffer_load_dwordx4 v96, s[48:51], s66 offen lds\n"
      "buffer_load_dwordx4 v97, s[48:51], s66 offen offset:1024 lds\n"
      "buffer_load_dwordx4 v98, s[48:51], s66 offen offset:2048 lds\n"
      "buffer_load_dwordx4 v99, s[48:51], s66 offen offset:3072 lds\n"
      "s_add_u32 s74, s74, 8192\n"
      "s_and_b32 s74, s74, 0x7ffff\n"
      "s_mov_b32 m0, 73728\n"
      "s_add_u32 s66, s77, 172032\n"
      "s_and_b32 s66, s66, 0x3ffff\n"
      "buffer_load_dwordx4 v84, s[44:47], s66 offen nt lds\n"
      "buffer_load_dwordx4 v85, s[44:47], s66 offen offset:1024 nt lds\n"
      "buffer_load_dwordx4 v86, s[44:47], s66 offen offset:2048 nt lds\n"
      "buffer_load_dwordx4 v87, s[44:47], s66 offen offset:3072 nt lds\n"
      "s_mov_b32 m0, 77824\n"
      "s_nop 0\n"
      "buffer_load_dwordx4 v88, s[44:47], s66 offen nt lds\n"
      "buffer_load_dwordx4 v89, s[44:47], s66 offen offset:1024 nt lds\n"
      "buffer_load_dwordx4 v90, s[44:47], s66 offen offset:2048 nt lds\n"
      "buffer_load_dwordx4 v91, s[44:47], s66 offen offset:3072 nt lds\n"
      "s_waitcnt lgkmcnt(0)\n"
      "s_waitcnt vmcnt(63)\n"
      "s_barrier\n"
      "ds_read_b64_tr_b16 a[146:147], v14 offset:24576\n"
      "ds_read_b64_tr_b16 a[150:151], v14 offset:24584\n"
      "ds_read_b64_tr_b16 a[154:155], v15 offset:24576\n"
      "ds_read_b64_tr_b16 a[158:159], v15 offset:24584\n"
      "s_mov_b32 m0, 40960\n"
      "s_add_u32 s66, s77, 188416\n"
      "s_and_b32 s66, s66, 0x3ffff\n"
      "buffer_load_dwordx4 v84, s[40:43], s66 offen nt lds\n"
      "buffer_load_dwordx4 v85, s[40:43], s66 offen offset:1024 nt lds\n"
      "buffer_load_dwordx4 v86, s[40:43], s66 offen offset:2048 nt lds\n"
      "buffer_load_dwordx4 v87, s[40:43], s66 offen offset:3072 nt lds\n"
      "s_mov_b32 m0, 45056\n"
      "s_nop 0\n"
      "buffer_load_dwordx4 v88, s[40:43], s66 offen nt lds\n"
      "buffer_load_dwordx4 v89, s[40:43], s66 offen offset:1024 nt lds\n"
      "buffer_load_dwordx4 v90, s[40:43], s66 offen offset:2048 nt lds\n"
      "buffer_load_dwordx4 v91, s[40:43], s66 offen offset:3072 nt lds\n"
      "s_mov_b32 m0, 98304\n"
      "s_add_u32 s66, s74, 0\n"
      "buffer_load_dwordx4 v92, s[48:51], s66 offen lds\n"
      "buffer_load_dwordx4 v93, s[48:51], s66 offen offset:1024 lds\n"
      "buffer_load_dwordx4 v94, s[48:51], s66 offen offset:2048 lds\n"
      "buffer_load_dwordx4 v95, s[48:51], s66 offen offset:3072 lds\n"
      "s_mov_b32 m0, 102400\n"
      "s_nop 0\n"
      "buffer_load_dwordx4 v96, s[48:51], s66 offen lds\n"
      "buffer_load_dwordx4 v97, s[48:51], s66 offen offset:1024 lds\n"
      "buffer_load_dwordx4 v98, s[48:51], s66 offen offset:2048 lds\n"
      "buffer_load_dwordx4 v99, s[48:51], s66 offen offset:3072 lds\n"
      "s_add_u32 s74, s74, 24576\n"
      "s_and_b32 s74, s74, 0x7ffff\n"
      "s_mov_b32 m0, 81920\n"
      "s_add_u32 s66, s77, 180224\n"
      "s_and_b32 s66, s66, 0x3ffff\n"
      "buffer_load_dwordx4 v84, s[44:47], s66 offen nt lds\n"
      "buffer_load_dwordx4 v85, s[44:47], s66 offen offset:1024 nt lds\n"
      "buffer_load_dwordx4 v86, s[44:47], s66 offen offset:2048 nt lds\n"
      "buffer_load_dwordx4 v87, s[44:47], s66 offen offset:3072 nt lds\n"
      "s_mov_b32 m0, 86016\n"
      "s_nop 0\n"
      "buffer_load_dwordx4 v88, s[44:47], s66 offen nt lds\n"
      "buffer_load_dwordx4 v89, s[44:47], s66 offen offset:1024 nt lds\n"
      "buffer_load_dwordx4 v90, s[44:47], s66 offen offset:2048 nt lds\n"
      "buffer_load_dwordx4 v91, s[44:47], s66 offen offset:3072 nt lds\n"
      "s_waitcnt lgkmcnt(0)\n"
      "s_waitcnt vmcnt(63)\n"
      "s_barrier\n"
      "ds_read_b64_tr_b16 a[160:161], v14 offset:32768\n"
      "ds_read_b64_tr_b16 a[164:165], v14 offset:32776\n"
      "ds_read_b64_tr_b16 a[168:169], v15 offset:32768\n"
      "ds_read_b64_tr_b16 a[172:173], v15 offset:32776\n"
      "s_mov_b32 m0, 106496\n"
      "s_add_u32 s66, s74, 0\n"
      "buffer_load_dwordx4 v92, s[48:51], s66 offen lds\n"
      "buffer_load_dwordx4 v93, s[48:51], s66 offen offset:1024 lds\n"
      "buffer_load_dwordx4 v94, s[48:51], s66 offen offset:2048 lds\n"
      "buffer_load_dwordx4 v95, s[48:51], s66 offen offset:3072 lds\n"
      "s_mov_b32 m0, 110592\n"
      "s_nop 0\n"
      "buffer_load_dwordx4 v96, s[48:51], s66 offen lds\n"
      "buffer_load_dwordx4 v97, s[48:51], s66 offen offset:1024 lds\n"
      "buffer_load_dwordx4 v98, s[48:51], s66 offen offset:2048 lds\n"
      "buffer_load_dwordx4 v99, s[48:51], s66 offen offset:3072 lds\n"
      "s_add_u32 s74, s74, 8192\n"
      "s_and_b32 s74, s74, 0x7ffff\n"
      "s_waitcnt lgkmcnt(0)\n"
      "s_waitcnt vmcnt(56)\n"
      "s_barrier\n"
      "ds_read_b64_tr_b16 a[162:163], v14 offset:49152\n"
      "ds_read_b64_tr_b16 a[166:167], v14 offset:49160\n"
      "ds_read_b64_tr_b16 a[170:171], v15 offset:49152\n"
      "ds_read_b64_tr_b16 a[174:175], v15 offset:49160\n"
      "s_mov_b32 m0, 0\n"
      "s_add_u32 s66, s77, 196608\n"
      "s_and_b32 s66, s66, 0x3ffff\n"
      "buffer_load_dwordx4 v84, s[40:43], s66 offen nt lds\n"
      "buffer_load_dwordx4 v85, s[40:43], s66 offen offset:1024 nt lds\n"
      "buffer_load_dwordx4 v86, s[40:43], s66 offen offset:2048 nt lds\n"
      "buffer_load_dwordx4 v87, s[40:43], s66 offen offset:3072 nt lds\n"
      "s_mov_b32 m0, 4096\n"
      "s_nop 0\n"
      "buffer_load_dwordx4 v88, s[40:43], s66 offen nt lds\n"
      "buffer_load_dwordx4 v89, s[40:43], s66 offen offset:1024 nt lds\n"
      "buffer_load_dwordx4 v90, s[40:43], s66 offen offset:2048 nt lds\n"
      "buffer_load_dwordx4 v91, s[40:43], s66 offen offset:3072 nt lds\n"
      "s_mov_b32 m0, 114688\n"
      "s_add_u32 s66, s74, 0\n"
      "buffer_load_dwordx4 v92, s[48:51], s66 offen lds\n"
      "buffer_load_dwordx4 v93, s[48:51], s66 offen offset:1024 lds\n"
      "buffer_load_dwordx4 v94, s[48:51], s66 offen offset:2048 lds\n"
      "buffer_load_dwordx4 v95, s[48:51], s66 offen offset:3072 lds\n"
      "s_mov_b32 m0, 118784\n"
      "s_nop 0\n"
      "buffer_load_dwordx4 v96, s[48:51], s66 offen lds\n"
      "buffer_load_dwordx4 v97, s[48:51], s66 offen offset:1024 lds\n"
      "buffer_load_dwordx4 v98, s[48:51], s66 offen offset:2048 lds\n"
      "buffer_load_dwordx4 v99, s[48:51], s66 offen offset:3072 lds\n"
      "s_add_u32 s74, s74, 24576\n"
      "s_and_b32 s74, s74, 0x7ffff\n"
      "s_mov_b32 m0, 90112\n"
      "s_add_u32 s66, s77, 188416\n"
      "s_and_b32 s66, s66, 0x3ffff\n"
      "buffer_load_dwordx4 v84, s[44:47], s66 offen nt lds\n"
      "buffer_load_dwordx4 v85, s[44:47], s66 offen offset:1024 nt lds\n"
      "buffer_load_dwordx4 v86, s[44:47], s66 offen offset:2048 nt lds\n"
      "buffer_load_dwordx4 v87, s[44:47], s66 offen offset:3072 nt lds\n"
      "s_mov_b32 m0, 94208\n"
      "s_nop 0\n"
      "buffer_load_dwordx4 v88, s[44:47], s66 offen nt lds\n"
      "buffer_load_dwordx4 v89, s[44:47], s66 offen offset:1024 nt lds\n"
      "buffer_load_dwordx4 v90, s[44:47], s66 offen offset:2048 nt lds\n"
      "buffer_load_dwordx4 v91, s[44:47], s66 offen offset:3072 nt lds\n"
      "s_waitcnt lgkmcnt(0)\n"
      "s_waitcnt vmcnt(56)\n"
      "s_barrier\n"
      "ds_read_b64_tr_b16 a[176:177], v14 offset:57344\n"
      "ds_read_b64_tr_b16 a[180:181], v14 offset:57352\n"
      "ds_read_b64_tr_b16 a[184:185], v15 offset:57344\n"
      "ds_read_b64_tr_b16 a[188:189], v15 offset:57352\n"
      "s_mov_b32 m0, 8192\n"
      "s_add_u32 s66, s77, 204800\n"
      "s_and_b32 s66, s66, 0x3ffff\n"
      "buffer_load_dwordx4 v84, s[40:43], s66 offen nt lds\n"
      "buffer_load_dwordx4 v85, s[40:43], s66 offen offset:1024 nt lds\n"
      "buffer_load_dwordx4 v86, s[40:43], s66 offen offset:2048 nt lds\n"
      "buffer_load_dwordx4 v87, s[40:43], s66 offen offset:3072 nt lds\n"
      "s_mov_b32 m0, 12288\n"
      "s_nop 0\n"
      "buffer_load_dwordx4 v88, s[40:43], s66 offen nt lds\n"
      "buffer_load_dwordx4 v89, s[40:43], s66 offen offset:1024 nt lds\n"
      "buffer_load_dwordx4 v90, s[40:43], s66 offen offset:2048 nt lds\n"
      "buffer_load_dwordx4 v91, s[40:43], s66 offen offset:3072 nt lds\n"
      "s_mov_b32 m0, 139264\n"
      "s_add_u32 s66, s74, 0\n"
      "buffer_load_dwordx4 v92, s[48:51], s66 offen lds\n"
      "buffer_load_dwordx4 v93, s[48:51], s66 offen offset:1024 lds\n"
      "buffer_load_dwordx4 v94, s[48:51], s66 offen offset:2048 lds\n"
      "buffer_load_dwordx4 v95, s[48:51], s66 offen offset:3072 lds\n"
      "s_mov_b32 m0, 143360\n"
      "s_nop 0\n"
      "buffer_load_dwordx4 v96, s[48:51], s66 offen lds\n"
      "buffer_load_dwordx4 v97, s[48:51], s66 offen offset:1024 lds\n"
      "buffer_load_dwordx4 v98, s[48:51], s66 offen offset:2048 lds\n"
      "buffer_load_dwordx4 v99, s[48:51], s66 offen offset:3072 lds\n"
      "s_add_u32 s74, s74, 8192\n"
      "s_and_b32 s74, s74, 0x7ffff\n"
      "s_mov_b32 m0, 49152\n"
      "s_add_u32 s66, s77, 196608\n"
      "s_and_b32 s66, s66, 0x3ffff\n"
      "buffer_load_dwordx4 v84, s[44:47], s66 offen nt lds\n"
      "buffer_load_dwordx4 v85, s[44:47], s66 offen offset:1024 nt lds\n"
      "buffer_load_dwordx4 v86, s[44:47], s66 offen offset:2048 nt lds\n"
      "buffer_load_dwordx4 v87, s[44:47], s66 offen offset:3072 nt lds\n"
      "s_mov_b32 m0, 53248\n"
      "s_nop 0\n"
      "buffer_load_dwordx4 v88, s[44:47], s66 offen nt lds\n"
      "buffer_load_dwordx4 v89, s[44:47], s66 offen offset:1024 nt lds\n"
      "buffer_load_dwordx4 v90, s[44:47], s66 offen offset:2048 nt lds\n"
      "buffer_load_dwordx4 v91, s[44:47], s66 offen offset:3072 nt lds\n"
      "s_waitcnt lgkmcnt(0)\n"
      "s_waitcnt vmcnt(56)\n"
      "s_barrier\n"
      "ds_read_b64_tr_b16 a[178:179], v14 offset:0\n"
      "ds_read_b64_tr_b16 a[182:183], v14 offset:8\n"
      "ds_read_b64_tr_b16 a[186:187], v15 offset:0\n"
      "ds_read_b64_tr_b16 a[190:191], v15 offset:8\n"
      "s_mov_b32 m0, 16384\n"
      "s_add_u32 s66, s77, 212992\n"
      "s_and_b32 s66, s66, 0x3ffff\n"
      "buffer_load_dwordx4 v84, s[40:43], s66 offen nt lds\n"
      "buffer_load_dwordx4 v85, s[40:43], s66 offen offset:1024 nt lds\n"
      "buffer_load_dwordx4 v86, s[40:43], s66 offen offset:2048 nt lds\n"
      "buffer_load_dwordx4 v87, s[40:43], s66 offen offset:3072 nt lds\n"
      "s_mov_b32 m0, 20480\n"
      "s_nop 0\n"
      "buffer_load_dwordx4 v88, s[40:43], s66 offen nt lds\n"
      "buffer_load_dwordx4 v89, s[40:43], s66 offen offset:1024 nt lds\n"
      "buffer_load_dwordx4 v90, s[40:43], s66 offen offset:2048 nt lds\n"
      "buffer_load_dwordx4 v91, s[40:43], s66 offen offset:3072 nt lds\n"
      "s_mov_b32 m0, 122880\n"
      "s_add_u32 s66, s74, 0\n"
      "buffer_load_dwordx4 v92, s[48:51], s66 offen lds\n"
      "buffer_load_dwordx4 v93, s[48:51], s66 offen offset:1024 lds\n"
      "buffer_load_dwordx4 v94, s[48:51], s66 offen offset:2048 lds\n"
      "buffer_load_dwordx4 v95, s[48:51], s66 offen offset:3072 lds\n"
      "s_mov_b32 m0, 126976\n"
      "s_nop 0\n"
      "buffer_load_dwordx4 v96, s[48:51], s66 offen lds\n"
      "buffer_load_dwordx4 v97, s[48:51], s66 offen offset:1024 lds\n"
      "buffer_load_dwordx4 v98, s[48:51], s66 offen offset:2048 lds\n"
      "buffer_load_dwordx4 v99, s[48:51], s66 offen offset:3072 lds\n"
      "s_add_u32 s74, s74, 24576\n"
      "s_and_b32 s74, s74, 0x7ffff\n"
      "s_mov_b32 m0, 57344\n"
      "s_add_u32 s66, s77, 204800\n"
      "s_and_b32 s66, s66, 0x3ffff\n"
      "buffer_load_dwordx4 v84, s[44:47], s66 offen nt lds\n"
      "buffer_load_dwordx4 v85, s[44:47], s66 offen offset:1024 nt lds\n"
      "buffer_load_dwordx4 v86, s[44:47], s66 offen offset:2048 nt lds\n"
      "buffer_load_dwordx4 v87, s[44:47], s66 offen offset:3072 nt lds\n"
      "s_mov_b32 m0, 61440\n"
      "s_nop 0\n"
      "buffer_load_dwordx4 v88, s[44:47], s66 offen nt lds\n"
      "buffer_load_dwordx4 v89, s[44:47], s66 offen offset:1024 nt lds\n"
      "buffer_load_dwordx4 v90, s[44:47], s66 offen offset:2048 nt lds\n"
      "buffer_load_dwordx4 v91, s[44:47], s66 offen offset:3072 nt lds\n"
      "s_waitcnt lgkmcnt(0)\n"
      "s_waitcnt vmcnt(48)\n"
      "s_barrier\n"
      "ds_read_b64_tr_b16 a[192:193], v14 offset:8192\n"
      "ds_read_b64_tr_b16 a[196:197], v14 offset:8200\n"
      "ds_read_b64_tr_b16 a[200:201], v15 offset:8192\n"
      "ds_read_b64_tr_b16 a[204:205], v15 offset:8200\n"
      "s_mov_b32 m0, 24576\n"
      "s_add_u32 s66, s77, 221184\n"
      "s_and_b32 s66, s66, 0x3ffff\n"
      "buffer_load_dwordx4 v84, s[40:43], s66 offen nt lds\n"
      "buffer_load_dwordx4 v85, s[40:43], s66 offen offset:1024 nt lds\n"
      "buffer_load_dwordx4 v86, s[40:43], s66 offen offset:2048 nt lds\n"
      "buffer_load_dwordx4 v87, s[40:43], s66 offen offset:3072 nt lds\n"
      "s_mov_b32 m0, 28672\n"
      "s_nop 0\n"
      "buffer_load_dwordx4 v88, s[40:43], s66 offen nt lds\n"
      "buffer_load_dwordx4 v89, s[40:43], s66 offen offset:1024 nt lds\n"
      "buffer_load_dwordx4 v90, s[40:43], s66 offen offset:2048 nt lds\n"
      "buffer_load_dwordx4 v91, s[40:43], s66 offen offset:3072 nt lds\n"
      "s_mov_b32 m0, 131072\n"
      "s_add_u32 s66, s74, 0\n"
      "buffer_load_dwordx4 v92, s[48:51], s66 offen lds\n"
      "buffer_load_dwordx4 v93, s[48:51], s66 offen offset:1024 lds\n"
      "buffer_load_dwordx4 v94, s[48:51], s66 offen offset:2048 lds\n"
      "buffer_load_dwordx4 v95, s[48:51], s66 offen offset:3072 lds\n"
      "s_mov_b32 m0, 135168\n"
      "s_nop 0\n"
      "buffer_load_dwordx4 v96, s[48:51], s66 offen lds\n"
      "buffer_load_dwordx4 v97, s[48:51], s66 offen offset:1024 lds\n"
      "buffer_load_dwordx4 v98, s[48:51], s66 offen offset:2048 lds\n"
      "buffer_load_dwordx4 v99, s[48:51], s66 offen offset:3072 lds\n"
      "s_add_u32 s74, s74, 8192\n"
      "s_and_b32 s74, s74, 0x7ffff\n"
      "s_mov_b32 m0, 65536\n"
      "s_add_u32 s66, s77, 212992\n"
      "s_and_b32 s66, s66, 0x3ffff\n"
      "buffer_load_dwordx4 v84, s[44:47], s66 offen nt lds\n"
      "buffer_load_dwordx4 v85, s[44:47], s66 offen offset:1024 nt lds\n"
      "buffer_load_dwordx4 v86, s[44:47], s66 offen offset:2048 nt lds\n"
      "buffer_load_dwordx4 v87, s[44:47], s66 offen offset:3072 nt lds\n"
      "s_mov_b32 m0, 69632\n"
      "s_nop 0\n"
      "buffer_load_dwordx4 v88, s[44:47], s66 offen nt lds\n"
      "buffer_load_dwordx4 v89, s[44:47], s66 offen offset:1024 nt lds\n"
      "buffer_load_dwordx4 v90, s[44:47], s66 offen offset:2048 nt lds\n"
      "buffer_load_dwordx4 v91, s[44:47], s66 offen offset:3072 nt lds\n"
      "s_waitcnt lgkmcnt(0)\n"
      "s_waitcnt vmcnt(48)\n"
      "s_barrier\n"
      "ds_read_b64_tr_b16 a[194:195], v14 offset:16384\n"
      "ds_read_b64_tr_b16 a[198:199], v14 offset:16392\n"
      "ds_read_b64_tr_b16 a[202:203], v15 offset:16384\n"
      "ds_read_b64_tr_b16 a[206:207], v15 offset:16392\n"
      "s_mov_b32 m0, 32768\n"
      "s_add_u32 s66, s77, 229376\n"
      "s_and_b32 s66, s66, 0x3ffff\n"
      "buffer_load_dwordx4 v84, s[40:43], s66 offen nt lds\n"
      "buffer_load_dwordx4 v85, s[40:43], s66 offen offset:1024 nt lds\n"
      "buffer_load_dwordx4 v86, s[40:43], s66 offen offset:2048 nt lds\n"
      "buffer_load_dwordx4 v87, s[40:43], s66 offen offset:3072 nt lds\n"
      "s_mov_b32 m0, 36864\n"
      "s_nop 0\n"
      "buffer_load_dwordx4 v88, s[40:43], s66 offen nt lds\n"
      "buffer_load_dwordx4 v89, s[40:43], s66 offen offset:1024 nt lds\n"
      "buffer_load_dwordx4 v90, s[40:43], s66 offen offset:2048 nt lds\n"
      "buffer_load_dwordx4 v91, s[40:43], s66 offen offset:3072 nt lds\n"
      "s_mov_b32 m0, 147456\n"
      "s_add_u32 s66, s74, 0\n"
      "buffer_load_dwordx4 v92, s[48:51], s66 offen lds\n"
      "buffer_load_dwordx4 v93, s[48:51], s66 offen offset:1024 lds\n"
      "buffer_load_dwordx4 v94, s[48:51], s66 offen offset:2048 lds\n"
      "buffer_load_dwordx4 v95, s[48:51], s66 offen offset:3072 lds\n"
      "s_mov_b32 m0, 151552\n"
      "s_nop 0\n"
      "buffer_load_dwordx4 v96, s[48:51], s66 offen lds\n"
      "buffer_load_dwordx4 v97, s[48:51], s66 offen offset:1024 lds\n"
      "buffer_load_dwordx4 v98, s[48:51], s66 offen offset:2048 lds\n"
      "buffer_load_dwordx4 v99, s[48:51], s66 offen offset:3072 lds\n"
      "s_add_u32 s74, s74, 24576\n"
      "s_and_b32 s74, s74, 0x7ffff\n"
      "s_mov_b32 m0, 73728\n"
      "s_add_u32 s66, s77, 221184\n"
      "s_and_b32 s66, s66, 0x3ffff\n"
      "buffer_load_dwordx4 v84, s[44:47], s66 offen nt lds\n"
      "buffer_load_dwordx4 v85, s[44:47], s66 offen offset:1024 nt lds\n"
      "buffer_load_dwordx4 v86, s[44:47], s66 offen offset:2048 nt lds\n"
      "buffer_load_dwordx4 v87, s[44:47], s66 offen offset:3072 nt lds\n"
      "s_mov_b32 m0, 77824\n"
      "s_nop 0\n"
      "buffer_load_dwordx4 v88, s[44:47], s66 offen nt lds\n"
      "buffer_load_dwordx4 v89, s[44:47], s66 offen offset:1024 nt lds\n"
      "buffer_load_dwordx4 v90, s[44:47], s66 offen offset:2048 nt lds\n"
      "buffer_load_dwordx4 v91, s[44:47], s66 offen offset:3072 nt lds\n"
      "s_waitcnt lgkmcnt(0)\n"
      "s_waitcnt vmcnt(48)\n"
      "s_barrier\n"
      "ds_read_b64_tr_b16 a[208:209], v14 offset:40960\n"
      "ds_read_b64_tr_b16 a[212:213], v14 offset:40968\n"
      "ds_read_b64_tr_b16 a[216:217], v15 offset:40960\n"
      "ds_read_b64_tr_b16 a[220:221], v15 offset:40968\n"
      "s_mov_b32 m0, 40960\n"
      "s_add_u32 s66, s77, 237568\n"
      "s_and_b32 s66, s66, 0x3ffff\n"
      "buffer_load_dwordx4 v84, s[40:43], s66 offen nt lds\n"
      "buffer_load_dwordx4 v85, s[40:43], s66 offen offset:1024 nt lds\n"
      "buffer_load_dwordx4 v86, s[40:43], s66 offen offset:2048 nt lds\n"
      "buffer_load_dwordx4 v87, s[40:43], s66 offen offset:3072 nt lds\n"
      "s_mov_b32 m0, 45056\n"
      "s_nop 0\n"
      "buffer_load_dwordx4 v88, s[40:43], s66 offen nt lds\n"
      "buffer_load_dwordx4 v89, s[40:43], s66 offen offset:1024 nt lds\n"
      "buffer_load_dwordx4 v90, s[40:43], s66 offen offset:2048 nt lds\n"
      "buffer_load_dwordx4 v91, s[40:43], s66 offen offset:3072 nt lds\n"
      "s_mov_b32 m0, 155648\n"
      "s_add_u32 s66, s74, 0\n"
      "buffer_load_dwordx4 v92, s[48:51], s66 offen lds\n"
      "buffer_load_dwordx4 v93, s[48:51], s66 offen offset:1024 lds\n"
      "buffer_load_dwordx4 v94, s[48:51], s66 offen offset:2048 lds\n"
      "buffer_load_dwordx4 v95, s[48:51], s66 offen offset:3072 lds\n"
      "s_mov_b32 m0, 159744\n"
      "s_nop 0\n"
      "buffer_load_dwordx4 v96, s[48:51], s66 offen lds\n"
      "buffer_load_dwordx4 v97, s[48:51], s66 offen offset:1024 lds\n"
      "buffer_load_dwordx4 v98, s[48:51], s66 offen offset:2048 lds\n"
      "buffer_load_dwordx4 v99, s[48:51], s66 offen offset:3072 lds\n"
      "s_add_u32 s74, s74, 8192\n"
      "s_and_b32 s74, s74, 0x7ffff\n"
      "s_mov_b32 m0, 81920\n"
      "s_add_u32 s66, s77, 229376\n"
      "s_and_b32 s66, s66, 0x3ffff\n"
      "buffer_load_dwordx4 v84, s[44:47], s66 offen nt lds\n"
      "buffer_load_dwordx4 v85, s[44:47], s66 offen offset:1024 nt lds\n"
      "buffer_load_dwordx4 v86, s[44:47], s66 offen offset:2048 nt lds\n"
      "buffer_load_dwordx4 v87, s[44:47], s66 offen offset:3072 nt lds\n"
      "s_mov_b32 m0, 86016\n"
      "s_nop 0\n"
      "buffer_load_dwordx4 v88, s[44:47], s66 offen nt lds\n"
      "buffer_load_dwordx4 v89, s[44:47], s66 offen offset:1024 nt lds\n"
      "buffer_load_dwordx4 v90, s[44:47], s66 offen offset:2048 nt lds\n"
      "buffer_load_dwordx4 v91, s[44:47], s66 offen offset:3072 nt lds\n"
      "s_waitcnt lgkmcnt(0)\n"
      "s_waitcnt vmcnt(48)\n"
      "s_barrier\n"
      "ds_read_b64_tr_b16 a[210:211], v14 offset:24576\n"
      "ds_read_b64_tr_b16 a[214:215], v14 offset:24584\n"
      "ds_read_b64_tr_b16 a[218:219], v15 offset:24576\n"
      "ds_read_b64_tr_b16 a[222:223], v15 offset:24584\n"
      "s_mov_b32 m0, 0\n"
      "s_add_u32 s66, s77, 245760\n"
      "s_and_b32 s66, s66, 0x3ffff\n"
      "buffer_load_dwordx4 v84, s[40:43], s66 offen nt lds\n"
      "buffer_load_dwordx4 v85, s[40:43], s66 offen offset:1024 nt lds\n"
      "buffer_load_dwordx4 v86, s[40:43], s66 offen offset:2048 nt lds\n"
      "buffer_load_dwordx4 v87, s[40:43], s66 offen offset:3072 nt lds\n"
      "s_mov_b32 m0, 4096\n"
      "s_nop 0\n"
      "buffer_load_dwordx4 v88, s[40:43], s66 offen nt lds\n"
      "buffer_load_dwordx4 v89, s[40:43], s66 offen offset:1024 nt lds\n"
      "buffer_load_dwordx4 v90, s[40:43], s66 offen offset:2048 nt lds\n"
      "buffer_load_dwordx4 v91, s[40:43], s66 offen offset:3072 nt lds\n"
      "s_mov_b32 m0, 98304\n"
      "s_add_u32 s66, s74, 0\n"
      "buffer_load_dwordx4 v92, s[48:51], s66 offen lds\n"
      "buffer_load_dwordx4 v93, s[48:51], s66 offen offset:1024 lds\n"
      "buffer_load_dwordx4 v94, s[48:51], s66 offen offset:2048 lds\n"
      "buffer_load_dwordx4 v95, s[48:51], s66 offen offset:3072 lds\n"
      "s_mov_b32 m0, 102400\n"
      "s_nop 0\n"
      "buffer_load_dwordx4 v96, s[48:51], s66 offen lds\n"
      "buffer_load_dwordx4 v97, s[48:51], s66 offen offset:1024 lds\n"
      "buffer_load_dwordx4 v98, s[48:51], s66 offen offset:2048 lds\n"
      "buffer_load_dwordx4 v99, s[48:51], s66 offen offset:3072 lds\n"
      "s_add_u32 s74, s74, 24576\n"
      "s_and_b32 s74, s74, 0x7ffff\n"
      "s_mov_b32 m0, 90112\n"
      "s_add_u32 s66, s77, 237568\n"
      "s_and_b32 s66, s66, 0x3ffff\n"
      "buffer_load_dwordx4 v84, s[44:47], s66 offen nt lds\n"
      "buffer_load_dwordx4 v85, s[44:47], s66 offen offset:1024 nt lds\n"
      "buffer_load_dwordx4 v86, s[44:47], s66 offen offset:2048 nt lds\n"
      "buffer_load_dwordx4 v87, s[44:47], s66 offen offset:3072 nt lds\n"
      "s_mov_b32 m0, 94208\n"
      "s_nop 0\n"
      "buffer_load_dwordx4 v88, s[44:47], s66 offen nt lds\n"
      "buffer_load_dwordx4 v89, s[44:47], s66 offen offset:1024 nt lds\n"
      "buffer_load_dwordx4 v90, s[44:47], s66 offen offset:2048 nt lds\n"
      "buffer_load_dwordx4 v91, s[44:47], s66 offen offset:3072 nt lds\n"
      "s_waitcnt lgkmcnt(0)\n"
      "s_waitcnt vmcnt(48)\n"
      "s_barrier\n"
      "ds_read_b64_tr_b16 a[224:225], v14 offset:32768\n"
      "ds_read_b64_tr_b16 a[228:229], v14 offset:32776\n"
      "ds_read_b64_tr_b16 a[232:233], v15 offset:32768\n"
      "ds_read_b64_tr_b16 a[236:237], v15 offset:32776\n"
      "s_mov_b32 m0, 8192\n"
      "s_add_u32 s66, s77, 253952\n"
      "s_and_b32 s66, s66, 0x3ffff\n"
      "buffer_load_dwordx4 v84, s[40:43], s66 offen nt lds\n"
      "buffer_load_dwordx4 v85, s[40:43], s66 offen offset:1024 nt lds\n"
      "buffer_load_dwordx4 v86, s[40:43], s66 offen offset:2048 nt lds\n"
      "buffer_load_dwordx4 v87, s[40:43], s66 offen offset:3072 nt lds\n"
      "s_mov_b32 m0, 12288\n"
      "s_nop 0\n"
      "buffer_load_dwordx4 v88, s[40:43], s66 offen nt lds\n"
      "buffer_load_dwordx4 v89, s[40:43], s66 offen offset:1024 nt lds\n"
      "buffer_load_dwordx4 v90, s[40:43], s66 offen offset:2048 nt lds\n"
      "buffer_load_dwordx4 v91, s[40:43], s66 offen offset:3072 nt lds\n"
      "s_mov_b32 m0, 49152\n"
      "s_add_u32 s66, s77, 245760\n"
      "s_and_b32 s66, s66, 0x3ffff\n"
      "buffer_load_dwordx4 v84, s[44:47], s66 offen nt lds\n"
      "buffer_load_dwordx4 v85, s[44:47], s66 offen offset:1024 nt lds\n"
      "buffer_load_dwordx4 v86, s[44:47], s66 offen offset:2048 nt lds\n"
      "buffer_load_dwordx4 v87, s[44:47], s66 offen offset:3072 nt lds\n"
      "s_mov_b32 m0, 53248\n"
      "s_nop 0\n"
      "buffer_load_dwordx4 v88, s[44:47], s66 offen nt lds\n"
      "buffer_load_dwordx4 v89, s[44:47], s66 offen offset:1024 nt lds\n"
      "buffer_load_dwordx4 v90, s[44:47], s66 offen offset:2048 nt lds\n"
      "buffer_load_dwordx4 v91, s[44:47], s66 offen offset:3072 nt lds\n"
      "s_mov_b32 m0, 57344\n"
      "s_add_u32 s66, s77, 253952\n"
      "s_and_b32 s66, s66, 0x3ffff\n"
      "buffer_load_dwordx4 v84, s[44:47], s66 offen nt lds\n"
      "buffer_load_dwordx4 v85, s[44:47], s66 offen offset:1024 nt lds\n"
      "buffer_load_dwordx4 v86, s[44:47], s66 offen offset:2048 nt lds\n"
      "buffer_load_dwordx4 v87, s[44:47], s66 offen offset:3072 nt lds\n"
      "s_mov_b32 m0, 61440\n"
      "s_nop 0\n"
      "buffer_load_dwordx4 v88, s[44:47], s66 offen nt lds\n"
      "buffer_load_dwordx4 v89, s[44:47], s66 offen offset:1024 nt lds\n"
      "buffer_load_dwordx4 v90, s[44:47], s66 offen offset:2048 nt lds\n"
      "buffer_load_dwordx4 v91, s[44:47], s66 offen offset:3072 nt lds\n"
      "s_waitcnt lgkmcnt(0)\n"
      "s_waitcnt vmcnt(48)\n"
      "s_barrier\n"
      "ds_read_b64_tr_b16 a[226:227], v14 offset:49152\n"
      "ds_read_b64_tr_b16 a[230:231], v14 offset:49160\n"
      "ds_read_b64_tr_b16 a[234:235], v15 offset:49152\n"
      "ds_read_b64_tr_b16 a[238:239], v15 offset:49160\n"
      "s_waitcnt lgkmcnt(0)\n"
      "s_waitcnt vmcnt(24)\n"
      "s_barrier\n"
      "ds_read_b64_tr_b16 a[240:241], v14 offset:57344\n"
      "ds_read_b64_tr_b16 a[244:245], v14 offset:57352\n"
      "ds_read_b64_tr_b16 a[248:249], v15 offset:57344\n"
      "ds_read_b64_tr_b16 a[252:253], v15 offset:57352\n"
      "s_waitcnt lgkmcnt(0)\n"
      "s_waitcnt vmcnt(16)\n"
      "s_barrier\n"
      "ds_read_b64_tr_b16 a[242:243], v14 offset:0\n"
      "ds_read_b64_tr_b16 a[246:247], v14 offset:8\n"
      "ds_read_b64_tr_b16 a[250:251], v15 offset:0\n"
      "ds_read_b64_tr_b16 a[254:255], v15 offset:8\n"
      "s_waitcnt lgkmcnt(0)\n"
      "s_waitcnt vmcnt(8)\n"
      "s_barrier\n"
      "s_lshl_b32 s29, s20, 1\n"
      "s_add_u32 s29, s29, 0\n"
      "s_sub_u32 s30, s21, s29\n"
      "s_and_b32 s30, s30, 7\n"
      "s_lshl_b32 s31, s28, 3\n"
      "s_add_u32 s29, s29, s31\n"
      "s_mul_i32 s29, s29, 26880\n"
      "s_mul_i32 s31, s30, 3840\n"
      "s_add_u32 s29, s29, s31\n"
      "s_add_u32 s45, s29, 8384768\n"
      "s_cmp_eq_u32 s30, 0\n"
      "s_cselect_b32 s31, 0x10000000, 0\n"
      "v_add_u32 v20, s31, v21\n"
      "v_lshrrev_b32 v22, 2, v20\n"
      "s_lshl_b32 s29, s20, 1\n"
      "s_add_u32 s29, s29, 1\n"
      "s_sub_u32 s30, s21, s29\n"
      "s_and_b32 s30, s30, 7\n"
      "s_lshl_b32 s31, s28, 3\n"
      "s_add_u32 s29, s29, s31\n"
      "s_mul_i32 s29, s29, 26880\n"
      "s_mul_i32 s31, s30, 3840\n"
      "s_add_u32 s29, s29, s31\n"
      "s_add_u32 s47, s29, 8384768\n"
      "s_cmp_eq_u32 s30, 0\n"
      "s_cselect_b32 s31, 0x10000000, 0\n"
      "v_add_u32 v4, s31, v21\n"
      "v_lshrrev_b32 v23, 2, v4\n"
      "s_mov_b32 s46, 0xffffff00\n"
      "s_mov_b32 s40, 15\n"
      "s_mov_b32 s42, 0\n"
      "s_lshl_b32 s29, s20, 2\n"
      "s_add_u32 s44, s29, 49168\n"
      "s_mul_i32 s30, %1, 26880\n"
      "s_mul_i32 s31, s20, 1280\n"
      "s_add_u32 s30, s30, s31\n"
      "s_add_u32 s48, s30, 8388608\n"
      "s_and_b32 s71, s60, -1\n"
      "v_mov_b32 v5, s71\n"
      "s_lshl_b32 s43, 1, s76\n"
      "s_mov_b32 s41, 0\n"
      "s_sub_u32 s31, s25, s22\n"
      "s_mul_i32 s29, s31, 24576\n"
      "s_mul_i32 s30, s26, 12288\n"
      "s_add_u32 s29, s29, s30\n"
      "s_add_u32 s36, s29, 1048576\n"
      "s_lshl_b32 s31, s31, 7\n"
      "s_add_u32 s37, s31, 4096\n"
      "s_lshl_b32 s69, 3, s26\n"
      "v_and_b32 v8, 3, v1\n"
      "v_bfe_u32 v9, v1, 2, 2\n"
      "v_lshlrev_b32 v8, 3, v8\n"
      "v_lshl_add_u32 v8, v9, 7, v8\n"
      "buffer_load_dwordx2 v[6:7], v8, s[52:55], s37 offen sc1\n"
      "s_waitcnt vmcnt(0)\n"
      "s_barrier\n"
      "s_mov_b32 s49, 0\n"
      "v_cmp_eq_u64 vcc, s[60:61], v[6:7]\n"
      "s_nop 1\n"
      "s_mov_b32 s71, vcc_lo\n"
      "s_add_u32 s30, s22, 1\n"
      "s_and_b32 s30, s30, 3\n"
      "s_lshl_b32 s29, s30, 2\n"
      "s_lshr_b32 s29, s71, s29\n"
      "s_and_b32 s29, s29, s69\n"
      "s_cmp_lg_u32 s29, s69\n"
      "s_cbranch_scc1 .Lle0_%=\n"
      "s_add_u32 s29, s76, 1\n"
      "s_and_b32 s29, s29, 3\n"
      "s_bitset1_b32 s49, s29\n"
      "s_mul_i32 s29, s29, 12288\n"
      "s_mul_i32 s30, s30, 24576\n"
      "s_add_u32 s30, s30, s36\n"
      "s_add_u32 m0, s29, 0\n"
      "s_add_u32 s66, s30, 0\n"
      "buffer_load_dwordx4 v21, s[52:55], s66 offen sc1 lds\n"
      "buffer_load_dwordx4 v21, s[52:55], s66 offen offset:1024 sc1 lds\n"
      "buffer_load_dwordx4 v21, s[52:55], s66 offen offset:2048 sc1 lds\n"
      "buffer_load_dwordx4 v21, s[52:55], s66 offen offset:3072 sc1 lds\n"
      "s_add_u32 m0, s29, 4096\n"
      "s_add_u32 s66, s30, 4096\n"
      "buffer_load_dwordx4 v21, s[52:55], s66 offen sc1 lds\n"
      "buffer_load_dwordx4 v21, s[52:55], s66 offen offset:1024 sc1 lds\n"
      "buffer_load_dwordx4 v21, s[52:55], s66 offen offset:2048 sc1 lds\n"
      "buffer_load_dwordx4 v21, s[52:55], s66 offen offset:3072 sc1 lds\n"
      "s_add_u32 m0, s29, 8192\n"
      "s_add_u32 s66, s30, 8192\n"
      "buffer_load_dwordx4 v21, s[52:55], s66 offen sc1 lds\n"
      "buffer_load_dwordx4 v21, s[52:55], s66 offen offset:1024 sc1 lds\n"
      "buffer_load_dwordx4 v21, s[52:55], s66 offen offset:2048 sc1 lds\n"
      "buffer_load_dwordx4 v21, s[52:55], s66 offen offset:3072 sc1 lds\n"
      ".Lle0_%=:\n"
      "s_add_u32 s30, s22, 2\n"
      "s_and_b32 s30, s30, 3\n"
      "s_lshl_b32 s29, s30, 2\n"
      "s_lshr_b32 s29, s71, s29\n"
      "s_and_b32 s29, s29, s69\n"
      "s_cmp_lg_u32 s29, s69\n"
      "s_cbranch_scc1 .Lle1_%=\n"
      "s_add_u32 s29, s76, 2\n"
      "s_and_b32 s29, s29, 3\n"
      "s_bitset1_b32 s49, s29\n"
      "s_mul_i32 s29, s29, 12288\n"
      "s_mul_i32 s30, s30, 24576\n"
      "s_add_u32 s30, s30, s36\n"
      "s_add_u32 m0, s29, 0\n"
      "s_add_u32 s66, s30, 0\n"
      "buffer_load_dwordx4 v21, s[52:55], s66 offen sc1 lds\n"
      "buffer_load_dwordx4 v21, s[52:55], s66 offen offset:1024 sc1 lds\n"
      "buffer_load_dwordx4 v21, s[52:55], s66 offen offset:2048 sc1 lds\n"
      "buffer_load_dwordx4 v21, s[52:55], s66 offen offset:3072 sc1 lds\n"
      "s_add_u32 m0, s29, 4096\n"
      "s_add_u32 s66, s30, 4096\n"
      "buffer_load_dwordx4 v21, s[52:55], s66 offen sc1 lds\n"
      "buffer_load_dwordx4 v21, s[52:55], s66 offen offset:1024 sc1 lds\n"
      "buffer_load_dwordx4 v21, s[52:55], s66 offen offset:2048 sc1 lds\n"
      "buffer_load_dwordx4 v21, s[52:55], s66 offen offset:3072 sc1 lds\n"
      "s_add_u32 m0, s29, 8192\n"
      "s_add_u32 s66, s30, 8192\n"
      "buffer_load_dwordx4 v21, s[52:55], s66 offen sc1 lds\n"
      "buffer_load_dwordx4 v21, s[52:55], s66 offen offset:1024 sc1 lds\n"
      "buffer_load_dwordx4 v21, s[52:55], s66 offen offset:2048 sc1 lds\n"
      "buffer_load_dwordx4 v21, s[52:55], s66 offen offset:3072 sc1 lds\n"
      ".Lle1_%=:\n"
      "s_add_u32 s30, s22, 3\n"
      "s_and_b32 s30, s30, 3\n"
      "s_lshl_b32 s29, s30, 2\n"
      "s_lshr_b32 s29, s71, s29\n"
      "s_and_b32 s29, s29, s69\n"
      "s_cmp_lg_u32 s29, s69\n"
      "s_cbranch_scc1 .Lle2_%=\n"
      "s_add_u32 s29, s76, 3\n"
      "s_and_b32 s29, s29, 3\n"
      "s_bitset1_b32 s49, s29\n"
      "s_mul_i32 s29, s29, 12288\n"
      "s_mul_i32 s30, s30, 24576\n"
      "s_add_u32 s30, s30, s36\n"
      "s_add_u32 m0, s29, 0\n"
      "s_add_u32 s66, s30, 0\n"
      "buffer_load_dwordx4 v21, s[52:55], s66 offen sc1 lds\n"
      "buffer_load_dwordx4 v21, s[52:55], s66 offen offset:1024 sc1 lds\n"
      "buffer_load_dwordx4 v21, s[52:55], s66 offen offset:2048 sc1 lds\n"
      "buffer_load_dwordx4 v21, s[52:55], s66 offen offset:3072 sc1 lds\n"
      "s_add_u32 m0, s29, 4096\n"
      "s_add_u32 s66, s30, 4096\n"
      "buffer_load_dwordx4 v21, s[52:55], s66 offen sc1 lds\n"
      "buffer_load_dwordx4 v21, s[52:55], s66 offen offset:1024 sc1 lds\n"
      "buffer_load_dwordx4 v21, s[52:55], s66 offen offset:2048 sc1 lds\n"
      "buffer_load_dwordx4 v21, s[52:55], s66 offen offset:3072 sc1 lds\n"
      "s_add_u32 m0, s29, 8192\n"
      "s_add_u32 s66, s30, 8192\n"
      "buffer_load_dwordx4 v21, s[52:55], s66 offen sc1 lds\n"
      "buffer_load_dwordx4 v21, s[52:55], s66 offen offset:1024 sc1 lds\n"
      "buffer_load_dwordx4 v21, s[52:55], s66 offen offset:2048 sc1 lds\n"
      "buffer_load_dwordx4 v21, s[52:55], s66 offen offset:3072 sc1 lds\n"
      ".Lle2_%=:\n"
      "v_mov_b32 v2, 49152\n"
      "v_mov_b32 v3, s49\n"
      "ds_write_b32 v2, v3\n"
      "s_branch .Ltail_%=\n"
      ".Lcompute_%=:\n"
      "v_and_b32 v2, 31, v1\n"
      "v_lshrrev_b32 v3, 5, v1\n"
      "v_bfe_u32 v4, v2, 2, 2\n"
      "v_xor_b32 v4, v4, v3\n"
      "v_and_b32 v5, 7, v2\n"
      "v_lshlrev_b32 v5, 6, v5\n"
      "v_lshl_add_u32 v5, v4, 4, v5\n"
      "v_lshrrev_b32 v6, 3, v2\n"
      "v_lshl_add_u32 v10, v6, 11, v5\n"
      "v_xor_b32 v11, 32, v10\n"
      "s_lshl_b32 s29, s20, 12\n"
      "s_add_u32 s29, s29, 139264\n"
      "v_lshl_add_u32 v7, v6, 10, v5\n"
      "v_add_u32 v210, s29, v7\n"
      "v_xor_b32 v211, 32, v210\n"
      "v_bfe_u32 v4, v1, 4, 1\n"
      "v_bfe_u32 v5, v1, 2, 2\n"
      "v_and_b32 v6, 3, v1\n"
      "v_lshrrev_b32 v7, 1, v6\n"
      "v_lshl_add_u32 v7, v4, 1, v7\n"
      "v_xor_b32 v7, v7, v3\n"
      "v_lshlrev_b32 v7, 4, v7\n"
      "v_and_b32 v8, 1, v6\n"
      "v_lshl_add_u32 v7, v8, 3, v7\n"
      "v_lshl_add_u32 v7, v5, 6, v7\n"
      "v_lshl_add_u32 v7, v3, 8, v7\n"
      "v_add_u32 v12, 49152, v7\n"
      "v_xor_b32 v13, 32, v12\n"
      "v_lshl_add_u32 v7, v4, 1, v8\n"
      "v_lshlrev_b32 v9, 1, v3\n"
      "v_xor_b32 v7, v7, v9\n"
      "v_lshlrev_b32 v7, 4, v7\n"
      "v_lshrrev_b32 v9, 1, v6\n"
      "v_lshl_add_u32 v7, v9, 3, v7\n"
      "v_lshl_add_u32 v7, v5, 6, v7\n"
      "v_lshl_add_u32 v7, v3, 11, v7\n"
      "v_add_u32 v208, 98304, v7\n"
      "v_xor_b32 v7, 16, v208\n"
      "v_add_u32 v209, 256, v7\n"
      "v_lshrrev_b32 v7, 4, v2\n"
      "v_and_b32 v8, 15, v2\n"
      "v_lshlrev_b32 v8, 4, v8\n"
      "v_lshl_add_u32 v8, v3, 8, v8\n"
      "v_lshl_add_u32 v8, v7, 12, v8\n"
      "s_lshl_b32 s29, s20, 13\n"
      "v_add_u32 v19, s29, v8\n"
      "v_lshrrev_b32 v2, 2, v1\n"
      "v_and_b32 v3, 3, v1\n"
      "v_and_b32 v4, 3, v2\n"
      "s_lshl_b32 s29, s20, 3\n"
      "v_lshlrev_b32 v4, 2, v4\n"
      "v_or_b32 v3, s29, v3\n"
      "v_xor_b32 v3, v3, v4\n"
      "v_lshlrev_b32 v3, 4, v3\n"
      "v_lshl_add_u32 v3, v2, 9, v3\n"
      "v_add_u32 v14, 98304, v3\n"
      "v_xor_b32 v15, 64, v14\n"
      "v_lshlrev_b32 v21, 4, v1\n"
      "s_lshl_b32 s29, s26, 1\n"
      "s_add_u32 s29, s29, s23\n"
      "s_and_b32 s29, s29, 3\n"
      "s_lshl_b32 s74, s29, 17\n"
      "s_sub_u32 s29, s22, s29\n"
      "s_and_b32 s76, s29, 3\n"
      "s_mov_b32 s72, 0x3e0293ee\n"
      "s_mov_b32 s73, 0x3e0293ee\n"
      "s_mov_b32 s67, 0x45800000\n"
      "s_getpc_b64 s[64:65]\n"
      ".Lanchor1_%=:\n"
      "s_add_u32 s92, s64, .Lrare1_%=-.Lanchor1_%=\n"
      "s_addc_u32 s93, s65, 0\n"
      "s_add_u32 s64, s64, .Lrare0_%=-.Lanchor1_%=\n"
      "s_addc_u32 s65, s65, 0\n"
      "s_mul_i32 s29, s25, 24576\n"
      "s_add_u32 s78, s29, 1048576\n"
      "s_lshl_b32 s29, s25, 7\n"
      "s_lshl_b32 s30, s20, 3\n"
      "s_add_u32 s29, s29, s30\n"
      "s_add_u32 s79, s29, 4096\n"
      "s_mov_b32 s75, 1\n"
      "s_mov_b32 s68, 0\n"
      "s_waitcnt vmcnt(20)\n"
      "s_barrier\n"
      "ds_read_b128 v[160:163], v210 offset:0\n"
      "ds_read_b128 v[164:167], v211 offset:0\n"
      "ds_read_b128 v[168:171], v210 offset:512\n"
      "ds_read_b128 v[172:175], v211 offset:512\n"
      "ds_read_b64_tr_b16 v[224:225], v208 offset:0\n"
      "ds_read_b64_tr_b16 v[226:227], v209 offset:0\n"
      "ds_read_b64_tr_b16 v[228:229], v208 offset:512\n"
      "ds_read_b64_tr_b16 v[230:231], v209 offset:512\n"
      "ds_read_b64_tr_b16 v[232:233], v208 offset:1024\n"
      "ds_read_b64_tr_b16 v[234:235], v209 offset:1024\n"
      "ds_read_b64_tr_b16 v[236:237], v208 offset:1536\n"
      "ds_read_b64_tr_b16 v[238:239], v209 offset:1536\n"
      "s_waitcnt lgkmcnt(8)\n"
      "ds_read_b64_tr_b16 v[240:241], v208 offset:4096\n"
      "ds_read_b64_tr_b16 v[242:243], v209 offset:4096\n"
      "ds_read_b64_tr_b16 v[244:245], v208 offset:4608\n"
      "ds_read_b64_tr_b16 v[246:247], v209 offset:4608\n"
      "s_waitcnt lgkmcnt(8)\n"
      "ds_read_b64_tr_b16 v[248:249], v208 offset:5120\n"
      "ds_read_b64_tr_b16 v[250:251], v209 offset:5120\n"
      "ds_read_b64_tr_b16 v[252:253], v208 offset:5632\n"
      "ds_read_b64_tr_b16 v[254:255], v209 offset:5632\n"
      "s_waitcnt lgkmcnt(0)\n"
      "s_waitcnt vmcnt(17)\n"
      "s_barrier\n"
      "v_mfma_f32_32x32x16_bf16 v[64:79], v[224:227], v[160:163], 0\n"
      "ds_read_b64_tr_b16 v[32:33], v208 offset:8192\n"
      "ds_read_b64_tr_b16 v[34:35], v209 offset:8192\n"
      "ds_read_b64_tr_b16 v[36:37], v208 offset:8704\n"
      "ds_read_b64_tr_b16 v[38:39], v209 offset:8704\n"
      "v_mfma_f32_32x32x16_bf16 v[80:95], v[228:231], v[160:163], 0\n"
      "ds_read_b64_tr_b16 v[40:41], v208 offset:9216\n"
      "ds_read_b64_tr_b16 v[42:43], v209 offset:9216\n"
      "ds_read_b64_tr_b16 v[44:45], v208 offset:9728\n"
      "ds_read_b64_tr_b16 v[46:47], v209 offset:9728\n"
      "v_mfma_f32_32x32x16_bf16 v[96:111], v[232:235], v[160:163], 0\n"
      "ds_read_b64_tr_b16 v[48:49], v208 offset:12288\n"
      "ds_read_b64_tr_b16 v[50:51], v209 offset:12288\n"
      "ds_read_b64_tr_b16 v[52:53], v208 offset:12800\n"
      "ds_read_b64_tr_b16 v[54:55], v209 offset:12800\n"
      "s_add_u32 m0, s95, 139264\n"
      "s_nop 0\n"
      "buffer_load_dwordx4 v212, s[96:99], s18 offen lds\n"
      "buffer_load_dwordx4 v213, s[96:99], s18 offen offset:1024 lds\n"
      "buffer_load_dwordx4 v214, s[96:99], s18 offen offset:2048 lds\n"
      "s_add_u32 s18, s18, 128\n"
      "s_and_b32 s18, s18, 1023\n"
      "v_mfma_f32_32x32x16_bf16 v[112:127], v[236:239], v[160:163], 0\n"
      "s_waitcnt lgkmcnt(8)\n"
      "ds_read_b64_tr_b16 v[56:57], v208 offset:13312\n"
      "ds_read_b64_tr_b16 v[58:59], v209 offset:13312\n"
      "ds_read_b64_tr_b16 v[60:61], v208 offset:13824\n"
      "ds_read_b64_tr_b16 v[62:63], v209 offset:13824\n"
      "s_add_u32 m0, s94, 98304\n"
      "s_nop 0\n"
      "buffer_load_dwordx4 v215, s[36:39], s19 offen lds\n"
      "buffer_load_dwordx4 v216, s[36:39], s19 offen offset:1024 lds\n"
      "s_add_u32 s19, s19, 0x40000\n"
      "s_and_b32 s19, s19, 0x3fffff\n"
      "v_mfma_f32_32x32x16_bf16 v[64:79], v[240:243], v[164:167], v[64:79]\n"
      "s_waitcnt lgkmcnt(8)\n"
      "v_mfma_f32_32x32x16_bf16 v[80:95], v[244:247], v[164:167], v[80:95]\n"
      "v_mfma_f32_32x32x16_bf16 v[96:111], v[248:251], v[164:167], v[96:111]\n"
      "v_mfma_f32_32x32x16_bf16 v[112:127], v[252:255], v[164:167], v[112:127]\n"
      "s_waitcnt lgkmcnt(0)\n"
      "s_waitcnt vmcnt(15)\n"
      "s_barrier\n"
      "v_mfma_f32_32x32x16_bf16 v[64:79], v[32:35], v[168:171], v[64:79]\n"
      "ds_read_b128 v[176:179], v210 offset:12288\n"
      "ds_read_b128 v[180:183], v211 offset:12288\n"
      "ds_read_b128 v[184:187], v210 offset:12800\n"
      "ds_read_b128 v[188:191], v211 offset:12800\n"
      "v_mfma_f32_32x32x16_bf16 v[80:95], v[36:39], v[168:171], v[80:95]\n"
      "ds_read_b64_tr_b16 v[224:225], v208 offset:16384\n"
      "ds_read_b64_tr_b16 v[226:227], v209 offset:16384\n"
      "ds_read_b64_tr_b16 v[228:229], v208 offset:16896\n"
      "ds_read_b64_tr_b16 v[230:231], v209 offset:16896\n"
      "v_mfma_f32_32x32x16_bf16 v[96:111], v[40:43], v[168:171], v[96:111]\n"
      "ds_read_b64_tr_b16 v[232:233], v208 offset:17408\n"
      "ds_read_b64_tr_b16 v[234:235], v209 offset:17408\n"
      "ds_read_b64_tr_b16 v[236:237], v208 offset:17920\n"
      "ds_read_b64_tr_b16 v[238:239], v209 offset:17920\n"
      "s_add_u32 m0, s94, 106496\n"
      "s_nop 0\n"
      "buffer_load_dwordx4 v215, s[36:39], s19 offen lds\n"
      "buffer_load_dwordx4 v216, s[36:39], s19 offen offset:1024 lds\n"
      "s_add_u32 s19, s19, 0x40000\n"
      "s_and_b32 s19, s19, 0x3fffff\n"
      "v_mfma_f32_32x32x16_bf16 v[112:127], v[44:47], v[168:171], v[112:127]\n"
      "s_waitcnt lgkmcnt(8)\n"
      "ds_read_b64_tr_b16 v[240:241], v208 offset:20480\n"
      "ds_read_b64_tr_b16 v[242:243], v209 offset:20480\n"
      "ds_read_b64_tr_b16 v[244:245], v208 offset:20992\n"
      "ds_read_b64_tr_b16 v[246:247], v209 offset:20992\n"
      "v_mfma_f32_32x32x16_bf16 v[64:79], v[48:51], v[172:175], v[64:79]\n"
      "s_waitcnt lgkmcnt(8)\n"
      "ds_read_b64_tr_b16 v[248:249], v208 offset:21504\n"
      "ds_read_b64_tr_b16 v[250:251], v209 offset:21504\n"
      "ds_read_b64_tr_b16 v[252:253], v208 offset:22016\n"
      "ds_read_b64_tr_b16 v[254:255], v209 offset:22016\n"
      "v_mfma_f32_32x32x16_bf16 v[80:95], v[52:55], v[172:175], v[80:95]\n"
      "v_mfma_f32_32x32x16_bf16 v[96:111], v[56:59], v[172:175], v[96:111]\n"
      "v_mfma_f32_32x32x16_bf16 v[112:127], v[60:63], v[172:175], v[112:127]\n"
      "s_waitcnt lgkmcnt(0)\n"
      "s_waitcnt vmcnt(15)\n"
      "s_barrier\n"
      "v_mfma_f32_32x32x16_bf16 v[64:79], v[224:227], v[176:179], v[64:79]\n"
      "ds_read_b64_tr_b16 v[32:33], v208 offset:24576\n"
      "ds_read_b64_tr_b16 v[34:35], v209 offset:24576\n"
      "ds_read_b64_tr_b16 v[36:37], v208 offset:25088\n"
      "ds_read_b64_tr_b16 v[38:39], v209 offset:25088\n"
      "v_mfma_f32_32x32x16_bf16 v[80:95], v[228:231], v[176:179], v[80:95]\n"
      "ds_read_b64_tr_b16 v[40:41], v208 offset:25600\n"
      "ds_read_b64_tr_b16 v[42:43], v209 offset:25600\n"
      "ds_read_b64_tr_b16 v[44:45], v208 offset:26112\n"
      "ds_read_b64_tr_b16 v[46:47], v209 offset:26112\n"
      "v_mfma_f32_32x32x16_bf16 v[96:111], v[232:235], v[176:179], v[96:111]\n"
      "ds_read_b64_tr_b16 v[48:49], v208 offset:28672\n"
      "ds_read_b64_tr_b16 v[50:51], v209 offset:28672\n"
      "ds_read_b64_tr_b16 v[52:53], v208 offset:29184\n"
      "ds_read_b64_tr_b16 v[54:55], v209 offset:29184\n"
      "s_add_u32 m0, s95, 151552\n"
      "s_nop 0\n"
      "buffer_load_dwordx4 v212, s[96:99], s18 offen lds\n"
      "buffer_load_dwordx4 v213, s[96:99], s18 offen offset:1024 lds\n"
      "buffer_load_dwordx4 v214, s[96:99], s18 offen offset:2048 lds\n"
      "s_add_u32 s18, s18, 128\n"
      "s_and_b32 s18, s18, 1023\n"
      "v_mfma_f32_32x32x16_bf16 v[112:127], v[236:239], v[176:179], v[112:127]\n"
      "s_waitcnt lgkmcnt(8)\n"
      "ds_read_b64_tr_b16 v[56:57], v208 offset:29696\n"
      "ds_read_b64_tr_b16 v[58:59], v209 offset:29696\n"
      "ds_read_b64_tr_b16 v[60:61], v208 offset:30208\n"
      "ds_read_b64_tr_b16 v[62:63], v209 offset:30208\n"
      "s_add_u32 m0, s94, 114688\n"
      "s_nop 0\n"
      "buffer_load_dwordx4 v215, s[36:39], s19 offen lds\n"
      "buffer_load_dwordx4 v216, s[36:39], s19 offen offset:1024 lds\n"
      "s_add_u32 s19, s19, 0x40000\n"
      "s_and_b32 s19, s19, 0x3fffff\n"
      "v_mfma_f32_32x32x16_bf16 v[64:79], v[240:243], v[180:183], v[64:79]\n"
      "s_waitcnt lgkmcnt(8)\n"
      "v_mfma_f32_32x32x16_bf16 v[80:95], v[244:247], v[180:183], v[80:95]\n"
      "v_mfma_f32_32x32x16_bf16 v[96:111], v[248:251], v[180:183], v[96:111]\n"
      "v_mfma_f32_32x32x16_bf16 v[112:127], v[252:255], v[180:183], v[112:127]\n"
      "s_waitcnt lgkmcnt(0)\n"
      "s_waitcnt vmcnt(9)\n"
      "s_barrier\n"
      "v_mfma_f32_32x32x16_bf16 v[64:79], v[32:35], v[184:187], v[64:79]\n"
      "ds_read_b128 v[160:163], v210 offset:0\n"
      "ds_read_b128 v[164:167], v211 offset:0\n"
      "ds_read_b128 v[168:171], v210 offset:512\n"
      "ds_read_b128 v[172:175], v211 offset:512\n"
      "v_mfma_f32_32x32x16_bf16 v[80:95], v[36:39], v[184:187], v[80:95]\n"
      "ds_read_b64_tr_b16 v[224:225], v208 offset:32768\n"
      "ds_read_b64_tr_b16 v[226:227], v209 offset:32768\n"
      "ds_read_b64_tr_b16 v[228:229], v208 offset:33280\n"
      "ds_read_b64_tr_b16 v[230:231], v209 offset:33280\n"
      "v_mfma_f32_32x32x16_bf16 v[96:111], v[40:43], v[184:187], v[96:111]\n"
      "ds_read_b64_tr_b16 v[232:233], v208 offset:33792\n"
      "ds_read_b64_tr_b16 v[234:235], v209 offset:33792\n"
      "ds_read_b64_tr_b16 v[236:237], v208 offset:34304\n"
      "ds_read_b64_tr_b16 v[238:239], v209 offset:34304\n"
      "s_add_u32 m0, s94, 122880\n"
      "s_nop 0\n"
      "buffer_load_dwordx4 v215, s[36:39], s19 offen lds\n"
      "buffer_load_dwordx4 v216, s[36:39], s19 offen offset:1024 lds\n"
      "s_add_u32 s19, s19, 0x40000\n"
      "s_and_b32 s19, s19, 0x3fffff\n"
      "v_mfma_f32_32x32x16_bf16 v[112:127], v[44:47], v[184:187], v[112:127]\n"
      "s_waitcnt lgkmcnt(8)\n"
      "ds_read_b64_tr_b16 v[240:241], v208 offset:36864\n"
      "ds_read_b64_tr_b16 v[242:243], v209 offset:36864\n"
      "ds_read_b64_tr_b16 v[244:245], v208 offset:37376\n"
      "ds_read_b64_tr_b16 v[246:247], v209 offset:37376\n"
      "s_add_u32 m0, s94, 57344\n"
      "s_add_u32 s66, s77, 8192\n"
      "s_and_b32 s66, s66, 0x3ffff\n"
      "buffer_load_dwordx4 v217, s[44:47], s66 offen nt lds\n"
      "buffer_load_dwordx4 v218, s[44:47], s66 offen offset:1024 nt lds\n"
      "v_mfma_f32_32x32x16_bf16 v[64:79], v[48:51], v[188:191], v[64:79]\n"
      "s_waitcnt lgkmcnt(8)\n"
      "ds_read_b64_tr_b16 v[248:249], v208 offset:37888\n"
      "ds_read_b64_tr_b16 v[250:251], v209 offset:37888\n"
      "ds_read_b64_tr_b16 v[252:253], v208 offset:38400\n"
      "ds_read_b64_tr_b16 v[254:255], v209 offset:38400\n"
      "s_add_u32 m0, s94, 24576\n"
      "s_add_u32 s66, s77, 24576\n"
      "s_and_b32 s66, s66, 0x3ffff\n"
      "buffer_load_dwordx4 v217, s[40:43], s66 offen nt lds\n"
      "buffer_load_dwordx4 v218, s[40:43], s66 offen offset:1024 nt lds\n"
      "v_mfma_f32_32x32x16_bf16 v[80:95], v[52:55], v[188:191], v[80:95]\n"
      "s_add_u32 m0, s94, 65536\n"
      "s_add_u32 s66, s77, 16384\n"
      "s_and_b32 s66, s66, 0x3ffff\n"
      "buffer_load_dwordx4 v217, s[44:47], s66 offen nt lds\n"
      "buffer_load_dwordx4 v218, s[44:47], s66 offen offset:1024 nt lds\n"
      "v_mfma_f32_32x32x16_bf16 v[96:111], v[56:59], v[188:191], v[96:111]\n"
      "v_mfma_f32_32x32x16_bf16 v[112:127], v[60:63], v[188:191], v[112:127]\n"
      "s_waitcnt lgkmcnt(0)\n"
      "s_waitcnt vmcnt(15)\n"
      "s_barrier\n"
      "v_mfma_f32_32x32x16_bf16 v[64:79], v[224:227], v[160:163], v[64:79]\n"
      "ds_read_b64_tr_b16 v[32:33], v208 offset:0\n"
      "ds_read_b64_tr_b16 v[34:35], v209 offset:0\n"
      "ds_read_b64_tr_b16 v[36:37], v208 offset:512\n"
      "ds_read_b64_tr_b16 v[38:39], v209 offset:512\n"
      "v_mfma_f32_32x32x16_bf16 v[80:95], v[228:231], v[160:163], v[80:95]\n"
      "ds_read_b64_tr_b16 v[40:41], v208 offset:1024\n"
      "ds_read_b64_tr_b16 v[42:43], v209 offset:1024\n"
      "ds_read_b64_tr_b16 v[44:45], v208 offset:1536\n"
      "ds_read_b64_tr_b16 v[46:47], v209 offset:1536\n"
      "v_mfma_f32_32x32x16_bf16 v[96:111], v[232:235], v[160:163], v[96:111]\n"
      "ds_read_b64_tr_b16 v[48:49], v208 offset:4096\n"
      "ds_read_b64_tr_b16 v[50:51], v209 offset:4096\n"
      "ds_read_b64_tr_b16 v[52:53], v208 offset:4608\n"
      "ds_read_b64_tr_b16 v[54:55], v209 offset:4608\n"
      "s_add_u32 m0, s95, 139264\n"
      "s_nop 0\n"
      "buffer_load_dwordx4 v212, s[96:99], s18 offen lds\n"
      "buffer_load_dwordx4 v213, s[96:99], s18 offen offset:1024 lds\n"
      "buffer_load_dwordx4 v214, s[96:99], s18 offen offset:2048 lds\n"
      "s_add_u32 s18, s18, 128\n"
      "s_and_b32 s18, s18, 1023\n"
      "v_mfma_f32_32x32x16_bf16 v[112:127], v[236:239], v[160:163], v[112:127]\n"
      "s_waitcnt lgkmcnt(8)\n"
      "ds_read_b64_tr_b16 v[56:57], v208 offset:5120\n"
      "ds_read_b64_tr_b16 v[58:59], v209 offset:5120\n"
      "ds_read_b64_tr_b16 v[60:61], v208 offset:5632\n"
      "ds_read_b64_tr_b16 v[62:63], v209 offset:5632\n"
      "s_add_u32 m0, s94, 131072\n"
      "s_nop 0\n"
      "buffer_load_dwordx4 v215, s[36:39], s19 offen lds\n"
      "buffer_load_dwordx4 v216, s[36:39], s19 offen offset:1024 lds\n"
      "s_add_u32 s19, s19, 0x40000\n"
      "s_and_b32 s19, s19, 0x3fffff\n"
      "v_mfma_f32_32x32x16_bf16 v[64:79], v[240:243], v[164:167], v[64:79]\n"
      "s_waitcnt lgkmcnt(8)\n"
      "v_mfma_f32_32x32x16_bf16 v[80:95], v[244:247], v[164:167], v[80:95]\n"
      "v_mfma_f32_32x32x16_bf16 v[96:111], v[248:251], v[164:167], v[96:111]\n"
      "v_mfma_f32_32x32x16_bf16 v[112:127], v[252:255], v[164:167], v[112:127]\n"
      "s_waitcnt lgkmcnt(0)\n"
      "s_waitcnt vmcnt(15)\n"
      "s_barrier\n"
      "v_mfma_f32_32x32x16_bf16 v[64:79], v[32:35], v[168:171], v[64:79]\n"
      "ds_read_b128 v[176:179], v210 offset:12288\n"
      "ds_read_b128 v[180:183], v211 offset:12288\n"
      "ds_read_b128 v[184:187], v210 offset:12800\n"
      "ds_read_b128 v[188:191], v211 offset:12800\n"
      "v_mfma_f32_32x32x16_bf16 v[80:95], v[36:39], v[168:171], v[80:95]\n"
      "ds_read_b64_tr_b16 v[224:225], v208 offset:8192\n"
      "ds_read_b64_tr_b16 v[226:227], v209 offset:8192\n"
      "ds_read_b64_tr_b16 v[228:229], v208 offset:8704\n"
      "ds_read_b64_tr_b16 v[230:231], v209 offset:8704\n"
      "v_mfma_f32_32x32x16_bf16 v[96:111], v[40:43], v[168:171], v[96:111]\n"
      "ds_read_b64_tr_b16 v[232:233], v208 offset:9216\n"
      "ds_read_b64_tr_b16 v[234:235], v209 offset:9216\n"
      "ds_read_b64_tr_b16 v[236:237], v208 offset:9728\n"
      "ds_read_b64_tr_b16 v[238:239], v209 offset:9728\n"
      "s_add_u32 m0, s94, 98304\n"
      "s_nop 0\n"
      "buffer_load_dwordx4 v215, s[36:39], s19 offen lds\n"
      "buffer_load_dwordx4 v216, s[36:39], s19 offen offset:1024 lds\n"
      "s_add_u32 s19, s19, 0x40000\n"
      "s_and_b32 s19, s19, 0x3fffff\n"
      "v_mfma_f32_32x32x16_bf16 v[112:127], v[44:47], v[168:171], v[112:127]\n"
      "s_waitcnt lgkmcnt(8)\n"
      "ds_read_b64_tr_b16 v[240:241], v208 offset:12288\n"
      "ds_read_b64_tr_b16 v[242:243], v209 offset:12288\n"
      "ds_read_b64_tr_b16 v[244:245], v208 offset:12800\n"
      "ds_read_b64_tr_b16 v[246:247], v209 offset:12800\n"
      "v_mfma_f32_32x32x16_bf16 v[64:79], v[48:51], v[172:175], v[64:79]\n"
      "s_waitcnt lgkmcnt(8)\n"
      "ds_read_b64_tr_b16 v[248:249], v208 offset:13312\n"
      "ds_read_b64_tr_b16 v[250:251], v209 offset:13312\n"
      "ds_read_b64_tr_b16 v[252:253], v208 offset:13824\n"
      "ds_read_b64_tr_b16 v[254:255], v209 offset:13824\n"
      "v_mfma_f32_32x32x16_bf16 v[80:95], v[52:55], v[172:175], v[80:95]\n"
      "v_mfma_f32_32x32x16_bf16 v[96:111], v[56:59], v[172:175], v[96:111]\n"
      "v_mfma_f32_32x32x16_bf16 v[112:127], v[60:63], v[172:175], v[112:127]\n"
      "s_waitcnt lgkmcnt(0)\n"
      "s_waitcnt vmcnt(15)\n"
      "s_barrier\n"
      "v_mfma_f32_32x32x16_bf16 v[64:79], v[224:227], v[176:179], v[64:79]\n"
      "ds_read_b64_tr_b16 v[32:33], v208 offset:16384\n"
      "ds_read_b64_tr_b16 v[34:35], v209 offset:16384\n"
      "ds_read_b64_tr_b16 v[36:37], v208 offset:16896\n"
      "ds_read_b64_tr_b16 v[38:39], v209 offset:16896\n"
      "v_mfma_f32_32x32x16_bf16 v[80:95], v[228:231], v[176:179], v[80:95]\n"
      "ds_read_b64_tr_b16 v[40:41], v208 offset:17408\n"
      "ds_read_b64_tr_b16 v[42:43], v209 offset:17408\n"
      "ds_read_b64_tr_b16 v[44:45], v208 offset:17920\n"
      "ds_read_b64_tr_b16 v[46:47], v209 offset:17920\n"
      "v_mfma_f32_32x32x16_bf16 v[96:111], v[232:235], v[176:179], v[96:111]\n"
      "ds_read_b64_tr_b16 v[48:49], v208 offset:20480\n"
      "ds_read_b64_tr_b16 v[50:51], v209 offset:20480\n"
      "ds_read_b64_tr_b16 v[52:53], v208 offset:20992\n"
      "ds_read_b64_tr_b16 v[54:55], v209 offset:20992\n"
      "s_add_u32 m0, s95, 151552\n"
      "s_nop 0\n"
      "buffer_load_dwordx4 v212, s[96:99], s18 offen lds\n"
      "buffer_load_dwordx4 v213, s[96:99], s18 offen offset:1024 lds\n"
      "buffer_load_dwordx4 v214, s[96:99], s18 offen offset:2048 lds\n"
      "s_add_u32 s18, s18, 128\n"
      "s_and_b32 s18, s18, 1023\n"
      "v_mfma_f32_32x32x16_bf16 v[112:127], v[236:239], v[176:179], v[112:127]\n"
      "s_waitcnt lgkmcnt(8)\n"
      "ds_read_b64_tr_b16 v[56:57], v208 offset:21504\n"
      "ds_read_b64_tr_b16 v[58:59], v209 offset:21504\n"
      "ds_read_b64_tr_b16 v[60:61], v208 offset:22016\n"
      "ds_read_b64_tr_b16 v[62:63], v209 offset:22016\n"
      "s_add_u32 m0, s94, 106496\n"
      "s_nop 0\n"
      "buffer_load_dwordx4 v215, s[36:39], s19 offen lds\n"
      "buffer_load_dwordx4 v216, s[36:39], s19 offen offset:1024 lds\n"
      "s_add_u32 s19, s19, 0x40000\n"
      "s_and_b32 s19, s19, 0x3fffff\n"
      "v_mfma_f32_32x32x16_bf16 v[64:79], v[240:243], v[180:183], v[64:79]\n"
      "s_waitcnt lgkmcnt(8)\n"
      "v_mfma_f32_32x32x16_bf16 v[80:95], v[244:247], v[180:183], v[80:95]\n"
      "v_mfma_f32_32x32x16_bf16 v[96:111], v[248:251], v[180:183], v[96:111]\n"
      "v_mfma_f32_32x32x16_bf16 v[112:127], v[252:255], v[180:183], v[112:127]\n"
      "s_waitcnt lgkmcnt(0)\n"
      "s_waitcnt vmcnt(9)\n"
      "s_barrier\n"
      "v_mfma_f32_32x32x16_bf16 v[64:79], v[32:35], v[184:187], v[64:79]\n"
      "ds_read_b128 v[160:163], v210 offset:0\n"
      "ds_read_b128 v[164:167], v211 offset:0\n"
      "ds_read_b128 v[168:171], v210 offset:512\n"
      "ds_read_b128 v[172:175], v211 offset:512\n"
      "v_mfma_f32_32x32x16_bf16 v[80:95], v[36:39], v[184:187], v[80:95]\n"
      "ds_read_b64_tr_b16 v[224:225], v208 offset:24576\n"
      "ds_read_b64_tr_b16 v[226:227], v209 offset:24576\n"
      "ds_read_b64_tr_b16 v[228:229], v208 offset:25088\n"
      "ds_read_b64_tr_b16 v[230:231], v209 offset:25088\n"
      "v_mfma_f32_32x32x16_bf16 v[96:111], v[40:43], v[184:187], v[96:111]\n"
      "ds_read_b64_tr_b16 v[232:233], v208 offset:25600\n"
      "ds_read_b64_tr_b16 v[234:235], v209 offset:25600\n"
      "ds_read_b64_tr_b16 v[236:237], v208 offset:26112\n"
      "ds_read_b64_tr_b16 v[238:239], v209 offset:26112\n"
      "s_add_u32 m0, s94, 114688\n"
      "s_nop 0\n"
      "buffer_load_dwordx4 v215, s[36:39], s19 offen lds\n"
      "buffer_load_dwordx4 v216, s[36:39], s19 offen offset:1024 lds\n"
      "s_add_u32 s19, s19, 0x40000\n"
      "s_and_b32 s19, s19, 0x3fffff\n"
      "v_mfma_f32_32x32x16_bf16 v[112:127], v[44:47], v[184:187], v[112:127]\n"
      "s_waitcnt lgkmcnt(8)\n"
      "ds_read_b64_tr_b16 v[240:241], v208 offset:28672\n"
      "ds_read_b64_tr_b16 v[242:243], v209 offset:28672\n"
      "ds_read_b64_tr_b16 v[244:245], v208 offset:29184\n"
      "ds_read_b64_tr_b16 v[246:247], v209 offset:29184\n"
      "s_add_u32 m0, s94, 32768\n"
      "s_add_u32 s66, s77, 32768\n"
      "s_and_b32 s66, s66, 0x3ffff\n"
      "buffer_load_dwordx4 v217, s[40:43], s66 offen nt lds\n"
      "buffer_load_dwordx4 v218, s[40:43], s66 offen offset:1024 nt lds\n"
      "v_mfma_f32_32x32x16_bf16 v[64:79], v[48:51], v[188:191], v[64:79]\n"
      "s_waitcnt lgkmcnt(8)\n"
      "ds_read_b64_tr_b16 v[248:249], v208 offset:29696\n"
      "ds_read_b64_tr_b16 v[250:251], v209 offset:29696\n"
      "ds_read_b64_tr_b16 v[252:253], v208 offset:30208\n"
      "ds_read_b64_tr_b16 v[254:255], v209 offset:30208\n"
      "s_add_u32 m0, s94, 73728\n"
      "s_add_u32 s66, s77, 24576\n"
      "s_and_b32 s66, s66, 0x3ffff\n"
      "buffer_load_dwordx4 v217, s[44:47], s66 offen nt lds\n"
      "buffer_load_dwordx4 v218, s[44:47], s66 offen offset:1024 nt lds\n"
      "v_mfma_f32_32x32x16_bf16 v[80:95], v[52:55], v[188:191], v[80:95]\n"
      "v_mfma_f32_32x32x16_bf16 v[96:111], v[56:59], v[188:191], v[96:111]\n"
      "v_mfma_f32_32x32x16_bf16 v[112:127], v[60:63], v[188:191], v[112:127]\n"
      "s_waitcnt lgkmcnt(0)\n"
      "s_waitcnt vmcnt(13)\n"
      "s_barrier\n"
      "v_readfirstlane_b32 s60, v220\n"
      "v_readfirstlane_b32 s61, v221\n"
      "s_nop 1\n"
      "s_add_u32 s60, s60, 0x7f4a7c15\n"
      "s_addc_u32 s61, s61, 0x9e3779b9\n"
      "v_mfma_f32_32x32x16_bf16 v[64:79], v[224:227], v[160:163], v[64:79]\n"
      "ds_read_b64_tr_b16 v[32:33], v208 offset:32768\n"
      "ds_read_b64_tr_b16 v[34:35], v209 offset:32768\n"
      "ds_read_b64_tr_b16 v[36:37], v208 offset:33280\n"
      "ds_read_b64_tr_b16 v[38:39], v209 offset:33280\n"
      "v_mfma_f32_32x32x16_bf16 v[80:95], v[228:231], v[160:163], v[80:95]\n"
      "ds_read_b64_tr_b16 v[40:41], v208 offset:33792\n"
      "ds_read_b64_tr_b16 v[42:43], v209 offset:33792\n"
      "ds_read_b64_tr_b16 v[44:45], v208 offset:34304\n"
      "ds_read_b64_tr_b16 v[46:47], v209 offset:34304\n"
      "v_mfma_f32_32x32x16_bf16 v[96:111], v[232:235], v[160:163], v[96:111]\n"
      "ds_read_b64_tr_b16 v[48:49], v208 offset:36864\n"
      "ds_read_b64_tr_b16 v[50:51], v209 offset:36864\n"
      "ds_read_b64_tr_b16 v[52:53], v208 offset:37376\n"
      "ds_read_b64_tr_b16 v[54:55], v209 offset:37376\n"
      "s_add_u32 m0, s95, 139264\n"
      "s_nop 0\n"
      "buffer_load_dwordx4 v212, s[96:99], s18 offen lds\n"
      "buffer_load_dwordx4 v213, s[96:99], s18 offen offset:1024 lds\n"
      "buffer_load_dwordx4 v214, s[96:99], s18 offen offset:2048 lds\n"
      "s_add_u32 s18, s18, 128\n"
      "s_and_b32 s18, s18, 1023\n"
      "v_mfma_f32_32x32x16_bf16 v[112:127], v[236:239], v[160:163], v[112:127]\n"
      "s_waitcnt lgkmcnt(8)\n"
      "ds_read_b64_tr_b16 v[56:57], v208 offset:37888\n"
      "ds_read_b64_tr_b16 v[58:59], v209 offset:37888\n"
      "ds_read_b64_tr_b16 v[60:61], v208 offset:38400\n"
      "ds_read_b64_tr_b16 v[62:63], v209 offset:38400\n"
      "s_add_u32 m0, s94, 122880\n"
      "s_nop 0\n"
      "buffer_load_dwordx4 v215, s[36:39], s19 offen lds\n"
      "buffer_load_dwordx4 v216, s[36:39], s19 offen offset:1024 lds\n"
      "s_add_u32 s19, s19, 0x40000\n"
      "s_and_b32 s19, s19, 0x3fffff\n"
      "v_mfma_f32_32x32x16_bf16 v[64:79], v[240:243], v[164:167], v[64:79]\n"
      "s_waitcnt lgkmcnt(8)\n"
      "v_mfma_f32_32x32x16_bf16 v[80:95], v[244:247], v[164:167], v[80:95]\n"
      "v_mfma_f32_32x32x16_bf16 v[96:111], v[248:251], v[164:167], v[96:111]\n"
      "v_mfma_f32_32x32x16_bf16 v[112:127], v[252:255], v[164:167], v[112:127]\n"
      "s_waitcnt lgkmcnt(0)\n"
      "s_waitcnt vmcnt(13)\n"
      "s_barrier\n"
      "v_mfma_f32_32x32x16_bf16 v[64:79], v[32:35], v[168:171], v[64:79]\n"
      "ds_read_b128 v[176:179], v210 offset:12288\n"
      "ds_read_b128 v[180:183], v211 offset:12288\n"
      "ds_read_b128 v[184:187], v210 offset:12800\n"
      "ds_read_b128 v[188:191], v211 offset:12800\n"
      "v_mfma_f32_32x32x16_bf16 v[80:95], v[36:39], v[168:171], v[80:95]\n"
      "ds_read_b64_tr_b16 v[224:225], v208 offset:0\n"
      "ds_read_b64_tr_b16 v[226:227], v209 offset:0\n"
      "ds_read_b64_tr_b16 v[228:229], v208 offset:512\n"
      "ds_read_b64_tr_b16 v[230:231], v209 offset:512\n"
      "v_mfma_f32_32x32x16_bf16 v[96:111], v[40:43], v[168:171], v[96:111]\n"
      "ds_read_b64_tr_b16 v[232:233], v208 offset:1024\n"
      "ds_read_b64_tr_b16 v[234:235], v209 offset:1024\n"
      "ds_read_b64_tr_b16 v[236:237], v208 offset:1536\n"
      "ds_read_b64_tr_b16 v[238:239], v209 offset:1536\n"
      "s_add_u32 m0, s94, 131072\n"
      "s_nop 0\n"
      "buffer_load_dwordx4 v215, s[36:39], s19 offen lds\n"
      "buffer_load_dwordx4 v216, s[36:39], s19 offen offset:1024 lds\n"
      "s_add_u32 s19, s19, 0x40000\n"
      "s_and_b32 s19, s19, 0x3fffff\n"
      "v_mfma_f32_32x32x16_bf16 v[112:127], v[44:47], v[168:171], v[112:127]\n"
      "s_waitcnt lgkmcnt(8)\n"
      "ds_read_b64_tr_b16 v[240:241], v208 offset:4096\n"
      "ds_read_b64_tr_b16 v[242:243], v209 offset:4096\n"
      "ds_read_b64_tr_b16 v[244:245], v208 offset:4608\n"
      "ds_read_b64_tr_b16 v[246:247], v209 offset:4608\n"
      "v_mfma_f32_32x32x16_bf16 v[64:79], v[48:51], v[172:175], v[64:79]\n"
      "s_waitcnt lgkmcnt(8)\n"
      "ds_read_b64_tr_b16 v[248:249], v208 offset:5120\n"
      "ds_read_b64_tr_b16 v[250:251], v209 offset:5120\n"
      "ds_read_b64_tr_b16 v[252:253], v208 offset:5632\n"
      "ds_read_b64_tr_b16 v[254:255], v209 offset:5632\n"
      "v_mfma_f32_32x32x16_bf16 v[80:95], v[52:55], v[172:175], v[80:95]\n"
      "v_mfma_f32_32x32x16_bf16 v[96:111], v[56:59], v[172:175], v[96:111]\n"
      "v_mfma_f32_32x32x16_bf16 v[112:127], v[60:63], v[172:175], v[112:127]\n"
      "s_waitcnt lgkmcnt(0)\n"
      "s_waitcnt vmcnt(13)\n"
      "s_barrier\n"
      "v_mfma_f32_32x32x16_bf16 v[64:79], v[224:227], v[176:179], v[64:79]\n"
      "ds_read_b64_tr_b16 v[32:33], v208 offset:8192\n"
      "ds_read_b64_tr_b16 v[34:35], v209 offset:8192\n"
      "ds_read_b64_tr_b16 v[36:37], v208 offset:8704\n"
      "ds_read_b64_tr_b16 v[38:39], v209 offset:8704\n"
      "v_mfma_f32_32x32x16_bf16 v[80:95], v[228:231], v[176:179], v[80:95]\n"
      "ds_read_b64_tr_b16 v[40:41], v208 offset:9216\n"
      "ds_read_b64_tr_b16 v[42:43], v209 offset:9216\n"
      "ds_read_b64_tr_b16 v[44:45], v208 offset:9728\n"
      "ds_read_b64_tr_b16 v[46:47], v209 offset:9728\n"
      "v_mfma_f32_32x32x16_bf16 v[96:111], v[232:235], v[176:179], v[96:111]\n"
      "ds_read_b64_tr_b16 v[48:49], v208 offset:12288\n"
      "ds_read_b64_tr_b16 v[50:51], v209 offset:12288\n"
      "ds_read_b64_tr_b16 v[52:53], v208 offset:12800\n"
      "ds_read_b64_tr_b16 v[54:55], v209 offset:12800\n"
      "s_add_u32 m0, s95, 151552\n"
      "s_nop 0\n"
      "buffer_load_dwordx4 v212, s[96:99], s18 offen lds\n"
      "buffer_load_dwordx4 v213, s[96:99], s18 offen offset:1024 lds\n"
      "buffer_load_dwordx4 v214, s[96:99], s18 offen offset:2048 lds\n"
      "s_add_u32 s18, s18, 128\n"
      "s_and_b32 s18, s18, 1023\n"
      "v_mfma_f32_32x32x16_bf16 v[112:127], v[236:239], v[176:179], v[112:127]\n"
      "s_waitcnt lgkmcnt(8)\n"
      "ds_read_b64_tr_b16 v[56:57], v208 offset:13312\n"
      "ds_read_b64_tr_b16 v[58:59], v209 offset:13312\n"
      "ds_read_b64_tr_b16 v[60:61], v208 offset:13824\n"
      "ds_read_b64_tr_b16 v[62:63], v209 offset:13824\n"
      "s_add_u32 m0, s94, 98304\n"
      "s_nop 0\n"
      "buffer_load_dwordx4 v215, s[36:39], s19 offen lds\n"
      "buffer_load_dwordx4 v216, s[36:39], s19 offen offset:1024 lds\n"
      "s_add_u32 s19, s19, 0x40000\n"
      "s_and_b32 s19, s19, 0x3fffff\n"
      "v_mfma_f32_32x32x16_bf16 v[64:79], v[240:243], v[180:183], v[64:79]\n"
      "s_waitcnt lgkmcnt(8)\n"
      "s_add_u32 m0, s94, 40960\n"
      "s_add_u32 s66, s77, 40960\n"
      "s_and_b32 s66, s66, 0x3ffff\n"
      "buffer_load_dwordx4 v217, s[40:43], s66 offen nt lds\n"
      "buffer_load_dwordx4 v218, s[40:43], s66 offen offset:1024 nt lds\n"
      "v_mfma_f32_32x32x16_bf16 v[80:95], v[244:247], v[180:183], v[80:95]\n"
      "s_add_u32 m0, s94, 81920\n"
      "s_add_u32 s66, s77, 32768\n"
      "s_and_b32 s66, s66, 0x3ffff\n"
      "buffer_load_dwordx4 v217, s[44:47], s66 offen nt lds\n"
      "buffer_load_dwordx4 v218, s[44:47], s66 offen offset:1024 nt lds\n"
      "v_mfma_f32_32x32x16_bf16 v[96:111], v[248:251], v[180:183], v[96:111]\n"
      "v_mfma_f32_32x32x16_bf16 v[112:127], v[252:255], v[180:183], v[112:127]\n"
      "s_waitcnt lgkmcnt(0)\n"
      "s_waitcnt vmcnt(13)\n"
      "s_barrier\n"
      "v_mfma_f32_32x32x16_bf16 v[64:79], v[32:35], v[184:187], v[64:79]\n"
      "ds_read_b128 v[160:163], v210 offset:0\n"
      "ds_read_b128 v[164:167], v211 offset:0\n"
      "ds_read_b128 v[168:171], v210 offset:512\n"
      "ds_read_b128 v[172:175], v211 offset:512\n"
      "v_mfma_f32_32x32x16_bf16 v[80:95], v[36:39], v[184:187], v[80:95]\n"
      "ds_read_b64_tr_b16 v[224:225], v208 offset:16384\n"
      "ds_read_b64_tr_b16 v[226:227], v209 offset:16384\n"
      "ds_read_b64_tr_b16 v[228:229], v208 offset:16896\n"
      "ds_read_b64_tr_b16 v[230:231], v209 offset:16896\n"
      "v_mfma_f32_32x32x16_bf16 v[96:111], v[40:43], v[184:187], v[96:111]\n"
      "ds_read_b64_tr_b16 v[232:233], v208 offset:17408\n"
      "ds_read_b64_tr_b16 v[234:235], v209 offset:17408\n"
      "ds_read_b64_tr_b16 v[236:237], v208 offset:17920\n"
      "ds_read_b64_tr_b16 v[238:239], v209 offset:17920\n"
      "v_mfma_f32_32x32x16_bf16 v[112:127], v[44:47], v[184:187], v[112:127]\n"
      "s_waitcnt lgkmcnt(8)\n"
      "ds_read_b64_tr_b16 v[240:241], v208 offset:20480\n"
      "ds_read_b64_tr_b16 v[242:243], v209 offset:20480\n"
      "ds_read_b64_tr_b16 v[244:245], v208 offset:20992\n"
      "ds_read_b64_tr_b16 v[246:247], v209 offset:20992\n"
      "v_mfma_f32_32x32x16_bf16 v[64:79], v[48:51], v[188:191], v[64:79]\n"
      "s_waitcnt lgkmcnt(8)\n"
      "ds_read_b64_tr_b16 v[248:249], v208 offset:21504\n"
      "ds_read_b64_tr_b16 v[250:251], v209 offset:21504\n"
      "ds_read_b64_tr_b16 v[252:253], v208 offset:22016\n"
      "ds_read_b64_tr_b16 v[254:255], v209 offset:22016\n"
      "v_mfma_f32_32x32x16_bf16 v[80:95], v[52:55], v[188:191], v[80:95]\n"
      "v_mfma_f32_32x32x16_bf16 v[96:111], v[56:59], v[188:191], v[96:111]\n"
      "v_mfma_f32_32x32x16_bf16 v[112:127], v[60:63], v[188:191], v[112:127]\n"
      "s_waitcnt lgkmcnt(0)\n"
      "s_waitcnt vmcnt(11)\n"
      "s_barrier\n"
      "v_mfma_f32_32x32x16_bf16 v[64:79], v[224:227], v[160:163], v[64:79]\n"
      "ds_read_b64_tr_b16 v[32:33], v208 offset:24576\n"
      "ds_read_b64_tr_b16 v[34:35], v209 offset:24576\n"
      "ds_read_b64_tr_b16 v[36:37], v208 offset:25088\n"
      "ds_read_b64_tr_b16 v[38:39], v209 offset:25088\n"
      "v_mfma_f32_32x32x16_bf16 v[80:95], v[228:231], v[160:163], v[80:95]\n"
      "ds_read_b64_tr_b16 v[40:41], v208 offset:25600\n"
      "ds_read_b64_tr_b16 v[42:43], v209 offset:25600\n"
      "ds_read_b64_tr_b16 v[44:45], v208 offset:26112\n"
      "ds_read_b64_tr_b16 v[46:47], v209 offset:26112\n"
      "v_mfma_f32_32x32x16_bf16 v[96:111], v[232:235], v[160:163], v[96:111]\n"
      "ds_read_b64_tr_b16 v[48:49], v208 offset:28672\n"
      "ds_read_b64_tr_b16 v[50:51], v209 offset:28672\n"
      "ds_read_b64_tr_b16 v[52:53], v208 offset:29184\n"
      "ds_read_b64_tr_b16 v[54:55], v209 offset:29184\n"
      "v_mfma_f32_32x32x16_bf16 v[112:127], v[236:239], v[160:163], v[112:127]\n"
      "s_waitcnt lgkmcnt(8)\n"
      "ds_read_b64_tr_b16 v[56:57], v208 offset:29696\n"
      "ds_read_b64_tr_b16 v[58:59], v209 offset:29696\n"
      "ds_read_b64_tr_b16 v[60:61], v208 offset:30208\n"
      "ds_read_b64_tr_b16 v[62:63], v209 offset:30208\n"
      "v_mfma_f32_32x32x16_bf16 v[64:79], v[240:243], v[164:167], v[64:79]\n"
      "s_waitcnt lgkmcnt(8)\n"
      "v_mfma_f32_32x32x16_bf16 v[80:95], v[244:247], v[164:167], v[80:95]\n"
      "v_mfma_f32_32x32x16_bf16 v[96:111], v[248:251], v[164:167], v[96:111]\n"
      "v_mfma_f32_32x32x16_bf16 v[112:127], v[252:255], v[164:167], v[112:127]\n"
      "s_waitcnt lgkmcnt(0)\n"
      "s_waitcnt vmcnt(6)\n"
      "s_barrier\n"
      "v_mfma_f32_32x32x16_bf16 v[64:79], v[32:35], v[168:171], v[64:79]\n"
      "ds_read_b128 v[176:179], v210 offset:12288\n"
      "ds_read_b128 v[180:183], v211 offset:12288\n"
      "ds_read_b128 v[184:187], v210 offset:12800\n"
      "ds_read_b128 v[188:191], v211 offset:12800\n"
      "v_mfma_f32_32x32x16_bf16 v[80:95], v[36:39], v[168:171], v[80:95]\n"
      "ds_read_b64_tr_b16 v[224:225], v208 offset:32768\n"
      "ds_read_b64_tr_b16 v[226:227], v209 offset:32768\n"
      "ds_read_b64_tr_b16 v[228:229], v208 offset:33280\n"
      "ds_read_b64_tr_b16 v[230:231], v209 offset:33280\n"
      "v_mfma_f32_32x32x16_bf16 v[96:111], v[40:43], v[168:171], v[96:111]\n"
      "ds_read_b64_tr_b16 v[232:233], v208 offset:33792\n"
      "ds_read_b64_tr_b16 v[234:235], v209 offset:33792\n"
      "ds_read_b64_tr_b16 v[236:237], v208 offset:34304\n"
      "ds_read_b64_tr_b16 v[238:239], v209 offset:34304\n"
      "v_mfma_f32_32x32x16_bf16 v[112:127], v[44:47], v[168:171], v[112:127]\n"
      "s_waitcnt lgkmcnt(8)\n"
      "ds_read_b64_tr_b16 v[240:241], v208 offset:36864\n"
      "ds_read_b64_tr_b16 v[242:243], v209 offset:36864\n"
      "ds_read_b64_tr_b16 v[244:245], v208 offset:37376\n"
      "ds_read_b64_tr_b16 v[246:247], v209 offset:37376\n"
      "v_mfma_f32_32x32x16_bf16 v[64:79], v[48:51], v[172:175], v[64:79]\n"
      "s_waitcnt lgkmcnt(8)\n"
      "ds_read_b64_tr_b16 v[248:249], v208 offset:37888\n"
      "ds_read_b64_tr_b16 v[250:251], v209 offset:37888\n"
      "ds_read_b64_tr_b16 v[252:253], v208 offset:38400\n"
      "ds_read_b64_tr_b16 v[254:255], v209 offset:38400\n"
      "v_mfma_f32_32x32x16_bf16 v[80:95], v[52:55], v[172:175], v[80:95]\n"
      "v_mfma_f32_32x32x16_bf16 v[96:111], v[56:59], v[172:175], v[96:111]\n"
      "v_mfma_f32_32x32x16_bf16 v[112:127], v[60:63], v[172:175], v[112:127]\n"
      "s_waitcnt lgkmcnt(0)\n"
      "s_waitcnt vmcnt(4)\n"
      "s_barrier\n"
      "v_mfma_f32_32x32x16_bf16 v[64:79], v[224:227], v[176:179], v[64:79]\n"
      "ds_read_b64_tr_b16 v[32:33], v208 offset:0\n"
      "ds_read_b64_tr_b16 v[34:35], v209 offset:0\n"
      "ds_read_b64_tr_b16 v[36:37], v208 offset:512\n"
      "ds_read_b64_tr_b16 v[38:39], v209 offset:512\n"
      "v_mfma_f32_32x32x16_bf16 v[80:95], v[228:231], v[176:179], v[80:95]\n"
      "ds_read_b64_tr_b16 v[40:41], v208 offset:1024\n"
      "ds_read_b64_tr_b16 v[42:43], v209 offset:1024\n"
      "ds_read_b64_tr_b16 v[44:45], v208 offset:1536\n"
      "ds_read_b64_tr_b16 v[46:47], v209 offset:1536\n"
      "v_mfma_f32_32x32x16_bf16 v[96:111], v[232:235], v[176:179], v[96:111]\n"
      "ds_read_b64_tr_b16 v[48:49], v208 offset:4096\n"
      "ds_read_b64_tr_b16 v[50:51], v209 offset:4096\n"
      "ds_read_b64_tr_b16 v[52:53], v208 offset:4608\n"
      "ds_read_b64_tr_b16 v[54:55], v209 offset:4608\n"
      "v_mfma_f32_32x32x16_bf16 v[112:127], v[236:239], v[176:179], v[112:127]\n"
      "s_waitcnt lgkmcnt(8)\n"
      "ds_read_b64_tr_b16 v[56:57], v208 offset:5120\n"
      "ds_read_b64_tr_b16 v[58:59], v209 offset:5120\n"
      "ds_read_b64_tr_b16 v[60:61], v208 offset:5632\n"
      "ds_read_b64_tr_b16 v[62:63], v209 offset:5632\n"
      "v_mfma_f32_32x32x16_bf16 v[64:79], v[240:243], v[180:183], v[64:79]\n"
      "s_waitcnt lgkmcnt(8)\n"
      "v_mfma_f32_32x32x16_bf16 v[80:95], v[244:247], v[180:183], v[80:95]\n"
      "v_mfma_f32_32x32x16_bf16 v[96:111], v[248:251], v[180:183], v[96:111]\n"
      "v_mfma_f32_32x32x16_bf16 v[112:127], v[252:255], v[180:183], v[112:127]\n"
      "s_waitcnt lgkmcnt(0)\n"
      "ds_read_b128 v[224:227], v10 offset:0\n"
      "ds_read_b128 v[228:231], v11 offset:0\n"
      "ds_read_b128 v[232:235], v10 offset:512\n"
      "ds_read_b128 v[236:239], v11 offset:512\n"
      "ds_read_b128 v[240:243], v10 offset:1024\n"
      "ds_read_b128 v[244:247], v11 offset:1024\n"
      "ds_read_b128 v[248:251], v10 offset:1536\n"
      "ds_read_b128 v[252:255], v11 offset:1536\n"
      "v_mfma_f32_32x32x16_bf16 v[64:79], v[32:35], v[184:187], v[64:79]\n"
      "v_mfma_f32_32x32x16_bf16 v[64:79], v[48:51], v[188:191], v[64:79]\n"
      "s_waitcnt lgkmcnt(0)\n"
      "s_barrier\n"
      "v_mfma_f32_32x32x16_bf16 v[80:95], v[36:39], v[184:187], v[80:95]\n"
      "v_mfma_f32_32x32x16_bf16 v[80:95], v[52:55], v[188:191], v[80:95]\n"
      "v_mfma_f32_32x32x16_bf16 v[96:111], v[40:43], v[184:187], v[96:111]\n"
      "s_nop 6\n"
      "v_pk_mul_f32 v[64:65], v[64:65], s[72:73]\n"
      "v_pk_mul_f32 v[66:67], v[66:67], s[72:73]\n"
      "v_pk_mul_f32 v[68:69], v[68:69], s[72:73]\n"
      "v_pk_mul_f32 v[70:71], v[70:71], s[72:73]\n"
      "v_pk_mul_f32 v[72:73], v[72:73], s[72:73]\n"
      "v_mfma_f32_32x32x16_bf16 v[96:111], v[56:59], v[188:191], v[96:111]\n"
      "v_pk_mul_f32 v[74:75], v[74:75], s[72:73]\n"
      "v_pk_mul_f32 v[76:77], v[76:77], s[72:73]\n"
      "v_pk_mul_f32 v[78:79], v[78:79], s[72:73]\n"
      "v_cvt_pk_bf16_f32 v128, v64, v65\n"
      "v_cvt_pk_bf16_f32 v129, v66, v67\n"
      "v_cvt_pk_bf16_f32 v130, v68, v69\n"
      "v_mfma_f32_32x32x16_bf16 v[112:127], v[44:47], v[184:187], v[112:127]\n"
      "v_cvt_pk_bf16_f32 v131, v70, v71\n"
      "v_cvt_pk_bf16_f32 v132, v72, v73\n"
      "v_cvt_pk_bf16_f32 v133, v74, v75\n"
      "v_cvt_pk_bf16_f32 v134, v76, v77\n"
      "v_cvt_pk_bf16_f32 v135, v78, v79\n"
      "v_mfma_f32_32x32x16_bf16 v[112:127], v[60:63], v[188:191], v[112:127]\n"
      "v_pk_mul_f32 v[80:81], v[80:81], s[72:73]\n"
      "v_pk_mul_f32 v[82:83], v[82:83], s[72:73]\n"
      "v_pk_mul_f32 v[84:85], v[84:85], s[72:73]\n"
      "v_pk_mul_f32 v[86:87], v[86:87], s[72:73]\n"
      "v_pk_mul_f32 v[88:89], v[88:89], s[72:73]\n"
      "v_pk_mul_f32 v[90:91], v[90:91], s[72:73]\n"
      "v_pk_mul_f32 v[92:93], v[92:93], s[72:73]\n"
      "v_pk_mul_f32 v[94:95], v[94:95], s[72:73]\n"
      "v_mfma_f32_32x32x16_bf16 v[160:175], v[224:227], v[128:131], 0\n"
      "v_cvt_pk_bf16_f32 v136, v80, v81\n"
      "v_cvt_pk_bf16_f32 v137, v82, v83\n"
      "v_cvt_pk_bf16_f32 v138, v84, v85\n"
      "v_cvt_pk_bf16_f32 v139, v86, v87\n"
      "v_cvt_pk_bf16_f32 v140, v88, v89\n"
      "v_cvt_pk_bf16_f32 v141, v90, v91\n"
      "v_cvt_pk_bf16_f32 v142, v92, v93\n"
      "v_cvt_pk_bf16_f32 v143, v94, v95\n"
      "v_mfma_f32_32x32x16_bf16 v[160:175], v[228:231], v[132:135], v[160:175]\n"
      "v_pk_mul_f32 v[96:97], v[96:97], s[72:73]\n"
      "v_pk_mul_f32 v[98:99], v[98:99], s[72:73]\n"
      "v_pk_mul_f32 v[100:101], v[100:101], s[72:73]\n"
      "v_pk_mul_f32 v[102:103], v[102:103], s[72:73]\n"
      "v_pk_mul_f32 v[104:105], v[104:105], s[72:73]\n"
      "v_pk_mul_f32 v[106:107], v[106:107], s[72:73]\n"
      "v_pk_mul_f32 v[108:109], v[108:109], s[72:73]\n"
      "v_pk_mul_f32 v[110:111], v[110:111], s[72:73]\n"
      "v_mfma_f32_32x32x16_bf16 v[160:175], v[232:235], v[136:139], v[160:175]\n"
      "v_cvt_pk_bf16_f32 v144, v96, v97\n"
      "v_cvt_pk_bf16_f32 v145, v98, v99\n"
      "v_cvt_pk_bf16_f32 v146, v100, v101\n"
      "v_cvt_pk_bf16_f32 v147, v102, v103\n"
      "v_cvt_pk_bf16_f32 v148, v104, v105\n"
      "v_cvt_pk_bf16_f32 v149, v106, v107\n"
      "v_cvt_pk_bf16_f32 v150, v108, v109\n"
      "v_cvt_pk_bf16_f32 v151, v110, v111\n"
      "v_mfma_f32_32x32x16_bf16 v[160:175], v[236:239], v[140:143], v[160:175]\n"
      "v_pk_mul_f32 v[112:113], v[112:113], s[72:73]\n"
      "v_pk_mul_f32 v[114:115], v[114:115], s[72:73]\n"
      "v_pk_mul_f32 v[116:117], v[116:117], s[72:73]\n"
      "v_pk_mul_f32 v[118:119], v[118:119], s[72:73]\n"
      "v_pk_mul_f32 v[120:121], v[120:121], s[72:73]\n"
      "v_pk_mul_f32 v[122:123], v[122:123], s[72:73]\n"
      "v_pk_mul_f32 v[124:125], v[124:125], s[72:73]\n"
      "v_pk_mul_f32 v[126:127], v[126:127], s[72:73]\n"
      "v_mfma_f32_32x32x16_bf16 v[160:175], v[240:243], v[144:147], v[160:175]\n"
      "v_cvt_pk_bf16_f32 v152, v112, v113\n"
      "v_cvt_pk_bf16_f32 v153, v114, v115\n"
      "v_cvt_pk_bf16_f32 v154, v116, v117\n"
      "v_cvt_pk_bf16_f32 v155, v118, v119\n"
      "v_cvt_pk_bf16_f32 v156, v120, v121\n"
      "v_cvt_pk_bf16_f32 v157, v122, v123\n"
      "v_cvt_pk_bf16_f32 v158, v124, v125\n"
      "v_cvt_pk_bf16_f32 v159, v126, v127\n"
      "v_mfma_f32_32x32x16_bf16 v[160:175], v[244:247], v[148:151], v[160:175]\n"
      "s_nop 0\n"
      "v_mfma_f32_32x32x16_bf16 v[160:175], v[248:251], v[152:155], v[160:175]\n"
      "v_mfma_f32_32x32x16_bf16 v[160:175], v[252:255], v[156:159], v[160:175]\n"
      "v_mov_b32 v16, 0\n"
      "s_nop 10\n"
      "v_max3_f32 v2, v160, v161, v162\n"
      "v_max3_f32 v3, v163, v164, v165\n"
      "v_max3_f32 v2, v2, v166, v167\n"
      "v_max3_f32 v3, v3, v168, v169\n"
      "v_max3_f32 v2, v2, v170, v171\n"
      "v_max3_f32 v3, v3, v172, v173\n"
      "v_max3_f32 v2, v2, v174, v175\n"
      "v_max_f32 v2, v2, v3\n"
      "v_mov_b32 v3, v2\n"
      "s_nop 1\n"
      "v_permlane32_swap_b32 v2, v3\n"
      "v_max_f32 v2, v2, v3\n"
      "v_sub_f32 v208, 0, v2\n"
      "v_sub_f32 v209, 0, v2\n"
      "v_sub_f32 v210, 0, v2\n"
      "v_sub_f32 v211, 0, v2\n"
      "v_sub_f32 v212, 0, v2\n"
      "v_sub_f32 v213, 0, v2\n"
      "v_sub_f32 v214, 0, v2\n"
      "v_sub_f32 v215, 0, v2\n"
      "v_sub_f32 v216, 0, v2\n"
      "v_sub_f32 v217, 0, v2\n"
      "v_sub_f32 v218, 0, v2\n"
      "v_sub_f32 v219, 0, v2\n"
      "v_sub_f32 v220, 0, v2\n"
      "v_sub_f32 v221, 0, v2\n"
      "v_sub_f32 v222, 0, v2\n"
      "v_sub_f32 v223, 0, v2\n"
      "v_sub_f32 v160, v160, v2\n"
      "v_sub_f32 v161, v161, v2\n"
      "v_sub_f32 v162, v162, v2\n"
      "v_sub_f32 v163, v163, v2\n"
      "v_sub_f32 v164, v164, v2\n"
      "v_sub_f32 v165, v165, v2\n"
      "v_sub_f32 v166, v166, v2\n"
      "v_sub_f32 v167, v167, v2\n"
      "v_sub_f32 v168, v168, v2\n"
      "v_sub_f32 v169, v169, v2\n"
      "v_sub_f32 v170, v170, v2\n"
      "v_sub_f32 v171, v171, v2\n"
      "v_sub_f32 v172, v172, v2\n"
      "v_sub_f32 v173, v173, v2\n"
      "v_sub_f32 v174, v174, v2\n"
      "v_sub_f32 v175, v175, v2\n"
      "s_waitcnt vmcnt(56)\n"
      "s_barrier\n"
      "ds_read_b128 v[224:227], v10 offset:8192\n"
      "ds_read_b128 v[228:231], v11 offset:8192\n"
      "ds_read_b128 v[232:235], v10 offset:8704\n"
      "ds_read_b128 v[236:239], v11 offset:8704\n"
      "v_exp_f32 v192, v160\n"
      "ds_read_b128 v[240:243], v10 offset:9216\n"
      "v_exp_f32 v193, v161\n"
      "ds_read_b128 v[244:247], v11 offset:9216\n"
      "v_exp_f32 v194, v162\n"
      "ds_read_b128 v[248:251], v10 offset:9728\n"
      "v_exp_f32 v195, v163\n"
      "ds_read_b128 v[252:255], v11 offset:9728\n"
      "ds_read_b64_tr_b16 a[0:1], v14 offset:8192\n"
      "v_exp_f32 v196, v164\n"
      "ds_read_b64_tr_b16 a[4:5], v14 offset:8200\n"
      "v_add_f32 v17, v192, v194\n"
      "v_add_f32 v18, v193, v195\n"
      "ds_read_b64_tr_b16 a[8:9], v15 offset:8192\n"
      "v_exp_f32 v197, v165\n"
      "ds_read_b64_tr_b16 a[12:13], v15 offset:8200\n"
      "v_add_f32 v17, v17, v196\n"
      "v_cvt_pk_bf16_f32 v24, v192, v193\n"
      "s_waitcnt lgkmcnt(8)\n"
      "ds_read_b64_tr_b16 a[2:3], v14 offset:16384\n"
      "v_exp_f32 v198, v166\n"
      "ds_read_b64_tr_b16 a[6:7], v14 offset:16392\n"
      "v_add_f32 v18, v18, v197\n"
      "v_exp_f32 v199, v167\n"
      "v_cvt_pk_bf16_f32 v25, v194, v195\n"
      "ds_read_b64_tr_b16 a[10:11], v15 offset:16384\n"
      "v_exp_f32 v200, v168\n"
      "ds_read_b64_tr_b16 a[14:15], v15 offset:16392\n"
      "v_add_f32 v17, v17, v198\n"
      "v_exp_f32 v201, v169\n"
      "v_cvt_pk_bf16_f32 v26, v196, v197\n"
      "v_add_f32 v18, v18, v199\n"
      "v_cvt_pk_bf16_f32 v27, v198, v199\n"
      "s_waitcnt lgkmcnt(0)\n"
      "v_mfma_f32_32x32x16_bf16 v[176:191], v[224:227], v[128:131], v[208:223]\n"
      "ds_read_b64_tr_b16 v[32:33], v12 offset:0\n"
      "ds_read_b64_tr_b16 v[34:35], v13 offset:2048\n"
      "v_exp_f32 v202, v170\n"
      "ds_read_b64_tr_b16 v[36:37], v12 offset:512\n"
      "v_add_f32 v17, v17, v200\n"
      "v_mfma_f32_32x32x16_bf16 v[176:191], v[228:231], v[132:135], v[176:191]\n"
      "ds_read_b64_tr_b16 v[38:39], v13 offset:2560\n"
      "ds_read_b64_tr_b16 v[40:41], v12 offset:1024\n"
      "v_exp_f32 v203, v171\n"
      "v_add_f32 v18, v18, v201\n"
      "v_mfma_f32_32x32x16_bf16 v[176:191], v[232:235], v[136:139], v[176:191]\n"
      "ds_read_b64_tr_b16 v[42:43], v13 offset:3072\n"
      "ds_read_b64_tr_b16 v[44:45], v12 offset:1536\n"
      "v_exp_f32 v204, v172\n"
      "ds_read_b64_tr_b16 v[46:47], v13 offset:3584\n"
      "v_add_f32 v17, v17, v202\n"
      "v_mfma_f32_32x32x16_bf16 v[176:191], v[236:239], v[140:143], v[176:191]\n"
      "ds_read_b64_tr_b16 v[48:49], v12 offset:4096\n"
      "ds_read_b64_tr_b16 v[50:51], v13 offset:6144\n"
      "v_exp_f32 v205, v173\n"
      "v_add_f32 v18, v18, v203\n"
      "v_mfma_f32_32x32x16_bf16 v[176:191], v[240:243], v[144:147], v[176:191]\n"
      "ds_read_b64_tr_b16 v[52:53], v12 offset:4608\n"
      "ds_read_b64_tr_b16 v[54:55], v13 offset:6656\n"
      "v_exp_f32 v206, v174\n"
      "v_add_f32 v17, v17, v204\n"
      "v_cvt_pk_bf16_f32 v28, v200, v201\n"
      "v_mfma_f32_32x32x16_bf16 v[176:191], v[244:247], v[148:151], v[176:191]\n"
      "s_waitcnt lgkmcnt(8)\n"
      "ds_read_b64_tr_b16 v[56:57], v12 offset:5120\n"
      "ds_read_b64_tr_b16 v[58:59], v13 offset:7168\n"
      "v_exp_f32 v207, v175\n"
      "v_add_f32 v18, v18, v205\n"
      "v_mfma_f32_32x32x16_bf16 v[176:191], v[248:251], v[152:155], v[176:191]\n"
      "ds_read_b64_tr_b16 v[60:61], v12 offset:5632\n"
      "ds_read_b64_tr_b16 v[62:63], v13 offset:7680\n"
      "v_add_f32 v17, v17, v206\n"
      "v_cvt_pk_bf16_f32 v29, v202, v203\n"
      "v_add_f32 v18, v18, v207\n"
      "v_cvt_pk_bf16_f32 v30, v204, v205\n"
      "v_mfma_f32_32x32x16_bf16 v[176:191], v[252:255], v[156:159], v[176:191]\n"
      "v_cvt_pk_bf16_f32 v31, v206, v207\n"
      "v_add_f32 v17, v17, v18\n"
      "s_nop 0\n"
      "v_cmp_lt_f32 vcc, s67, v17\n"
      "s_cbranch_vccz .Lok0_%=\n"
      "s_swappc_b64 s[62:63], s[64:65]\n"
      ".Lok0_%=:\n"
      "v_add_f32 v16, v16, v17\n"
      "s_waitcnt lgkmcnt(0)\n"
      "s_waitcnt vmcnt(38)\n"
      "s_barrier\n"
      "v_mfma_f32_32x32x16_bf16 v[64:79], v[32:35], v[24:27], 0\n"
      "ds_read_b128 v[224:227], v10 offset:16384\n"
      "ds_read_b128 v[228:231], v11 offset:16384\n"
      "ds_read_b128 v[232:235], v10 offset:16896\n"
      "ds_read_b128 v[236:239], v11 offset:16896\n"
      "v_mfma_f32_32x32x16_bf16 v[80:95], v[36:39], v[24:27], 0\n"
      "v_exp_f32 v192, v176\n"
      "ds_read_b128 v[240:243], v10 offset:17408\n"
      "v_exp_f32 v193, v177\n"
      "ds_read_b128 v[244:247], v11 offset:17408\n"
      "v_mfma_f32_32x32x16_bf16 v[96:111], v[40:43], v[24:27], 0\n"
      "v_exp_f32 v194, v178\n"
      "ds_read_b128 v[248:251], v10 offset:17920\n"
      "v_exp_f32 v195, v179\n"
      "ds_read_b128 v[252:255], v11 offset:17920\n"
      "v_mfma_f32_32x32x16_bf16 v[112:127], v[44:47], v[24:27], 0\n"
      "ds_read_b64_tr_b16 a[16:17], v14 offset:40960\n"
      "v_exp_f32 v196, v180\n"
      "ds_read_b64_tr_b16 a[20:21], v14 offset:40968\n"
      "v_add_f32 v17, v192, v194\n"
      "v_add_f32 v18, v193, v195\n"
      "v_mfma_f32_32x32x16_bf16 v[64:79], v[48:51], v[28:31], v[64:79]\n"
      "ds_read_b64_tr_b16 a[24:25], v15 offset:40960\n"
      "v_exp_f32 v197, v181\n"
      "ds_read_b64_tr_b16 a[28:29], v15 offset:40968\n"
      "v_add_f32 v17, v17, v196\n"
      "v_cvt_pk_bf16_f32 v24, v192, v193\n"
      "v_mfma_f32_32x32x16_bf16 v[80:95], v[52:55], v[28:31], v[80:95]\n"
      "v_exp_f32 v198, v182\n"
      "v_add_f32 v18, v18, v197\n"
      "v_exp_f32 v199, v183\n"
      "v_cvt_pk_bf16_f32 v25, v194, v195\n"
      "v_mfma_f32_32x32x16_bf16 v[96:111], v[56:59], v[28:31], v[96:111]\n"
      "v_exp_f32 v200, v184\n"
      "v_add_f32 v17, v17, v198\n"
      "v_exp_f32 v201, v185\n"
      "v_cvt_pk_bf16_f32 v26, v196, v197\n"
      "v_mfma_f32_32x32x16_bf16 v[112:127], v[60:63], v[28:31], v[112:127]\n"
      "v_add_f32 v18, v18, v199\n"
      "v_cvt_pk_bf16_f32 v27, v198, v199\n"
      "s_waitcnt lgkmcnt(0)\n"
      "v_mfma_f32_32x32x16_bf16 v[160:175], v[224:227], v[128:131], v[208:223]\n"
      "ds_read_b64_tr_b16 v[32:33], v12 offset:8192\n"
      "ds_read_b64_tr_b16 v[34:35], v13 offset:10240\n"
      "v_exp_f32 v202, v186\n"
      "ds_read_b64_tr_b16 v[36:37], v12 offset:8704\n"
      "v_add_f32 v17, v17, v200\n"
      "v_mfma_f32_32x32x16_bf16 v[160:175], v[228:231], v[132:135], v[160:175]\n"
      "ds_read_b64_tr_b16 v[38:39], v13 offset:10752\n"
      "ds_read_b64_tr_b16 v[40:41], v12 offset:9216\n"
      "v_exp_f32 v203, v187\n"
      "v_add_f32 v18, v18, v201\n"
      "v_mfma_f32_32x32x16_bf16 v[160:175], v[232:235], v[136:139], v[160:175]\n"
      "ds_read_b64_tr_b16 v[42:43], v13 offset:11264\n"
      "ds_read_b64_tr_b16 v[44:45], v12 offset:9728\n"
      "v_exp_f32 v204, v188\n"
      "ds_read_b64_tr_b16 v[46:47], v13 offset:11776\n"
      "v_add_f32 v17, v17, v202\n"
      "v_mfma_f32_32x32x16_bf16 v[160:175], v[236:239], v[140:143], v[160:175]\n"
      "ds_read_b64_tr_b16 v[48:49], v12 offset:12288\n"
      "ds_read_b64_tr_b16 v[50:51], v13 offset:14336\n"
      "v_exp_f32 v205, v189\n"
      "v_add_f32 v18, v18, v203\n"
      "v_mfma_f32_32x32x16_bf16 v[160:175], v[240:243], v[144:147], v[160:175]\n"
      "ds_read_b64_tr_b16 v[52:53], v12 offset:12800\n"
      "ds_read_b64_tr_b16 v[54:55], v13 offset:14848\n"
      "v_exp_f32 v206, v190\n"
      "v_add_f32 v17, v17, v204\n"
      "v_cvt_pk_bf16_f32 v28, v200, v201\n"
      "v_mfma_f32_32x32x16_bf16 v[160:175], v[244:247], v[148:151], v[160:175]\n"
      "s_waitcnt lgkmcnt(8)\n"
      "ds_read_b64_tr_b16 v[56:57], v12 offset:13312\n"
      "ds_read_b64_tr_b16 v[58:59], v13 offset:15360\n"
      "v_exp_f32 v207, v191\n"
      "v_add_f32 v18, v18, v205\n"
      "v_mfma_f32_32x32x16_bf16 v[160:175], v[248:251], v[152:155], v[160:175]\n"
      "ds_read_b64_tr_b16 v[60:61], v12 offset:13824\n"
      "ds_read_b64_tr_b16 v[62:63], v13 offset:15872\n"
      "v_add_f32 v17, v17, v206\n"
      "v_cvt_pk_bf16_f32 v29, v202, v203\n"
      "v_add_f32 v18, v18, v207\n"
      "v_cvt_pk_bf16_f32 v30, v204, v205\n"
      "v_mfma_f32_32x32x16_bf16 v[160:175], v[252:255], v[156:159], v[160:175]\n"
      "v_cvt_pk_bf16_f32 v31, v206, v207\n"
      "v_add_f32 v17, v17, v18\n"
      "s_nop 0\n"
      "v_cmp_lt_f32 vcc, s67, v17\n"
      "s_cbranch_vccz .Lok1_%=\n"
      "s_swappc_b64 s[62:63], s[92:93]\n"
      ".Lok1_%=:\n"
      "v_add_f32 v16, v16, v17\n"
      "s_waitcnt lgkmcnt(0)\n"
      "s_waitcnt vmcnt(34)\n"
      "s_barrier\n"
      "v_mfma_f32_32x32x16_bf16 v[64:79], v[32:35], v[24:27], v[64:79]\n"
      "ds_read_b128 v[224:227], v10 offset:24576\n"
      "ds_read_b128 v[228:231], v11 offset:24576\n"
      "ds_read_b128 v[232:235], v10 offset:25088\n"
      "ds_read_b128 v[236:239], v11 offset:25088\n"
      "v_mfma_f32_32x32x16_bf16 v[80:95], v[36:39], v[24:27], v[80:95]\n"
      "v_exp_f32 v192, v160\n"
      "ds_read_b128 v[240:243], v10 offset:25600\n"
      "v_exp_f32 v193, v161\n"
      "ds_read_b128 v[244:247], v11 offset:25600\n"
      "v_mfma_f32_32x32x16_bf16 v[96:111], v[40:43], v[24:27], v[96:111]\n"
      "v_exp_f32 v194, v162\n"
      "ds_read_b128 v[248:251], v10 offset:26112\n"
      "v_exp_f32 v195, v163\n"
      "ds_read_b128 v[252:255], v11 offset:26112\n"
      "v_mfma_f32_32x32x16_bf16 v[112:127], v[44:47], v[24:27], v[112:127]\n"
      "ds_read_b64_tr_b16 a[18:19], v14 offset:24576\n"
      "v_exp_f32 v196, v164\n"
      "ds_read_b64_tr_b16 a[22:23], v14 offset:24584\n"
      "v_add_f32 v17, v192, v194\n"
      "v_add_f32 v18, v193, v195\n"
      "v_mfma_f32_32x32x16_bf16 v[64:79], v[48:51], v[28:31], v[64:79]\n"
      "ds_read_b64_tr_b16 a[26:27], v15 offset:24576\n"
      "v_exp_f32 v197, v165\n"
      "ds_read_b64_tr_b16 a[30:31], v15 offset:24584\n"
      "v_add_f32 v17, v17, v196\n"
      "v_cvt_pk_bf16_f32 v24, v192, v193\n"
      "v_mfma_f32_32x32x16_bf16 v[80:95], v[52:55], v[28:31], v[80:95]\n"
      "v_exp_f32 v198, v166\n"
      "v_add_f32 v18, v18, v197\n"
      "v_exp_f32 v199, v167\n"
      "v_cvt_pk_bf16_f32 v25, v194, v195\n"
      "v_mfma_f32_32x32x16_bf16 v[96:111], v[56:59], v[28:31], v[96:111]\n"
      "v_exp_f32 v200, v168\n"
      "v_add_f32 v17, v17, v198\n"
      "v_exp_f32 v201, v169\n"
      "v_cvt_pk_bf16_f32 v26, v196, v197\n"
      "v_mfma_f32_32x32x16_bf16 v[112:127], v[60:63], v[28:31], v[112:127]\n"
      "v_add_f32 v18, v18, v199\n"
      "v_cvt_pk_bf16_f32 v27, v198, v199\n"
      "s_waitcnt lgkmcnt(0)\n"
      "v_mfma_f32_32x32x16_bf16 v[176:191], v[224:227], v[128:131], v[208:223]\n"
      "ds_read_b64_tr_b16 v[32:33], v12 offset:16384\n"
      "ds_read_b64_tr_b16 v[34:35], v13 offset:18432\n"
      "v_exp_f32 v202, v170\n"
      "ds_read_b64_tr_b16 v[36:37], v12 offset:16896\n"
      "v_add_f32 v17, v17, v200\n"
      "v_mfma_f32_32x32x16_bf16 v[176:191], v[228:231], v[132:135], v[176:191]\n"
      "ds_read_b64_tr_b16 v[38:39], v13 offset:18944\n"
      "ds_read_b64_tr_b16 v[40:41], v12 offset:17408\n"
      "v_exp_f32 v203, v171\n"
      "v_add_f32 v18, v18, v201\n"
      "v_mfma_f32_32x32x16_bf16 v[176:191], v[232:235], v[136:139], v[176:191]\n"
      "ds_read_b64_tr_b16 v[42:43], v13 offset:19456\n"
      "ds_read_b64_tr_b16 v[44:45], v12 offset:17920\n"
      "v_exp_f32 v204, v172\n"
      "ds_read_b64_tr_b16 v[46:47], v13 offset:19968\n"
      "v_add_f32 v17, v17, v202\n"
      "v_mfma_f32_32x32x16_bf16 v[176:191], v[236:239], v[140:143], v[176:191]\n"
      "ds_read_b64_tr_b16 v[48:49], v12 offset:20480\n"
      "ds_read_b64_tr_b16 v[50:51], v13 offset:22528\n"
      "v_exp_f32 v205, v173\n"
      "v_add_f32 v18, v18, v203\n"
      "v_mfma_f32_32x32x16_bf16 v[176:191], v[240:243], v[144:147], v[176:191]\n"
      "ds_read_b64_tr_b16 v[52:53], v12 offset:20992\n"
      "ds_read_b64_tr_b16 v[54:55], v13 offset:23040\n"
      "v_exp_f32 v206, v174\n"
      "v_add_f32 v17, v17, v204\n"
      "v_cvt_pk_bf16_f32 v28, v200, v201\n"
      "v_mfma_f32_32x32x16_bf16 v[176:191], v[244:247], v[148:151], v[176:191]\n"
      "s_waitcnt lgkmcnt(8)\n"
      "ds_read_b64_tr_b16 v[56:57], v12 offset:21504\n"
      "ds_read_b64_tr_b16 v[58:59], v13 offset:23552\n"
      "v_exp_f32 v207, v175\n"
      "v_add_f32 v18, v18, v205\n"
      "v_mfma_f32_32x32x16_bf16 v[176:191], v[248:251], v[152:155], v[176:191]\n"
      "ds_read_b64_tr_b16 v[60:61], v12 offset:22016\n"
      "ds_read_b64_tr_b16 v[62:63], v13 offset:24064\n"
      "v_add_f32 v17, v17, v206\n"
      "v_cvt_pk_bf16_f32 v29, v202, v203\n"
      "v_add_f32 v18, v18, v207\n"
      "v_cvt_pk_bf16_f32 v30, v204, v205\n"
      "v_mfma_f32_32x32x16_bf16 v[176:191], v[252:255], v[156:159], v[176:191]\n"
      "v_cvt_pk_bf16_f32 v31, v206, v207\n"
      "v_add_f32 v17, v17, v18\n"
      "s_nop 0\n"
      "v_cmp_lt_f32 vcc, s67, v17\n"
      "s_cbranch_vccz .Lok2_%=\n"
      "s_swappc_b64 s[62:63], s[64:65]\n"
      ".Lok2_%=:\n"
      "v_add_f32 v16, v16, v17\n"
      "s_waitcnt lgkmcnt(0)\n"
      "s_waitcnt vmcnt(16)\n"
      "s_barrier\n"
      "v_mfma_f32_32x32x16_bf16 v[64:79], v[32:35], v[24:27], v[64:79]\n"
      "ds_read_b128 v[224:227], v10 offset:32768\n"
      "ds_read_b128 v[228:231], v11 offset:32768\n"
      "ds_read_b128 v[232:235], v10 offset:33280\n"
      "ds_read_b128 v[236:239], v11 offset:33280\n"
      "v_mfma_f32_32x32x16_bf16 v[80:95], v[36:39], v[24:27], v[80:95]\n"
      "v_exp_f32 v192, v176\n"
      "ds_read_b128 v[240:243], v10 offset:33792\n"
      "v_exp_f32 v193, v177\n"
      "ds_read_b128 v[244:247], v11 offset:33792\n"
      "v_mfma_f32_32x32x16_bf16 v[96:111], v[40:43], v[24:27], v[96:111]\n"
      "v_exp_f32 v194, v178\n"
      "ds_read_b128 v[248:251], v10 offset:34304\n"
      "v_exp_f32 v195, v179\n"
      "ds_read_b128 v[252:255], v11 offset:34304\n"
      "v_mfma_f32_32x32x16_bf16 v[112:127], v[44:47], v[24:27], v[112:127]\n"
      "ds_read_b64_tr_b16 a[32:33], v14 offset:32768\n"
      "v_exp_f32 v196, v180\n"
      "ds_read_b64_tr_b16 a[36:37], v14 offset:32776\n"
      "v_add_f32 v17, v192, v194\n"
      "v_add_f32 v18, v193, v195\n"
      "v_mfma_f32_32x32x16_bf16 v[64:79], v[48:51], v[28:31], v[64:79]\n"
      "ds_read_b64_tr_b16 a[40:41], v15 offset:32768\n"
      "v_exp_f32 v197, v181\n"
      "ds_read_b64_tr_b16 a[44:45], v15 offset:32776\n"
      "v_add_f32 v17, v17, v196\n"
      "v_cvt_pk_bf16_f32 v24, v192, v193\n"
      "v_mfma_f32_32x32x16_bf16 v[80:95], v[52:55], v[28:31], v[80:95]\n"
      "v_exp_f32 v198, v182\n"
      "v_add_f32 v18, v18, v197\n"
      "v_exp_f32 v199, v183\n"
      "v_cvt_pk_bf16_f32 v25, v194, v195\n"
      "v_mfma_f32_32x32x16_bf16 v[96:111], v[56:59], v[28:31], v[96:111]\n"
      "v_exp_f32 v200, v184\n"
      "v_add_f32 v17, v17, v198\n"
      "v_exp_f32 v201, v185\n"
      "v_cvt_pk_bf16_f32 v26, v196, v197\n"
      "v_mfma_f32_32x32x16_bf16 v[112:127], v[60:63], v[28:31], v[112:127]\n"
      "v_add_f32 v18, v18, v199\n"
      "v_cvt_pk_bf16_f32 v27, v198, v199\n"
      "s_waitcnt lgkmcnt(0)\n"
      "v_mfma_f32_32x32x16_bf16 v[160:175], v[224:227], v[128:131], v[208:223]\n"
      "ds_read_b64_tr_b16 v[32:33], v12 offset:24576\n"
      "ds_read_b64_tr_b16 v[34:35], v13 offset:26624\n"
      "v_exp_f32 v202, v186\n"
      "ds_read_b64_tr_b16 v[36:37], v12 offset:25088\n"
      "v_add_f32 v17, v17, v200\n"
      "v_mfma_f32_32x32x16_bf16 v[160:175], v[228:231], v[132:135], v[160:175]\n"
      "ds_read_b64_tr_b16 v[38:39], v13 offset:27136\n"
      "ds_read_b64_tr_b16 v[40:41], v12 offset:25600\n"
      "v_exp_f32 v203, v187\n"
      "v_add_f32 v18, v18, v201\n"
      "v_mfma_f32_32x32x16_bf16 v[160:175], v[232:235], v[136:139], v[160:175]\n"
      "ds_read_b64_tr_b16 v[42:43], v13 offset:27648\n"
      "ds_read_b64_tr_b16 v[44:45], v12 offset:26112\n"
      "v_exp_f32 v204, v188\n"
      "ds_read_b64_tr_b16 v[46:47], v13 offset:28160\n"
      "v_add_f32 v17, v17, v202\n"
      "v_mfma_f32_32x32x16_bf16 v[160:175], v[236:239], v[140:143], v[160:175]\n"
      "ds_read_b64_tr_b16 v[48:49], v12 offset:28672\n"
      "ds_read_b64_tr_b16 v[50:51], v13 offset:30720\n"
      "v_exp_f32 v205, v189\n"
      "v_add_f32 v18, v18, v203\n"
      "v_mfma_f32_32x32x16_bf16 v[160:175], v[240:243], v[144:147], v[160:175]\n"
      "ds_read_b64_tr_b16 v[52:53], v12 offset:29184\n"
      "ds_read_b64_tr_b16 v[54:55], v13 offset:31232\n"
      "v_exp_f32 v206, v190\n"
      "v_add_f32 v17, v17, v204\n"
      "v_cvt_pk_bf16_f32 v28, v200, v201\n"
      "v_mfma_f32_32x32x16_bf16 v[160:175], v[244:247], v[148:151], v[160:175]\n"
      "s_waitcnt lgkmcnt(8)\n"
      "ds_read_b64_tr_b16 v[56:57], v12 offset:29696\n"
      "ds_read_b64_tr_b16 v[58:59], v13 offset:31744\n"
      "v_exp_f32 v207, v191\n"
      "v_add_f32 v18, v18, v205\n"
      "v_mfma_f32_32x32x16_bf16 v[160:175], v[248:251], v[152:155], v[160:175]\n"
      "ds_read_b64_tr_b16 v[60:61], v12 offset:30208\n"
      "ds_read_b64_tr_b16 v[62:63], v13 offset:32256\n"
      "v_add_f32 v17, v17, v206\n"
      "v_cvt_pk_bf16_f32 v29, v202, v203\n"
      "v_add_f32 v18, v18, v207\n"
      "v_cvt_pk_bf16_f32 v30, v204, v205\n"
      "v_mfma_f32_32x32x16_bf16 v[160:175], v[252:255], v[156:159], v[160:175]\n"
      "v_cvt_pk_bf16_f32 v31, v206, v207\n"
      "v_add_f32 v17, v17, v18\n"
      "s_nop 0\n"
      "v_cmp_lt_f32 vcc, s67, v17\n"
      "s_cbranch_vccz .Lok3_%=\n"
      "s_swappc_b64 s[62:63], s[92:93]\n"
      ".Lok3_%=:\n"
      "v_add_f32 v16, v16, v17\n"
      "s_waitcnt lgkmcnt(0)\n"
      "s_waitcnt vmcnt(0)\n"
      "s_barrier\n"
      "v_mfma_f32_32x32x16_bf16 v[64:79], v[32:35], v[24:27], v[64:79]\n"
      "ds_read_b128 v[224:227], v10 offset:40960\n"
      "ds_read_b128 v[228:231], v11 offset:40960\n"
      "ds_read_b128 v[232:235], v10 offset:41472\n"
      "ds_read_b128 v[236:239], v11 offset:41472\n"
      "v_mfma_f32_32x32x16_bf16 v[80:95], v[36:39], v[24:27], v[80:95]\n"
      "v_exp_f32 v192, v160\n"
      "ds_read_b128 v[240:243], v10 offset:41984\n"
      "v_exp_f32 v193, v161\n"
      "ds_read_b128 v[244:247], v11 offset:41984\n"
      "v_mfma_f32_32x32x16_bf16 v[96:111], v[40:43], v[24:27], v[96:111]\n"
      "v_exp_f32 v194, v162\n"
      "ds_read_b128 v[248:251], v10 offset:42496\n"
      "v_exp_f32 v195, v163\n"
      "ds_read_b128 v[252:255], v11 offset:42496\n"
      "v_mfma_f32_32x32x16_bf16 v[112:127], v[44:47], v[24:27], v[112:127]\n"
      "ds_read_b64_tr_b16 a[34:35], v14 offset:49152\n"
      "v_exp_f32 v196, v164\n"
      "ds_read_b64_tr_b16 a[38:39], v14 offset:49160\n"
      "v_add_f32 v17, v192, v194\n"
      "v_add_f32 v18, v193, v195\n"
      "v_mfma_f32_32x32x16_bf16 v[64:79], v[48:51], v[28:31], v[64:79]\n"
      "ds_read_b64_tr_b16 a[42:43], v15 offset:49152\n"
      "v_exp_f32 v197, v165\n"
      "ds_read_b64_tr_b16 a[46:47], v15 offset:49160\n"
      "v_add_f32 v17, v17, v196\n"
      "v_cvt_pk_bf16_f32 v24, v192, v193\n"
      "v_mfma_f32_32x32x16_bf16 v[80:95], v[52:55], v[28:31], v[80:95]\n"
      "v_exp_f32 v198, v166\n"
      "v_add_f32 v18, v18, v197\n"
      "v_exp_f32 v199, v167\n"
      "v_cvt_pk_bf16_f32 v25, v194, v195\n"
      "v_mfma_f32_32x32x16_bf16 v[96:111], v[56:59], v[28:31], v[96:111]\n"
      "v_exp_f32 v200, v168\n"
      "v_add_f32 v17, v17, v198\n"
      "v_exp_f32 v201, v169\n"
      "v_cvt_pk_bf16_f32 v26, v196, v197\n"
      "v_mfma_f32_32x32x16_bf16 v[112:127], v[60:63], v[28:31], v[112:127]\n"
      "v_add_f32 v18, v18, v199\n"
      "v_cvt_pk_bf16_f32 v27, v198, v199\n"
      "s_waitcnt lgkmcnt(0)\n"
      "v_mfma_f32_32x32x16_bf16 v[176:191], v[224:227], v[128:131], v[208:223]\n"
      "ds_read_b64_tr_b16 v[32:33], v12 offset:32768\n"
      "ds_read_b64_tr_b16 v[34:35], v13 offset:34816\n"
      "v_exp_f32 v202, v170\n"
      "ds_read_b64_tr_b16 v[36:37], v12 offset:33280\n"
      "v_add_f32 v17, v17, v200\n"
      "v_mfma_f32_32x32x16_bf16 v[176:191], v[228:231], v[132:135], v[176:191]\n"
      "ds_read_b64_tr_b16 v[38:39], v13 offset:35328\n"
      "ds_read_b64_tr_b16 v[40:41], v12 offset:33792\n"
      "v_exp_f32 v203, v171\n"
      "v_add_f32 v18, v18, v201\n"
      "v_mfma_f32_32x32x16_bf16 v[176:191], v[232:235], v[136:139], v[176:191]\n"
      "ds_read_b64_tr_b16 v[42:43], v13 offset:35840\n"
      "ds_read_b64_tr_b16 v[44:45], v12 offset:34304\n"
      "v_exp_f32 v204, v172\n"
      "ds_read_b64_tr_b16 v[46:47], v13 offset:36352\n"
      "v_add_f32 v17, v17, v202\n"
      "v_mfma_f32_32x32x16_bf16 v[176:191], v[236:239], v[140:143], v[176:191]\n"
      "ds_read_b64_tr_b16 v[48:49], v12 offset:36864\n"
      "ds_read_b64_tr_b16 v[50:51], v13 offset:38912\n"
      "v_exp_f32 v205, v173\n"
      "v_add_f32 v18, v18, v203\n"
      "v_mfma_f32_32x32x16_bf16 v[176:191], v[240:243], v[144:147], v[176:191]\n"
      "ds_read_b64_tr_b16 v[52:53], v12 offset:37376\n"
      "ds_read_b64_tr_b16 v[54:55], v13 offset:39424\n"
      "v_exp_f32 v206, v174\n"
      "v_add_f32 v17, v17, v204\n"
      "v_cvt_pk_bf16_f32 v28, v200, v201\n"
      "v_mfma_f32_32x32x16_bf16 v[176:191], v[244:247], v[148:151], v[176:191]\n"
      "s_waitcnt lgkmcnt(8)\n"
      "ds_read_b64_tr_b16 v[56:57], v12 offset:37888\n"
      "ds_read_b64_tr_b16 v[58:59], v13 offset:39936\n"
      "v_exp_f32 v207, v175\n"
      "v_add_f32 v18, v18, v205\n"
      "v_mfma_f32_32x32x16_bf16 v[176:191], v[248:251], v[152:155], v[176:191]\n"
      "ds_read_b64_tr_b16 v[60:61], v12 offset:38400\n"
      "ds_read_b64_tr_b16 v[62:63], v13 offset:40448\n"
      "v_add_f32 v17, v17, v206\n"
      "v_cvt_pk_bf16_f32 v29, v202, v203\n"
      "v_add_f32 v18, v18, v207\n"
      "v_cvt_pk_bf16_f32 v30, v204, v205\n"
      "v_mfma_f32_32x32x16_bf16 v[176:191], v[252:255], v[156:159], v[176:191]\n"
      "v_cvt_pk_bf16_f32 v31, v206, v207\n"
      "v_add_f32 v17, v17, v18\n"
      "s_nop 0\n"
      "v_cmp_lt_f32 vcc, s67, v17\n"
      "s_cbranch_vccz .Lok4_%=\n"
      "s_swappc_b64 s[62:63], s[64:65]\n"
      ".Lok4_%=:\n"
      "v_add_f32 v16, v16, v17\n"
      "s_waitcnt lgkmcnt(0)\n"
      "s_barrier\n"
      "v_mfma_f32_32x32x16_bf16 v[64:79], v[32:35], v[24:27], v[64:79]\n"
      "ds_read_b128 v[224:227], v10 offset:0\n"
      "ds_read_b128 v[228:231], v11 offset:0\n"
      "ds_read_b128 v[232:235], v10 offset:512\n"
      "ds_read_b128 v[236:239], v11 offset:512\n"
      "v_mfma_f32_32x32x16_bf16 v[80:95], v[36:39], v[24:27], v[80:95]\n"
      "v_exp_f32 v192, v176\n"
      "ds_read_b128 v[240:243], v10 offset:1024\n"
      "v_exp_f32 v193, v177\n"
      "ds_read_b128 v[244:247], v11 offset:1024\n"
      "v_mfma_f32_32x32x16_bf16 v[96:111], v[40:43], v[24:27], v[96:111]\n"
      "v_exp_f32 v194, v178\n"
      "ds_read_b128 v[248:251], v10 offset:1536\n"
      "v_exp_f32 v195, v179\n"
      "ds_read_b128 v[252:255], v11 offset:1536\n"
      "v_mfma_f32_32x32x16_bf16 v[112:127], v[44:47], v[24:27], v[112:127]\n"
      "ds_read_b64_tr_b16 a[48:49], v14 offset:57344\n"
      "v_exp_f32 v196, v180\n"
      "ds_read_b64_tr_b16 a[52:53], v14 offset:57352\n"
      "v_add_f32 v17, v192, v194\n"
      "v_add_f32 v18, v193, v195\n"
      "v_mfma_f32_32x32x16_bf16 v[64:79], v[48:51], v[28:31], v[64:79]\n"
      "ds_read_b64_tr_b16 a[56:57], v15 offset:57344\n"
      "v_exp_f32 v197, v181\n"
      "ds_read_b64_tr_b16 a[60:61], v15 offset:57352\n"
      "v_add_f32 v17, v17, v196\n"
      "v_cvt_pk_bf16_f32 v24, v192, v193\n"
      "v_mfma_f32_32x32x16_bf16 v[80:95], v[52:55], v[28:31], v[80:95]\n"
      "v_exp_f32 v198, v182\n"
      "v_add_f32 v18, v18, v197\n"
      "v_exp_f32 v199, v183\n"
      "v_cvt_pk_bf16_f32 v25, v194, v195\n"
      "v_mfma_f32_32x32x16_bf16 v[96:111], v[56:59], v[28:31], v[96:111]\n"
      "v_exp_f32 v200, v184\n"
      "v_add_f32 v17, v17, v198\n"
      "v_exp_f32 v201, v185\n"
      "v_cvt_pk_bf16_f32 v26, v196, v197\n"
      "v_mfma_f32_32x32x16_bf16 v[112:127], v[60:63], v[28:31], v[112:127]\n"
      "v_add_f32 v18, v18, v199\n"
      "v_cvt_pk_bf16_f32 v27, v198, v199\n"
      "s_waitcnt lgkmcnt(0)\n"
      "v_mfma_f32_32x32x16_bf16 v[160:175], v[224:227], v[128:131], v[208:223]\n"
      "ds_read_b64_tr_b16 v[32:33], v12 offset:40960\n"
      "ds_read_b64_tr_b16 v[34:35], v13 offset:43008\n"
      "v_exp_f32 v202, v186\n"
      "ds_read_b64_tr_b16 v[36:37], v12 offset:41472\n"
      "v_add_f32 v17, v17, v200\n"
      "v_mfma_f32_32x32x16_bf16 v[160:175], v[228:231], v[132:135], v[160:175]\n"
      "ds_read_b64_tr_b16 v[38:39], v13 offset:43520\n"
      "ds_read_b64_tr_b16 v[40:41], v12 offset:41984\n"
      "v_exp_f32 v203, v187\n"
      "v_add_f32 v18, v18, v201\n"
      "v_mfma_f32_32x32x16_bf16 v[160:175], v[232:235], v[136:139], v[160:175]\n"
      "ds_read_b64_tr_b16 v[42:43], v13 offset:44032\n"
      "ds_read_b64_tr_b16 v[44:45], v12 offset:42496\n"
      "v_exp_f32 v204, v188\n"
      "ds_read_b64_tr_b16 v[46:47], v13 offset:44544\n"
      "v_add_f32 v17, v17, v202\n"
      "v_mfma_f32_32x32x16_bf16 v[160:175], v[236:239], v[140:143], v[160:175]\n"
      "ds_read_b64_tr_b16 v[48:49], v12 offset:45056\n"
      "ds_read_b64_tr_b16 v[50:51], v13 offset:47104\n"
      "v_exp_f32 v205, v189\n"
      "v_add_f32 v18, v18, v203\n"
      "v_mfma_f32_32x32x16_bf16 v[160:175], v[240:243], v[144:147], v[160:175]\n"
      "ds_read_b64_tr_b16 v[52:53], v12 offset:45568\n"
      "ds_read_b64_tr_b16 v[54:55], v13 offset:47616\n"
      "v_exp_f32 v206, v190\n"
      "v_add_f32 v17, v17, v204\n"
      "v_cvt_pk_bf16_f32 v28, v200, v201\n"
      "v_mfma_f32_32x32x16_bf16 v[160:175], v[244:247], v[148:151], v[160:175]\n"
      "s_waitcnt lgkmcnt(8)\n"
      "ds_read_b64_tr_b16 v[56:57], v12 offset:46080\n"
      "ds_read_b64_tr_b16 v[58:59], v13 offset:48128\n"
      "v_exp_f32 v207, v191\n"
      "v_add_f32 v18, v18, v205\n"
      "v_mfma_f32_32x32x16_bf16 v[160:175], v[248:251], v[152:155], v[160:175]\n"
      "ds_read_b64_tr_b16 v[60:61], v12 offset:46592\n"
      "ds_read_b64_tr_b16 v[62:63], v13 offset:48640\n"
      "v_add_f32 v17, v17, v206\n"
      "v_cvt_pk_bf16_f32 v29, v202, v203\n"
      "v_add_f32 v18, v18, v207\n"
      "v_cvt_pk_bf16_f32 v30, v204, v205\n"
      "v_mfma_f32_32x32x16_bf16 v[160:175], v[252:255], v[156:159], v[160:175]\n"
      "v_cvt_pk_bf16_f32 v31, v206, v207\n"
      "v_add_f32 v17, v17, v18\n"
      "s_nop 0\n"
      "v_cmp_lt_f32 vcc, s67, v17\n"
      "s_cbranch_vccz .Lok5_%=\n"
      "s_swappc_b64 s[62:63], s[92:93]\n"
      ".Lok5_%=:\n"
      "v_add_f32 v16, v16, v17\n"
      "s_waitcnt lgkmcnt(0)\n"
      "s_barrier\n"
      "v_mfma_f32_32x32x16_bf16 v[64:79], v[32:35], v[24:27], v[64:79]\n"
      "ds_read_b128 v[224:227], v10 offset:8192\n"
      "ds_read_b128 v[228:231], v11 offset:8192\n"
      "ds_read_b128 v[232:235], v10 offset:8704\n"
      "ds_read_b128 v[236:239], v11 offset:8704\n"
      "v_mfma_f32_32x32x16_bf16 v[80:95], v[36:39], v[24:27], v[80:95]\n"
      "v_exp_f32 v192, v160\n"
      "ds_read_b128 v[240:243], v10 offset:9216\n"
      "v_exp_f32 v193, v161\n"
      "ds_read_b128 v[244:247], v11 offset:9216\n"
      "v_mfma_f32_32x32x16_bf16 v[96:111], v[40:43], v[24:27], v[96:111]\n"
      "v_exp_f32 v194, v162\n"
      "ds_read_b128 v[248:251], v10 offset:9728\n"
      "v_exp_f32 v195, v163\n"
      "ds_read_b128 v[252:255], v11 offset:9728\n"
      "v_mfma_f32_32x32x16_bf16 v[112:127], v[44:47], v[24:27], v[112:127]\n"
      "ds_read_b64_tr_b16 a[50:51], v14 offset:0\n"
      "v_exp_f32 v196, v164\n"
      "ds_read_b64_tr_b16 a[54:55], v14 offset:8\n"
      "v_add_f32 v17, v192, v194\n"
      "v_add_f32 v18, v193, v195\n"
      "v_mfma_f32_32x32x16_bf16 v[64:79], v[48:51], v[28:31], v[64:79]\n"
      "ds_read_b64_tr_b16 a[58:59], v15 offset:0\n"
      "v_exp_f32 v197, v165\n"
      "ds_read_b64_tr_b16 a[62:63], v15 offset:8\n"
      "v_add_f32 v17, v17, v196\n"
      "v_cvt_pk_bf16_f32 v24, v192, v193\n"
      "v_mfma_f32_32x32x16_bf16 v[80:95], v[52:55], v[28:31], v[80:95]\n"
      "v_exp_f32 v198, v166\n"
      "v_add_f32 v18, v18, v197\n"
      "v_exp_f32 v199, v167\n"
      "v_cvt_pk_bf16_f32 v25, v194, v195\n"
      "v_mfma_f32_32x32x16_bf16 v[96:111], v[56:59], v[28:31], v[96:111]\n"
      "v_exp_f32 v200, v168\n"
      "v_add_f32 v17, v17, v198\n"
      "v_exp_f32 v201, v169\n"
      "v_cvt_pk_bf16_f32 v26, v196, v197\n"
      "v_mfma_f32_32x32x16_bf16 v[112:127], v[60:63], v[28:31], v[112:127]\n"
      "v_add_f32 v18, v18, v199\n"
      "v_cvt_pk_bf16_f32 v27, v198, v199\n"
      "s_waitcnt lgkmcnt(0)\n"
      "v_mfma_f32_32x32x16_bf16 v[176:191], v[224:227], v[128:131], v[208:223]\n"
      "ds_read_b64_tr_b16 v[32:33], v12 offset:0\n"
      "ds_read_b64_tr_b16 v[34:35], v13 offset:2048\n"
      "v_exp_f32 v202, v170\n"
      "ds_read_b64_tr_b16 v[36:37], v12 offset:512\n"
      "v_add_f32 v17, v17, v200\n"
      "v_mfma_f32_32x32x16_bf16 v[176:191], v[228:231], v[132:135], v[176:191]\n"
      "ds_read_b64_tr_b16 v[38:39], v13 offset:2560\n"
      "ds_read_b64_tr_b16 v[40:41], v12 offset:1024\n"
      "v_exp_f32 v203, v171\n"
      "v_add_f32 v18, v18, v201\n"
      "v_mfma_f32_32x32x16_bf16 v[176:191], v[232:235], v[136:139], v[176:191]\n"
      "ds_read_b64_tr_b16 v[42:43], v13 offset:3072\n"
      "ds_read_b64_tr_b16 v[44:45], v12 offset:1536\n"
      "v_exp_f32 v204, v172\n"
      "ds_read_b64_tr_b16 v[46:47], v13 offset:3584\n"
      "v_add_f32 v17, v17, v202\n"
      "v_mfma_f32_32x32x16_bf16 v[176:191], v[236:239], v[140:143], v[176:191]\n"
      "ds_read_b64_tr_b16 v[48:49], v12 offset:4096\n"
      "ds_read_b64_tr_b16 v[50:51], v13 offset:6144\n"
      "v_exp_f32 v205, v173\n"
      "v_add_f32 v18, v18, v203\n"
      "v_mfma_f32_32x32x16_bf16 v[176:191], v[240:243], v[144:147], v[176:191]\n"
      "ds_read_b64_tr_b16 v[52:53], v12 offset:4608\n"
      "ds_read_b64_tr_b16 v[54:55], v13 offset:6656\n"
      "v_exp_f32 v206, v174\n"
      "v_add_f32 v17, v17, v204\n"
      "v_cvt_pk_bf16_f32 v28, v200, v201\n"
      "v_mfma_f32_32x32x16_bf16 v[176:191], v[244:247], v[148:151], v[176:191]\n"
      "s_waitcnt lgkmcnt(8)\n"
      "ds_read_b64_tr_b16 v[56:57], v12 offset:5120\n"
      "ds_read_b64_tr_b16 v[58:59], v13 offset:7168\n"
      "v_exp_f32 v207, v175\n"
      "v_add_f32 v18, v18, v205\n"
      "v_mfma_f32_32x32x16_bf16 v[176:191], v[248:251], v[152:155], v[176:191]\n"
      "ds_read_b64_tr_b16 v[60:61], v12 offset:5632\n"
      "ds_read_b64_tr_b16 v[62:63], v13 offset:7680\n"
      "v_add_f32 v17, v17, v206\n"
      "v_cvt_pk_bf16_f32 v29, v202, v203\n"
      "v_add_f32 v18, v18, v207\n"
      "v_cvt_pk_bf16_f32 v30, v204, v205\n"
      "v_mfma_f32_32x32x16_bf16 v[176:191], v[252:255], v[156:159], v[176:191]\n"
      "v_cvt_pk_bf16_f32 v31, v206, v207\n"
      "v_add_f32 v17, v17, v18\n"
      "s_nop 0\n"
      "v_cmp_lt_f32 vcc, s67, v17\n"
      "s_cbranch_vccz .Lok6_%=\n"
      "s_swappc_b64 s[62:63], s[64:65]\n"
      ".Lok6_%=:\n"
      "v_add_f32 v16, v16, v17\n"
      "s_waitcnt lgkmcnt(0)\n"
      "s_barrier\n"
      "v_mfma_f32_32x32x16_bf16 v[64:79], v[32:35], v[24:27], v[64:79]\n"
      "ds_read_b128 v[224:227], v10 offset:16384\n"
      "ds_read_b128 v[228:231], v11 offset:16384\n"
      "ds_read_b128 v[232:235], v10 offset:16896\n"
      "ds_read_b128 v[236:239], v11 offset:16896\n"
      "v_mfma_f32_32x32x16_bf16 v[80:95], v[36:39], v[24:27], v[80:95]\n"
      "v_exp_f32 v192, v176\n"
      "ds_read_b128 v[240:243], v10 offset:17408\n"
      "v_exp_f32 v193, v177\n"
      "ds_read_b128 v[244:247], v11 offset:17408\n"
      "v_mfma_f32_32x32x16_bf16 v[96:111], v[40:43], v[24:27], v[96:111]\n"
      "v_exp_f32 v194, v178\n"
      "ds_read_b128 v[248:251], v10 offset:17920\n"
      "v_exp_f32 v195, v179\n"
      "ds_read_b128 v[252:255], v11 offset:17920\n"
      "v_mfma_f32_32x32x16_bf16 v[112:127], v[44:47], v[24:27], v[112:127]\n"
      "ds_read_b64_tr_b16 a[64:65], v14 offset:8192\n"
      "v_exp_f32 v196, v180\n"
      "ds_read_b64_tr_b16 a[68:69], v14 offset:8200\n"
      "v_add_f32 v17, v192, v194\n"
      "v_add_f32 v18, v193, v195\n"
      "v_mfma_f32_32x32x16_bf16 v[64:79], v[48:51], v[28:31], v[64:79]\n"
      "ds_read_b64_tr_b16 a[72:73], v15 offset:8192\n"
      "v_exp_f32 v197, v181\n"
      "ds_read_b64_tr_b16 a[76:77], v15 offset:8200\n"
      "v_add_f32 v17, v17, v196\n"
      "v_cvt_pk_bf16_f32 v24, v192, v193\n"
      "v_mfma_f32_32x32x16_bf16 v[80:95], v[52:55], v[28:31], v[80:95]\n"
      "v_exp_f32 v198, v182\n"
      "v_add_f32 v18, v18, v197\n"
      "v_exp_f32 v199, v183\n"
      "v_cvt_pk_bf16_f32 v25, v194, v195\n"
      "v_mfma_f32_32x32x16_bf16 v[96:111], v[56:59], v[28:31], v[96:111]\n"
      "v_exp_f32 v200, v184\n"
      "v_add_f32 v17, v17, v198\n"
      "v_exp_f32 v201, v185\n"
      "v_cvt_pk_bf16_f32 v26, v196, v197\n"
      "v_mfma_f32_32x32x16_bf16 v[112:127], v[60:63], v[28:31], v[112:127]\n"
      "v_add_f32 v18, v18, v199\n"
      "v_cvt_pk_bf16_f32 v27, v198, v199\n"
      "s_waitcnt lgkmcnt(0)\n"
      "v_mfma_f32_32x32x16_bf16 v[160:175], v[224:227], v[128:131], v[208:223]\n"
      "ds_read_b64_tr_b16 v[32:33], v12 offset:8192\n"
      "ds_read_b64_tr_b16 v[34:35], v13 offset:10240\n"
      "v_exp_f32 v202, v186\n"
      "ds_read_b64_tr_b16 v[36:37], v12 offset:8704\n"
      "v_add_f32 v17, v17, v200\n"
      "v_mfma_f32_32x32x16_bf16 v[160:175], v[228:231], v[132:135], v[160:175]\n"
      "ds_read_b64_tr_b16 v[38:39], v13 offset:10752\n"
      "ds_read_b64_tr_b16 v[40:41], v12 offset:9216\n"
      "v_exp_f32 v203, v187\n"
      "v_add_f32 v18, v18, v201\n"
      "v_mfma_f32_32x32x16_bf16 v[160:175], v[232:235], v[136:139], v[160:175]\n"
      "ds_read_b64_tr_b16 v[42:43], v13 offset:11264\n"
      "ds_read_b64_tr_b16 v[44:45], v12 offset:9728\n"
      "v_exp_f32 v204, v188\n"
      "ds_read_b64_tr_b16 v[46:47], v13 offset:11776\n"
      "v_add_f32 v17, v17, v202\n"
      "v_mfma_f32_32x32x16_bf16 v[160:175], v[236:239], v[140:143], v[160:175]\n"
      "ds_read_b64_tr_b16 v[48:49], v12 offset:12288\n"
      "ds_read_b64_tr_b16 v[50:51], v13 offset:14336\n"
      "v_exp_f32 v205, v189\n"
      "v_add_f32 v18, v18, v203\n"
      "v_mfma_f32_32x32x16_bf16 v[160:175], v[240:243], v[144:147], v[160:175]\n"
      "ds_read_b64_tr_b16 v[52:53], v12 offset:12800\n"
      "ds_read_b64_tr_b16 v[54:55], v13 offset:14848\n"
      "v_exp_f32 v206, v190\n"
      "v_add_f32 v17, v17, v204\n"
      "v_cvt_pk_bf16_f32 v28, v200, v201\n"
      "v_mfma_f32_32x32x16_bf16 v[160:175], v[244:247], v[148:151], v[160:175]\n"
      "s_waitcnt lgkmcnt(8)\n"
      "ds_read_b64_tr_b16 v[56:57], v12 offset:13312\n"
      "ds_read_b64_tr_b16 v[58:59], v13 offset:15360\n"
      "v_exp_f32 v207, v191\n"
      "v_add_f32 v18, v18, v205\n"
      "v_mfma_f32_32x32x16_bf16 v[160:175], v[248:251], v[152:155], v[160:175]\n"
      "ds_read_b64_tr_b16 v[60:61], v12 offset:13824\n"
      "ds_read_b64_tr_b16 v[62:63], v13 offset:15872\n"
      "v_add_f32 v17, v17, v206\n"
      "v_cvt_pk_bf16_f32 v29, v202, v203\n"
      "v_add_f32 v18, v18, v207\n"
      "v_cvt_pk_bf16_f32 v30, v204, v205\n"
      "v_mfma_f32_32x32x16_bf16 v[160:175], v[252:255], v[156:159], v[160:175]\n"
      "v_cvt_pk_bf16_f32 v31, v206, v207\n"
      "v_add_f32 v17, v17, v18\n"
      "s_nop 0\n"
      "v_cmp_lt_f32 vcc, s67, v17\n"
      "s_cbranch_vccz .Lok7_%=\n"
      "s_swappc_b64 s[62:63], s[92:93]\n"
      ".Lok7_%=:\n"
      "v_add_f32 v16, v16, v17\n"
      "s_waitcnt lgkmcnt(0)\n"
      "s_barrier\n"
      "v_mfma_f32_32x32x16_bf16 v[64:79], v[32:35], v[24:27], v[64:79]\n"
      "ds_read_b128 v[224:227], v10 offset:24576\n"
      "ds_read_b128 v[228:231], v11 offset:24576\n"
      "ds_read_b128 v[232:235], v10 offset:25088\n"
      "ds_read_b128 v[236:239], v11 offset:25088\n"
      "v_mfma_f32_32x32x16_bf16 v[80:95], v[36:39], v[24:27], v[80:95]\n"
      "v_exp_f32 v192, v160\n"
      "ds_read_b128 v[240:243], v10 offset:25600\n"
      "v_exp_f32 v193, v161\n"
      "ds_read_b128 v[244:247], v11 offset:25600\n"
      "v_mfma_f32_32x32x16_bf16 v[96:111], v[40:43], v[24:27], v[96:111]\n"
      "v_exp_f32 v194, v162\n"
      "ds_read_b128 v[248:251], v10 offset:26112\n"
      "v_exp_f32 v195, v163\n"
      "ds_read_b128 v[252:255], v11 offset:26112\n"
      "v_mfma_f32_32x32x16_bf16 v[112:127], v[44:47], v[24:27], v[112:127]\n"
      "ds_read_b64_tr_b16 a[66:67], v14 offset:16384\n"
      "v_exp_f32 v196, v164\n"
      "ds_read_b64_tr_b16 a[70:71], v14 offset:16392\n"
      "v_add_f32 v17, v192, v194\n"
      "v_add_f32 v18, v193, v195\n"
      "v_mfma_f32_32x32x16_bf16 v[64:79], v[48:51], v[28:31], v[64:79]\n"
      "ds_read_b64_tr_b16 a[74:75], v15 offset:16384\n"
      "v_exp_f32 v197, v165\n"
      "ds_read_b64_tr_b16 a[78:79], v15 offset:16392\n"
      "v_add_f32 v17, v17, v196\n"
      "v_cvt_pk_bf16_f32 v24, v192, v193\n"
      "v_mfma_f32_32x32x16_bf16 v[80:95], v[52:55], v[28:31], v[80:95]\n"
      "v_exp_f32 v198, v166\n"
      "v_add_f32 v18, v18, v197\n"
      "v_exp_f32 v199, v167\n"
      "v_cvt_pk_bf16_f32 v25, v194, v195\n"
      "v_mfma_f32_32x32x16_bf16 v[96:111], v[56:59], v[28:31], v[96:111]\n"
      "v_exp_f32 v200, v168\n"
      "v_add_f32 v17, v17, v198\n"
      "v_exp_f32 v201, v169\n"
      "v_cvt_pk_bf16_f32 v26, v196, v197\n"
      "v_mfma_f32_32x32x16_bf16 v[112:127], v[60:63], v[28:31], v[112:127]\n"
      "v_add_f32 v18, v18, v199\n"
      "v_cvt_pk_bf16_f32 v27, v198, v199\n"
      "s_waitcnt lgkmcnt(0)\n"
      "v_mfma_f32_32x32x16_bf16 v[176:191], v[224:227], v[128:131], v[208:223]\n"
      "ds_read_b64_tr_b16 v[32:33], v12 offset:16384\n"
      "ds_read_b64_tr_b16 v[34:35], v13 offset:18432\n"
      "v_exp_f32 v202, v170\n"
      "ds_read_b64_tr_b16 v[36:37], v12 offset:16896\n"
      "v_add_f32 v17, v17, v200\n"
      "v_mfma_f32_32x32x16_bf16 v[176:191], v[228:231], v[132:135], v[176:191]\n"
      "ds_read_b64_tr_b16 v[38:39], v13 offset:18944\n"
      "ds_read_b64_tr_b16 v[40:41], v12 offset:17408\n"
      "v_exp_f32 v203, v171\n"
      "v_add_f32 v18, v18, v201\n"
      "v_mfma_f32_32x32x16_bf16 v[176:191], v[232:235], v[136:139], v[176:191]\n"
      "ds_read_b64_tr_b16 v[42:43], v13 offset:19456\n"
      "ds_read_b64_tr_b16 v[44:45], v12 offset:17920\n"
      "v_exp_f32 v204, v172\n"
      "ds_read_b64_tr_b16 v[46:47], v13 offset:19968\n"
      "v_add_f32 v17, v17, v202\n"
      "v_mfma_f32_32x32x16_bf16 v[176:191], v[236:239], v[140:143], v[176:191]\n"
      "ds_read_b64_tr_b16 v[48:49], v12 offset:20480\n"
      "ds_read_b64_tr_b16 v[50:51], v13 offset:22528\n"
      "v_exp_f32 v205, v173\n"
      "v_add_f32 v18, v18, v203\n"
      "v_mfma_f32_32x32x16_bf16 v[176:191], v[240:243], v[144:147], v[176:191]\n"
      "ds_read_b64_tr_b16 v[52:53], v12 offset:20992\n"
      "ds_read_b64_tr_b16 v[54:55], v13 offset:23040\n"
      "v_exp_f32 v206, v174\n"
      "v_add_f32 v17, v17, v204\n"
      "v_cvt_pk_bf16_f32 v28, v200, v201\n"
      "v_mfma_f32_32x32x16_bf16 v[176:191], v[244:247], v[148:151], v[176:191]\n"
      "s_waitcnt lgkmcnt(8)\n"
      "ds_read_b64_tr_b16 v[56:57], v12 offset:21504\n"
      "ds_read_b64_tr_b16 v[58:59], v13 offset:23552\n"
      "v_exp_f32 v207, v175\n"
      "v_add_f32 v18, v18, v205\n"
      "v_mfma_f32_32x32x16_bf16 v[176:191], v[248:251], v[152:155], v[176:191]\n"
      "ds_read_b64_tr_b16 v[60:61], v12 offset:22016\n"
      "ds_read_b64_tr_b16 v[62:63], v13 offset:24064\n"
      "v_add_f32 v17, v17, v206\n"
      "v_cvt_pk_bf16_f32 v29, v202, v203\n"
      "v_add_f32 v18, v18, v207\n"
      "v_cvt_pk_bf16_f32 v30, v204, v205\n"
      "v_mfma_f32_32x32x16_bf16 v[176:191], v[252:255], v[156:159], v[176:191]\n"
      "v_cvt_pk_bf16_f32 v31, v206, v207\n"
      "v_add_f32 v17, v17, v18\n"
      "s_nop 0\n"
      "v_cmp_lt_f32 vcc, s67, v17\n"
      "s_cbranch_vccz .Lok8_%=\n"
      "s_swappc_b64 s[62:63], s[64:65]\n"
      ".Lok8_%=:\n"
      "v_add_f32 v16, v16, v17\n"
      "s_waitcnt lgkmcnt(0)\n"
      "s_barrier\n"
      "v_mfma_f32_32x32x16_bf16 v[64:79], v[32:35], v[24:27], v[64:79]\n"
      "ds_read_b128 v[224:227], v10 offset:32768\n"
      "ds_read_b128 v[228:231], v11 offset:32768\n"
      "ds_read_b128 v[232:235], v10 offset:33280\n"
      "ds_read_b128 v[236:239], v11 offset:33280\n"
      "v_mfma_f32_32x32x16_bf16 v[80:95], v[36:39], v[24:27], v[80:95]\n"
      "v_exp_f32 v192, v176\n"
      "ds_read_b128 v[240:243], v10 offset:33792\n"
      "v_exp_f32 v193, v177\n"
      "ds_read_b128 v[244:247], v11 offset:33792\n"
      "v_mfma_f32_32x32x16_bf16 v[96:111], v[40:43], v[24:27], v[96:111]\n"
      "v_exp_f32 v194, v178\n"
      "ds_read_b128 v[248:251], v10 offset:34304\n"
      "v_exp_f32 v195, v179\n"
      "ds_read_b128 v[252:255], v11 offset:34304\n"
      "v_mfma_f32_32x32x16_bf16 v[112:127], v[44:47], v[24:27], v[112:127]\n"
      "ds_read_b64_tr_b16 a[80:81], v14 offset:40960\n"
      "v_exp_f32 v196, v180\n"
      "ds_read_b64_tr_b16 a[84:85], v14 offset:40968\n"
      "v_add_f32 v17, v192, v194\n"
      "v_add_f32 v18, v193, v195\n"
      "v_mfma_f32_32x32x16_bf16 v[64:79], v[48:51], v[28:31], v[64:79]\n"
      "ds_read_b64_tr_b16 a[88:89], v15 offset:40960\n"
      "v_exp_f32 v197, v181\n"
      "ds_read_b64_tr_b16 a[92:93], v15 offset:40968\n"
      "v_add_f32 v17, v17, v196\n"
      "v_cvt_pk_bf16_f32 v24, v192, v193\n"
      "v_mfma_f32_32x32x16_bf16 v[80:95], v[52:55], v[28:31], v[80:95]\n"
      "v_exp_f32 v198, v182\n"
      "v_add_f32 v18, v18, v197\n"
      "v_exp_f32 v199, v183\n"
      "v_cvt_pk_bf16_f32 v25, v194, v195\n"
      "v_mfma_f32_32x32x16_bf16 v[96:111], v[56:59], v[28:31], v[96:111]\n"
      "v_exp_f32 v200, v184\n"
      "v_add_f32 v17, v17, v198\n"
      "v_exp_f32 v201, v185\n"
      "v_cvt_pk_bf16_f32 v26, v196, v197\n"
      "v_mfma_f32_32x32x16_bf16 v[112:127], v[60:63], v[28:31], v[112:127]\n"
      "v_add_f32 v18, v18, v199\n"
      "v_cvt_pk_bf16_f32 v27, v198, v199\n"
      "s_waitcnt lgkmcnt(0)\n"
      "v_mfma_f32_32x32x16_bf16 v[160:175], v[224:227], v[128:131], v[208:223]\n"
      "ds_read_b64_tr_b16 v[32:33], v12 offset:24576\n"
      "ds_read_b64_tr_b16 v[34:35], v13 offset:26624\n"
      "v_exp_f32 v202, v186\n"
      "ds_read_b64_tr_b16 v[36:37], v12 offset:25088\n"
      "v_add_f32 v17, v17, v200\n"
      "v_mfma_f32_32x32x16_bf16 v[160:175], v[228:231], v[132:135], v[160:175]\n"
      "ds_read_b64_tr_b16 v[38:39], v13 offset:27136\n"
      "ds_read_b64_tr_b16 v[40:41], v12 offset:25600\n"
      "v_exp_f32 v203, v187\n"
      "v_add_f32 v18, v18, v201\n"
      "v_mfma_f32_32x32x16_bf16 v[160:175], v[232:235], v[136:139], v[160:175]\n"
      "ds_read_b64_tr_b16 v[42:43], v13 offset:27648\n"
      "ds_read_b64_tr_b16 v[44:45], v12 offset:26112\n"
      "v_exp_f32 v204, v188\n"
      "ds_read_b64_tr_b16 v[46:47], v13 offset:28160\n"
      "v_add_f32 v17, v17, v202\n"
      "v_mfma_f32_32x32x16_bf16 v[160:175], v[236:239], v[140:143], v[160:175]\n"
      "ds_read_b64_tr_b16 v[48:49], v12 offset:28672\n"
      "ds_read_b64_tr_b16 v[50:51], v13 offset:30720\n"
      "v_exp_f32 v205, v189\n"
      "v_add_f32 v18, v18, v203\n"
      "v_mfma_f32_32x32x16_bf16 v[160:175], v[240:243], v[144:147], v[160:175]\n"
      "ds_read_b64_tr_b16 v[52:53], v12 offset:29184\n"
      "ds_read_b64_tr_b16 v[54:55], v13 offset:31232\n"
      "v_exp_f32 v206, v190\n"
      "v_add_f32 v17, v17, v204\n"
      "v_cvt_pk_bf16_f32 v28, v200, v201\n"
      "v_mfma_f32_32x32x16_bf16 v[160:175], v[244:247], v[148:151], v[160:175]\n"
      "s_waitcnt lgkmcnt(8)\n"
      "ds_read_b64_tr_b16 v[56:57], v12 offset:29696\n"
      "ds_read_b64_tr_b16 v[58:59], v13 offset:31744\n"
      "v_exp_f32 v207, v191\n"
      "v_add_f32 v18, v18, v205\n"
      "v_mfma_f32_32x32x16_bf16 v[160:175], v[248:251], v[152:155], v[160:175]\n"
      "ds_read_b64_tr_b16 v[60:61], v12 offset:30208\n"
      "ds_read_b64_tr_b16 v[62:63], v13 offset:32256\n"
      "v_add_f32 v17, v17, v206\n"
      "v_cvt_pk_bf16_f32 v29, v202, v203\n"
      "v_add_f32 v18, v18, v207\n"
      "v_cvt_pk_bf16_f32 v30, v204, v205\n"
      "v_mfma_f32_32x32x16_bf16 v[160:175], v[252:255], v[156:159], v[160:175]\n"
      "v_cvt_pk_bf16_f32 v31, v206, v207\n"
      "v_add_f32 v17, v17, v18\n"
      "s_nop 0\n"
      "v_cmp_lt_f32 vcc, s67, v17\n"
      "s_cbranch_vccz .Lok9_%=\n"
      "s_swappc_b64 s[62:63], s[92:93]\n"
      ".Lok9_%=:\n"
      "v_add_f32 v16, v16, v17\n"
      "s_waitcnt lgkmcnt(0)\n"
      "s_barrier\n"
      "v_mfma_f32_32x32x16_bf16 v[64:79], v[32:35], v[24:27], v[64:79]\n"
      "ds_read_b128 v[224:227], v10 offset:40960\n"
      "ds_read_b128 v[228:231], v11 offset:40960\n"
      "ds_read_b128 v[232:235], v10 offset:41472\n"
      "ds_read_b128 v[236:239], v11 offset:41472\n"
      "v_mfma_f32_32x32x16_bf16 v[80:95], v[36:39], v[24:27], v[80:95]\n"
      "v_exp_f32 v192, v160\n"
      "ds_read_b128 v[240:243], v10 offset:41984\n"
      "v_exp_f32 v193, v161\n"
      "ds_read_b128 v[244:247], v11 offset:41984\n"
      "v_mfma_f32_32x32x16_bf16 v[96:111], v[40:43], v[24:27], v[96:111]\n"
      "v_exp_f32 v194, v162\n"
      "ds_read_b128 v[248:251], v10 offset:42496\n"
      "v_exp_f32 v195, v163\n"
      "ds_read_b128 v[252:255], v11 offset:42496\n"
      "v_mfma_f32_32x32x16_bf16 v[112:127], v[44:47], v[24:27], v[112:127]\n"
      "ds_read_b64_tr_b16 a[82:83], v14 offset:24576\n"
      "v_exp_f32 v196, v164\n"
      "ds_read_b64_tr_b16 a[86:87], v14 offset:24584\n"
      "v_add_f32 v17, v192, v194\n"
      "v_add_f32 v18, v193, v195\n"
      "v_mfma_f32_32x32x16_bf16 v[64:79], v[48:51], v[28:31], v[64:79]\n"
      "ds_read_b64_tr_b16 a[90:91], v15 offset:24576\n"
      "v_exp_f32 v197, v165\n"
      "ds_read_b64_tr_b16 a[94:95], v15 offset:24584\n"
      "v_add_f32 v17, v17, v196\n"
      "v_cvt_pk_bf16_f32 v24, v192, v193\n"
      "v_mfma_f32_32x32x16_bf16 v[80:95], v[52:55], v[28:31], v[80:95]\n"
      "v_exp_f32 v198, v166\n"
      "v_add_f32 v18, v18, v197\n"
      "v_exp_f32 v199, v167\n"
      "v_cvt_pk_bf16_f32 v25, v194, v195\n"
      "v_mfma_f32_32x32x16_bf16 v[96:111], v[56:59], v[28:31], v[96:111]\n"
      "v_exp_f32 v200, v168\n"
      "v_add_f32 v17, v17, v198\n"
      "v_exp_f32 v201, v169\n"
      "v_cvt_pk_bf16_f32 v26, v196, v197\n"
      "v_mfma_f32_32x32x16_bf16 v[112:127], v[60:63], v[28:31], v[112:127]\n"
      "v_add_f32 v18, v18, v199\n"
      "v_cvt_pk_bf16_f32 v27, v198, v199\n"
      "s_waitcnt lgkmcnt(0)\n"
      "v_mfma_f32_32x32x16_bf16 v[176:191], v[224:227], v[128:131], v[208:223]\n"
      "ds_read_b64_tr_b16 v[32:33], v12 offset:32768\n"
      "ds_read_b64_tr_b16 v[34:35], v13 offset:34816\n"
      "v_exp_f32 v202, v170\n"
      "ds_read_b64_tr_b16 v[36:37], v12 offset:33280\n"
      "v_add_f32 v17, v17, v200\n"
      "v_mfma_f32_32x32x16_bf16 v[176:191], v[228:231], v[132:135], v[176:191]\n"
      "ds_read_b64_tr_b16 v[38:39], v13 offset:35328\n"
      "ds_read_b64_tr_b16 v[40:41], v12 offset:33792\n"
      "v_exp_f32 v203, v171\n"
      "v_add_f32 v18, v18, v201\n"
      "v_mfma_f32_32x32x16_bf16 v[176:191], v[232:235], v[136:139], v[176:191]\n"
      "ds_read_b64_tr_b16 v[42:43], v13 offset:35840\n"
      "ds_read_b64_tr_b16 v[44:45], v12 offset:34304\n"
      "v_exp_f32 v204, v172\n"
      "ds_read_b64_tr_b16 v[46:47], v13 offset:36352\n"
      "v_add_f32 v17, v17, v202\n"
      "v_mfma_f32_32x32x16_bf16 v[176:191], v[236:239], v[140:143], v[176:191]\n"
      "ds_read_b64_tr_b16 v[48:49], v12 offset:36864\n"
      "ds_read_b64_tr_b16 v[50:51], v13 offset:38912\n"
      "v_exp_f32 v205, v173\n"
      "v_add_f32 v18, v18, v203\n"
      "v_mfma_f32_32x32x16_bf16 v[176:191], v[240:243], v[144:147], v[176:191]\n"
      "ds_read_b64_tr_b16 v[52:53], v12 offset:37376\n"
      "ds_read_b64_tr_b16 v[54:55], v13 offset:39424\n"
      "v_exp_f32 v206, v174\n"
      "v_add_f32 v17, v17, v204\n"
      "v_cvt_pk_bf16_f32 v28, v200, v201\n"
      "v_mfma_f32_32x32x16_bf16 v[176:191], v[244:247], v[148:151], v[176:191]\n"
      "s_waitcnt lgkmcnt(8)\n"
      "ds_read_b64_tr_b16 v[56:57], v12 offset:37888\n"
      "ds_read_b64_tr_b16 v[58:59], v13 offset:39936\n"
      "v_exp_f32 v207, v175\n"
      "v_add_f32 v18, v18, v205\n"
      "v_mfma_f32_32x32x16_bf16 v[176:191], v[248:251], v[152:155], v[176:191]\n"
      "ds_read_b64_tr_b16 v[60:61], v12 offset:38400\n"
      "ds_read_b64_tr_b16 v[62:63], v13 offset:40448\n"
      "v_add_f32 v17, v17, v206\n"
      "v_cvt_pk_bf16_f32 v29, v202, v203\n"
      "v_add_f32 v18, v18, v207\n"
      "v_cvt_pk_bf16_f32 v30, v204, v205\n"
      "v_mfma_f32_32x32x16_bf16 v[176:191], v[252:255], v[156:159], v[176:191]\n"
      "v_cvt_pk_bf16_f32 v31, v206, v207\n"
      "v_add_f32 v17, v17, v18\n"
      "s_nop 0\n"
      "v_cmp_lt_f32 vcc, s67, v17\n"
      "s_cbranch_vccz .Lok10_%=\n"
      "s_swappc_b64 s[62:63], s[64:65]\n"
      ".Lok10_%=:\n"
      "v_add_f32 v16, v16, v17\n"
      "s_waitcnt lgkmcnt(0)\n"
      "s_barrier\n"
      "v_mfma_f32_32x32x16_bf16 v[64:79], v[32:35], v[24:27], v[64:79]\n"
      "ds_read_b128 v[224:227], v10 offset:0\n"
      "ds_read_b128 v[228:231], v11 offset:0\n"
      "ds_read_b128 v[232:235], v10 offset:512\n"
      "ds_read_b128 v[236:239], v11 offset:512\n"
      "v_mfma_f32_32x32x16_bf16 v[80:95], v[36:39], v[24:27], v[80:95]\n"
      "v_exp_f32 v192, v176\n"
      "ds_read_b128 v[240:243], v10 offset:1024\n"
      "v_exp_f32 v193, v177\n"
      "ds_read_b128 v[244:247], v11 offset:1024\n"
      "v_mfma_f32_32x32x16_bf16 v[96:111], v[40:43], v[24:27], v[96:111]\n"
      "v_exp_f32 v194, v178\n"
      "ds_read_b128 v[248:251], v10 offset:1536\n"
      "v_exp_f32 v195, v179\n"
      "ds_read_b128 v[252:255], v11 offset:1536\n"
      "v_mfma_f32_32x32x16_bf16 v[112:127], v[44:47], v[24:27], v[112:127]\n"
      "ds_read_b64_tr_b16 a[96:97], v14 offset:32768\n"
      "v_exp_f32 v196, v180\n"
      "ds_read_b64_tr_b16 a[100:101], v14 offset:32776\n"
      "v_add_f32 v17, v192, v194\n"
      "v_add_f32 v18, v193, v195\n"
      "v_mfma_f32_32x32x16_bf16 v[64:79], v[48:51], v[28:31], v[64:79]\n"
      "ds_read_b64_tr_b16 a[104:105], v15 offset:32768\n"
      "v_exp_f32 v197, v181\n"
      "ds_read_b64_tr_b16 a[108:109], v15 offset:32776\n"
      "v_add_f32 v17, v17, v196\n"
      "v_cvt_pk_bf16_f32 v24, v192, v193\n"
      "v_mfma_f32_32x32x16_bf16 v[80:95], v[52:55], v[28:31], v[80:95]\n"
      "v_exp_f32 v198, v182\n"
      "v_add_f32 v18, v18, v197\n"
      "v_exp_f32 v199, v183\n"
      "v_cvt_pk_bf16_f32 v25, v194, v195\n"
      "v_mfma_f32_32x32x16_bf16 v[96:111], v[56:59], v[28:31], v[96:111]\n"
      "v_exp_f32 v200, v184\n"
      "v_add_f32 v17, v17, v198\n"
      "v_exp_f32 v201, v185\n"
      "v_cvt_pk_bf16_f32 v26, v196, v197\n"
      "v_mfma_f32_32x32x16_bf16 v[112:127], v[60:63], v[28:31], v[112:127]\n"
      "v_add_f32 v18, v18, v199\n"
      "v_cvt_pk_bf16_f32 v27, v198, v199\n"
      "s_waitcnt lgkmcnt(0)\n"
      "v_mfma_f32_32x32x16_bf16 v[160:175], v[224:227], v[128:131], v[208:223]\n"
      "ds_read_b64_tr_b16 v[32:33], v12 offset:40960\n"
      "ds_read_b64_tr_b16 v[34:35], v13 offset:43008\n"
      "v_exp_f32 v202, v186\n"
      "ds_read_b64_tr_b16 v[36:37], v12 offset:41472\n"
      "v_add_f32 v17, v17, v200\n"
      "v_mfma_f32_32x32x16_bf16 v[160:175], v[228:231], v[132:135], v[160:175]\n"
      "ds_read_b64_tr_b16 v[38:39], v13 offset:43520\n"
      "ds_read_b64_tr_b16 v[40:41], v12 offset:41984\n"
      "v_exp_f32 v203, v187\n"
      "v_add_f32 v18, v18, v201\n"
      "v_mfma_f32_32x32x16_bf16 v[160:175], v[232:235], v[136:139], v[160:175]\n"
      "ds_read_b64_tr_b16 v[42:43], v13 offset:44032\n"
      "ds_read_b64_tr_b16 v[44:45], v12 offset:42496\n"
      "v_exp_f32 v204, v188\n"
      "ds_read_b64_tr_b16 v[46:47], v13 offset:44544\n"
      "v_add_f32 v17, v17, v202\n"
      "v_mfma_f32_32x32x16_bf16 v[160:175], v[236:239], v[140:143], v[160:175]\n"
      "ds_read_b64_tr_b16 v[48:49], v12 offset:45056\n"
      "ds_read_b64_tr_b16 v[50:51], v13 offset:47104\n"
      "v_exp_f32 v205, v189\n"
      "v_add_f32 v18, v18, v203\n"
      "v_mfma_f32_32x32x16_bf16 v[160:175], v[240:243], v[144:147], v[160:175]\n"
      "ds_read_b64_tr_b16 v[52:53], v12 offset:45568\n"
      "ds_read_b64_tr_b16 v[54:55], v13 offset:47616\n"
      "v_exp_f32 v206, v190\n"
      "v_add_f32 v17, v17, v204\n"
      "v_cvt_pk_bf16_f32 v28, v200, v201\n"
      "v_mfma_f32_32x32x16_bf16 v[160:175], v[244:247], v[148:151], v[160:175]\n"
      "s_waitcnt lgkmcnt(8)\n"
      "ds_read_b64_tr_b16 v[56:57], v12 offset:46080\n"
      "ds_read_b64_tr_b16 v[58:59], v13 offset:48128\n"
      "v_exp_f32 v207, v191\n"
      "v_add_f32 v18, v18, v205\n"
      "v_mfma_f32_32x32x16_bf16 v[160:175], v[248:251], v[152:155], v[160:175]\n"
      "ds_read_b64_tr_b16 v[60:61], v12 offset:46592\n"
      "ds_read_b64_tr_b16 v[62:63], v13 offset:48640\n"
      "v_add_f32 v17, v17, v206\n"
      "v_cvt_pk_bf16_f32 v29, v202, v203\n"
      "v_add_f32 v18, v18, v207\n"
      "v_cvt_pk_bf16_f32 v30, v204, v205\n"
      "v_mfma_f32_32x32x16_bf16 v[160:175], v[252:255], v[156:159], v[160:175]\n"
      "v_cvt_pk_bf16_f32 v31, v206, v207\n"
      "v_add_f32 v17, v17, v18\n"
      "s_nop 0\n"
      "v_cmp_lt_f32 vcc, s67, v17\n"
      "s_cbranch_vccz .Lok11_%=\n"
      "s_swappc_b64 s[62:63], s[92:93]\n"
      ".Lok11_%=:\n"
      "v_add_f32 v16, v16, v17\n"
      "s_waitcnt lgkmcnt(0)\n"
      "s_barrier\n"
      "v_mfma_f32_32x32x16_bf16 v[64:79], v[32:35], v[24:27], v[64:79]\n"
      "ds_read_b128 v[224:227], v10 offset:8192\n"
      "ds_read_b128 v[228:231], v11 offset:8192\n"
      "ds_read_b128 v[232:235], v10 offset:8704\n"
      "ds_read_b128 v[236:239], v11 offset:8704\n"
      "v_mfma_f32_32x32x16_bf16 v[80:95], v[36:39], v[24:27], v[80:95]\n"
      "v_exp_f32 v192, v160\n"
      "ds_read_b128 v[240:243], v10 offset:9216\n"
      "v_exp_f32 v193, v161\n"
      "ds_read_b128 v[244:247], v11 offset:9216\n"
      "v_mfma_f32_32x32x16_bf16 v[96:111], v[40:43], v[24:27], v[96:111]\n"
      "v_exp_f32 v194, v162\n"
      "ds_read_b128 v[248:251], v10 offset:9728\n"
      "v_exp_f32 v195, v163\n"
      "ds_read_b128 v[252:255], v11 offset:9728\n"
      "v_mfma_f32_32x32x16_bf16 v[112:127], v[44:47], v[24:27], v[112:127]\n"
      "ds_read_b64_tr_b16 a[98:99], v14 offset:49152\n"
      "v_exp_f32 v196, v164\n"
      "ds_read_b64_tr_b16 a[102:103], v14 offset:49160\n"
      "v_add_f32 v17, v192, v194\n"
      "v_add_f32 v18, v193, v195\n"
      "v_mfma_f32_32x32x16_bf16 v[64:79], v[48:51], v[28:31], v[64:79]\n"
      "ds_read_b64_tr_b16 a[106:107], v15 offset:49152\n"
      "v_exp_f32 v197, v165\n"
      "ds_read_b64_tr_b16 a[110:111], v15 offset:49160\n"
      "v_add_f32 v17, v17, v196\n"
      "v_cvt_pk_bf16_f32 v24, v192, v193\n"
      "v_mfma_f32_32x32x16_bf16 v[80:95], v[52:55], v[28:31], v[80:95]\n"
      "v_exp_f32 v198, v166\n"
      "v_add_f32 v18, v18, v197\n"
      "v_exp_f32 v199, v167\n"
      "v_cvt_pk_bf16_f32 v25, v194, v195\n"
      "v_mfma_f32_32x32x16_bf16 v[96:111], v[56:59], v[28:31], v[96:111]\n"
      "v_exp_f32 v200, v168\n"
      "v_add_f32 v17, v17, v198\n"
      "v_exp_f32 v201, v169\n"
      "v_cvt_pk_bf16_f32 v26, v196, v197\n"
      "v_mfma_f32_32x32x16_bf16 v[112:127], v[60:63], v[28:31], v[112:127]\n"
      "v_add_f32 v18, v18, v199\n"
      "v_cvt_pk_bf16_f32 v27, v198, v199\n"
      "s_waitcnt lgkmcnt(0)\n"
      "v_mfma_f32_32x32x16_bf16 v[176:191], v[224:227], v[128:131], v[208:223]\n"
      "ds_read_b64_tr_b16 v[32:33], v12 offset:0\n"
      "ds_read_b64_tr_b16 v[34:35], v13 offset:2048\n"
      "v_exp_f32 v202, v170\n"
      "ds_read_b64_tr_b16 v[36:37], v12 offset:512\n"
      "v_add_f32 v17, v17, v200\n"
      "v_mfma_f32_32x32x16_bf16 v[176:191], v[228:231], v[132:135], v[176:191]\n"
      "ds_read_b64_tr_b16 v[38:39], v13 offset:2560\n"
      "ds_read_b64_tr_b16 v[40:41], v12 offset:1024\n"
      "v_exp_f32 v203, v171\n"
      "v_add_f32 v18, v18, v201\n"
      "v_mfma_f32_32x32x16_bf16 v[176:191], v[232:235], v[136:139], v[176:191]\n"
      "ds_read_b64_tr_b16 v[42:43], v13 offset:3072\n"
      "ds_read_b64_tr_b16 v[44:45], v12 offset:1536\n"
      "v_exp_f32 v204, v172\n"
      "ds_read_b64_tr_b16 v[46:47], v13 offset:3584\n"
      "v_add_f32 v17, v17, v202\n"
      "v_mfma_f32_32x32x16_bf16 v[176:191], v[236:239], v[140:143], v[176:191]\n"
      "ds_read_b64_tr_b16 v[48:49], v12 offset:4096\n"
      "ds_read_b64_tr_b16 v[50:51], v13 offset:6144\n"
      "v_exp_f32 v205, v173\n"
      "v_add_f32 v18, v18, v203\n"
      "v_mfma_f32_32x32x16_bf16 v[176:191], v[240:243], v[144:147], v[176:191]\n"
      "ds_read_b64_tr_b16 v[52:53], v12 offset:4608\n"
      "ds_read_b64_tr_b16 v[54:55], v13 offset:6656\n"
      "v_exp_f32 v206, v174\n"
      "v_add_f32 v17, v17, v204\n"
      "v_cvt_pk_bf16_f32 v28, v200, v201\n"
      "v_mfma_f32_32x32x16_bf16 v[176:191], v[244:247], v[148:151], v[176:191]\n"
      "s_waitcnt lgkmcnt(8)\n"
      "ds_read_b64_tr_b16 v[56:57], v12 offset:5120\n"
      "ds_read_b64_tr_b16 v[58:59], v13 offset:7168\n"
      "v_exp_f32 v207, v175\n"
      "v_add_f32 v18, v18, v205\n"
      "v_mfma_f32_32x32x16_bf16 v[176:191], v[248:251], v[152:155], v[176:191]\n"
      "ds_read_b64_tr_b16 v[60:61], v12 offset:5632\n"
      "ds_read_b64_tr_b16 v[62:63], v13 offset:7680\n"
      "v_add_f32 v17, v17, v206\n"
      "v_cvt_pk_bf16_f32 v29, v202, v203\n"
      "v_add_f32 v18, v18, v207\n"
      "v_cvt_pk_bf16_f32 v30, v204, v205\n"
      "v_mfma_f32_32x32x16_bf16 v[176:191], v[252:255], v[156:159], v[176:191]\n"
      "v_cvt_pk_bf16_f32 v31, v206, v207\n"
      "v_add_f32 v17, v17, v18\n"
      "s_nop 0\n"
      "v_cmp_lt_f32 vcc, s67, v17\n"
      "s_cbranch_vccz .Lok12_%=\n"
      "s_swappc_b64 s[62:63], s[64:65]\n"
      ".Lok12_%=:\n"
      "v_add_f32 v16, v16, v17\n"
      "s_waitcnt lgkmcnt(0)\n"
      "s_barrier\n"
      "v_mfma_f32_32x32x16_bf16 v[64:79], v[32:35], v[24:27], v[64:79]\n"
      "ds_read_b128 v[224:227], v10 offset:16384\n"
      "ds_read_b128 v[228:231], v11 offset:16384\n"
      "ds_read_b128 v[232:235], v10 offset:16896\n"
      "ds_read_b128 v[236:239], v11 offset:16896\n"
      "v_mfma_f32_32x32x16_bf16 v[80:95], v[36:39], v[24:27], v[80:95]\n"
      "v_exp_f32 v192, v176\n"
      "ds_read_b128 v[240:243], v10 offset:17408\n"
      "v_exp_f32 v193, v177\n"
      "ds_read_b128 v[244:247], v11 offset:17408\n"
      "v_mfma_f32_32x32x16_bf16 v[96:111], v[40:43], v[24:27], v[96:111]\n"
      "v_exp_f32 v194, v178\n"
      "ds_read_b128 v[248:251], v10 offset:17920\n"
      "v_exp_f32 v195, v179\n"
      "ds_read_b128 v[252:255], v11 offset:17920\n"
      "v_mfma_f32_32x32x16_bf16 v[112:127], v[44:47], v[24:27], v[112:127]\n"
      "ds_read_b64_tr_b16 a[112:113], v14 offset:57344\n"
      "v_exp_f32 v196, v180\n"
      "ds_read_b64_tr_b16 a[116:117], v14 offset:57352\n"
      "v_add_f32 v17, v192, v194\n"
      "v_add_f32 v18, v193, v195\n"
      "v_mfma_f32_32x32x16_bf16 v[64:79], v[48:51], v[28:31], v[64:79]\n"
      "ds_read_b64_tr_b16 a[120:121], v15 offset:57344\n"
      "v_exp_f32 v197, v181\n"
      "ds_read_b64_tr_b16 a[124:125], v15 offset:57352\n"
      "v_add_f32 v17, v17, v196\n"
      "v_cvt_pk_bf16_f32 v24, v192, v193\n"
      "v_mfma_f32_32x32x16_bf16 v[80:95], v[52:55], v[28:31], v[80:95]\n"
      "v_exp_f32 v198, v182\n"
      "v_add_f32 v18, v18, v197\n"
      "v_exp_f32 v199, v183\n"
      "v_cvt_pk_bf16_f32 v25, v194, v195\n"
      "v_mfma_f32_32x32x16_bf16 v[96:111], v[56:59], v[28:31], v[96:111]\n"
      "v_exp_f32 v200, v184\n"
      "v_add_f32 v17, v17, v198\n"
      "v_exp_f32 v201, v185\n"
      "v_cvt_pk_bf16_f32 v26, v196, v197\n"
      "v_mfma_f32_32x32x16_bf16 v[112:127], v[60:63], v[28:31], v[112:127]\n"
      "v_add_f32 v18, v18, v199\n"
      "v_cvt_pk_bf16_f32 v27, v198, v199\n"
      "s_waitcnt lgkmcnt(0)\n"
      "v_mfma_f32_32x32x16_bf16 v[160:175], v[224:227], v[128:131], v[208:223]\n"
      "ds_read_b64_tr_b16 v[32:33], v12 offset:8192\n"
      "ds_read_b64_tr_b16 v[34:35], v13 offset:10240\n"
      "v_exp_f32 v202, v186\n"
      "ds_read_b64_tr_b16 v[36:37], v12 offset:8704\n"
      "v_add_f32 v17, v17, v200\n"
      "v_mfma_f32_32x32x16_bf16 v[160:175], v[228:231], v[132:135], v[160:175]\n"
      "ds_read_b64_tr_b16 v[38:39], v13 offset:10752\n"
      "ds_read_b64_tr_b16 v[40:41], v12 offset:9216\n"
      "v_exp_f32 v203, v187\n"
      "v_add_f32 v18, v18, v201\n"
      "v_mfma_f32_32x32x16_bf16 v[160:175], v[232:235], v[136:139], v[160:175]\n"
      "ds_read_b64_tr_b16 v[42:43], v13 offset:11264\n"
      "ds_read_b64_tr_b16 v[44:45], v12 offset:9728\n"
      "v_exp_f32 v204, v188\n"
      "ds_read_b64_tr_b16 v[46:47], v13 offset:11776\n"
      "v_add_f32 v17, v17, v202\n"
      "v_mfma_f32_32x32x16_bf16 v[160:175], v[236:239], v[140:143], v[160:175]\n"
      "ds_read_b64_tr_b16 v[48:49], v12 offset:12288\n"
      "ds_read_b64_tr_b16 v[50:51], v13 offset:14336\n"
      "v_exp_f32 v205, v189\n"
      "v_add_f32 v18, v18, v203\n"
      "v_mfma_f32_32x32x16_bf16 v[160:175], v[240:243], v[144:147], v[160:175]\n"
      "ds_read_b64_tr_b16 v[52:53], v12 offset:12800\n"
      "ds_read_b64_tr_b16 v[54:55], v13 offset:14848\n"
      "v_exp_f32 v206, v190\n"
      "v_add_f32 v17, v17, v204\n"
      "v_cvt_pk_bf16_f32 v28, v200, v201\n"
      "v_mfma_f32_32x32x16_bf16 v[160:175], v[244:247], v[148:151], v[160:175]\n"
      "s_waitcnt lgkmcnt(8)\n"
      "ds_read_b64_tr_b16 v[56:57], v12 offset:13312\n"
      "ds_read_b64_tr_b16 v[58:59], v13 offset:15360\n"
      "v_exp_f32 v207, v191\n"
      "v_add_f32 v18, v18, v205\n"
      "v_mfma_f32_32x32x16_bf16 v[160:175], v[248:251], v[152:155], v[160:175]\n"
      "ds_read_b64_tr_b16 v[60:61], v12 offset:13824\n"
      "ds_read_b64_tr_b16 v[62:63], v13 offset:15872\n"
      "v_add_f32 v17, v17, v206\n"
      "v_cvt_pk_bf16_f32 v29, v202, v203\n"
      "v_add_f32 v18, v18, v207\n"
      "v_cvt_pk_bf16_f32 v30, v204, v205\n"
      "v_mfma_f32_32x32x16_bf16 v[160:175], v[252:255], v[156:159], v[160:175]\n"
      "v_cvt_pk_bf16_f32 v31, v206, v207\n"
      "v_add_f32 v17, v17, v18\n"
      "s_nop 0\n"
      "v_cmp_lt_f32 vcc, s67, v17\n"
      "s_cbranch_vccz .Lok13_%=\n"
      "s_swappc_b64 s[62:63], s[92:93]\n"
      ".Lok13_%=:\n"
      "v_add_f32 v16, v16, v17\n"
      "s_waitcnt lgkmcnt(0)\n"
      "s_barrier\n"
      "v_mfma_f32_32x32x16_bf16 v[64:79], v[32:35], v[24:27], v[64:79]\n"
      "ds_read_b128 v[224:227], v10 offset:24576\n"
      "ds_read_b128 v[228:231], v11 offset:24576\n"
      "ds_read_b128 v[232:235], v10 offset:25088\n"
      "ds_read_b128 v[236:239], v11 offset:25088\n"
      "v_mfma_f32_32x32x16_bf16 v[80:95], v[36:39], v[24:27], v[80:95]\n"
      "v_exp_f32 v192, v160\n"
      "ds_read_b128 v[240:243], v10 offset:25600\n"
      "v_exp_f32 v193, v161\n"
      "ds_read_b128 v[244:247], v11 offset:25600\n"
      "v_mfma_f32_32x32x16_bf16 v[96:111], v[40:43], v[24:27], v[96:111]\n"
      "v_exp_f32 v194, v162\n"
      "ds_read_b128 v[248:251], v10 offset:26112\n"
      "v_exp_f32 v195, v163\n"
      "ds_read_b128 v[252:255], v11 offset:26112\n"
      "v_mfma_f32_32x32x16_bf16 v[112:127], v[44:47], v[24:27], v[112:127]\n"
      "ds_read_b64_tr_b16 a[114:115], v14 offset:0\n"
      "v_exp_f32 v196, v164\n"
      "ds_read_b64_tr_b16 a[118:119], v14 offset:8\n"
      "v_add_f32 v17, v192, v194\n"
      "v_add_f32 v18, v193, v195\n"
      "v_mfma_f32_32x32x16_bf16 v[64:79], v[48:51], v[28:31], v[64:79]\n"
      "ds_read_b64_tr_b16 a[122:123], v15 offset:0\n"
      "v_exp_f32 v197, v165\n"
      "ds_read_b64_tr_b16 a[126:127], v15 offset:8\n"
      "v_add_f32 v17, v17, v196\n"
      "v_cvt_pk_bf16_f32 v24, v192, v193\n"
      "v_mfma_f32_32x32x16_bf16 v[80:95], v[52:55], v[28:31], v[80:95]\n"
      "v_exp_f32 v198, v166\n"
      "v_add_f32 v18, v18, v197\n"
      "v_exp_f32 v199, v167\n"
      "v_cvt_pk_bf16_f32 v25, v194, v195\n"
      "v_mfma_f32_32x32x16_bf16 v[96:111], v[56:59], v[28:31], v[96:111]\n"
      "v_exp_f32 v200, v168\n"
      "v_add_f32 v17, v17, v198\n"
      "v_exp_f32 v201, v169\n"
      "v_cvt_pk_bf16_f32 v26, v196, v197\n"
      "v_mfma_f32_32x32x16_bf16 v[112:127], v[60:63], v[28:31], v[112:127]\n"
      "v_add_f32 v18, v18, v199\n"
      "v_cvt_pk_bf16_f32 v27, v198, v199\n"
      "s_waitcnt lgkmcnt(0)\n"
      "v_mfma_f32_32x32x16_bf16 v[176:191], v[224:227], v[128:131], v[208:223]\n"
      "ds_read_b64_tr_b16 v[32:33], v12 offset:16384\n"
      "ds_read_b64_tr_b16 v[34:35], v13 offset:18432\n"
      "v_exp_f32 v202, v170\n"
      "ds_read_b64_tr_b16 v[36:37], v12 offset:16896\n"
      "v_add_f32 v17, v17, v200\n"
      "v_mfma_f32_32x32x16_bf16 v[176:191], v[228:231], v[132:135], v[176:191]\n"
      "ds_read_b64_tr_b16 v[38:39], v13 offset:18944\n"
      "ds_read_b64_tr_b16 v[40:41], v12 offset:17408\n"
      "v_exp_f32 v203, v171\n"
      "v_add_f32 v18, v18, v201\n"
      "v_mfma_f32_32x32x16_bf16 v[176:191], v[232:235], v[136:139], v[176:191]\n"
      "ds_read_b64_tr_b16 v[42:43], v13 offset:19456\n"
      "ds_read_b64_tr_b16 v[44:45], v12 offset:17920\n"
      "v_exp_f32 v204, v172\n"
      "ds_read_b64_tr_b16 v[46:47], v13 offset:19968\n"
      "v_add_f32 v17, v17, v202\n"
      "v_mfma_f32_32x32x16_bf16 v[176:191], v[236:239], v[140:143], v[176:191]\n"
      "ds_read_b64_tr_b16 v[48:49], v12 offset:20480\n"
      "ds_read_b64_tr_b16 v[50:51], v13 offset:22528\n"
      "v_exp_f32 v205, v173\n"
      "v_add_f32 v18, v18, v203\n"
      "v_mfma_f32_32x32x16_bf16 v[176:191], v[240:243], v[144:147], v[176:191]\n"
      "ds_read_b64_tr_b16 v[52:53], v12 offset:20992\n"
      "ds_read_b64_tr_b16 v[54:55], v13 offset:23040\n"
      "v_exp_f32 v206, v174\n"
      "v_add_f32 v17, v17, v204\n"
      "v_cvt_pk_bf16_f32 v28, v200, v201\n"
      "v_mfma_f32_32x32x16_bf16 v[176:191], v[244:247], v[148:151], v[176:191]\n"
      "s_waitcnt lgkmcnt(8)\n"
      "ds_read_b64_tr_b16 v[56:57], v12 offset:21504\n"
      "ds_read_b64_tr_b16 v[58:59], v13 offset:23552\n"
      "v_exp_f32 v207, v175\n"
      "v_add_f32 v18, v18, v205\n"
      "v_mfma_f32_32x32x16_bf16 v[176:191], v[248:251], v[152:155], v[176:191]\n"
      "ds_read_b64_tr_b16 v[60:61], v12 offset:22016\n"
      "ds_read_b64_tr_b16 v[62:63], v13 offset:24064\n"
      "v_add_f32 v17, v17, v206\n"
      "v_cvt_pk_bf16_f32 v29, v202, v203\n"
      "v_add_f32 v18, v18, v207\n"
      "v_cvt_pk_bf16_f32 v30, v204, v205\n"
      "v_mfma_f32_32x32x16_bf16 v[176:191], v[252:255], v[156:159], v[176:191]\n"
      "v_cvt_pk_bf16_f32 v31, v206, v207\n"
      "v_add_f32 v17, v17, v18\n"
      "s_nop 0\n"
      "v_cmp_lt_f32 vcc, s67, v17\n"
      "s_cbranch_vccz .Lok14_%=\n"
      "s_swappc_b64 s[62:63], s[64:65]\n"
      ".Lok14_%=:\n"
      "v_add_f32 v16, v16, v17\n"
      "s_waitcnt lgkmcnt(0)\n"
      "s_barrier\n"
      "v_mfma_f32_32x32x16_bf16 v[64:79], v[32:35], v[24:27], v[64:79]\n"
      "ds_read_b128 v[224:227], v10 offset:32768\n"
      "ds_read_b128 v[228:231], v11 offset:32768\n"
      "ds_read_b128 v[232:235], v10 offset:33280\n"
      "ds_read_b128 v[236:239], v11 offset:33280\n"
      "v_mfma_f32_32x32x16_bf16 v[80:95], v[36:39], v[24:27], v[80:95]\n"
      "v_exp_f32 v192, v176\n"
      "ds_read_b128 v[240:243], v10 offset:33792\n"
      "v_exp_f32 v193, v177\n"
      "ds_read_b128 v[244:247], v11 offset:33792\n"
      "v_mfma_f32_32x32x16_bf16 v[96:111], v[40:43], v[24:27], v[96:111]\n"
      "v_exp_f32 v194, v178\n"
      "ds_read_b128 v[248:251], v10 offset:34304\n"
      "v_exp_f32 v195, v179\n"
      "ds_read_b128 v[252:255], v11 offset:34304\n"
      "v_mfma_f32_32x32x16_bf16 v[112:127], v[44:47], v[24:27], v[112:127]\n"
      "ds_read_b64_tr_b16 a[128:129], v14 offset:8192\n"
      "v_exp_f32 v196, v180\n"
      "ds_read_b64_tr_b16 a[132:133], v14 offset:8200\n"
      "v_add_f32 v17, v192, v194\n"
      "v_add_f32 v18, v193, v195\n"
      "v_mfma_f32_32x32x16_bf16 v[64:79], v[48:51], v[28:31], v[64:79]\n"
      "ds_read_b64_tr_b16 a[136:137], v15 offset:8192\n"
      "v_exp_f32 v197, v181\n"
      "ds_read_b64_tr_b16 a[140:141], v15 offset:8200\n"
      "v_add_f32 v17, v17, v196\n"
      "v_cvt_pk_bf16_f32 v24, v192, v193\n"
      "v_mfma_f32_32x32x16_bf16 v[80:95], v[52:55], v[28:31], v[80:95]\n"
      "v_exp_f32 v198, v182\n"
      "v_add_f32 v18, v18, v197\n"
      "v_exp_f32 v199, v183\n"
      "v_cvt_pk_bf16_f32 v25, v194, v195\n"
      "v_mfma_f32_32x32x16_bf16 v[96:111], v[56:59], v[28:31], v[96:111]\n"
      "v_exp_f32 v200, v184\n"
      "v_add_f32 v17, v17, v198\n"
      "v_exp_f32 v201, v185\n"
      "v_cvt_pk_bf16_f32 v26, v196, v197\n"
      "v_mfma_f32_32x32x16_bf16 v[112:127], v[60:63], v[28:31], v[112:127]\n"
      "v_add_f32 v18, v18, v199\n"
      "v_cvt_pk_bf16_f32 v27, v198, v199\n"
      "s_waitcnt lgkmcnt(0)\n"
      "v_mfma_f32_32x32x16_bf16 v[160:175], v[224:227], v[128:131], v[208:223]\n"
      "ds_read_b64_tr_b16 v[32:33], v12 offset:24576\n"
      "ds_read_b64_tr_b16 v[34:35], v13 offset:26624\n"
      "v_exp_f32 v202, v186\n"
      "ds_read_b64_tr_b16 v[36:37], v12 offset:25088\n"
      "v_add_f32 v17, v17, v200\n"
      "v_mfma_f32_32x32x16_bf16 v[160:175], v[228:231], v[132:135], v[160:175]\n"
      "ds_read_b64_tr_b16 v[38:39], v13 offset:27136\n"
      "ds_read_b64_tr_b16 v[40:41], v12 offset:25600\n"
      "v_exp_f32 v203, v187\n"
      "v_add_f32 v18, v18, v201\n"
      "v_mfma_f32_32x32x16_bf16 v[160:175], v[232:235], v[136:139], v[160:175]\n"
      "ds_read_b64_tr_b16 v[42:43], v13 offset:27648\n"
      "ds_read_b64_tr_b16 v[44:45], v12 offset:26112\n"
      "v_exp_f32 v204, v188\n"
      "ds_read_b64_tr_b16 v[46:47], v13 offset:28160\n"
      "v_add_f32 v17, v17, v202\n"
      "v_mfma_f32_32x32x16_bf16 v[160:175], v[236:239], v[140:143], v[160:175]\n"
      "ds_read_b64_tr_b16 v[48:49], v12 offset:28672\n"
      "ds_read_b64_tr_b16 v[50:51], v13 offset:30720\n"
      "v_exp_f32 v205, v189\n"
      "v_add_f32 v18, v18, v203\n"
      "v_mfma_f32_32x32x16_bf16 v[160:175], v[240:243], v[144:147], v[160:175]\n"
      "ds_read_b64_tr_b16 v[52:53], v12 offset:29184\n"
      "ds_read_b64_tr_b16 v[54:55], v13 offset:31232\n"
      "v_exp_f32 v206, v190\n"
      "v_add_f32 v17, v17, v204\n"
      "v_cvt_pk_bf16_f32 v28, v200, v201\n"
      "v_mfma_f32_32x32x16_bf16 v[160:175], v[244:247], v[148:151], v[160:175]\n"
      "s_waitcnt lgkmcnt(8)\n"
      "ds_read_b64_tr_b16 v[56:57], v12 offset:29696\n"
      "ds_read_b64_tr_b16 v[58:59], v13 offset:31744\n"
      "v_exp_f32 v207, v191\n"
      "v_add_f32 v18, v18, v205\n"
      "v_mfma_f32_32x32x16_bf16 v[160:175], v[248:251], v[152:155], v[160:175]\n"
      "ds_read_b64_tr_b16 v[60:61], v12 offset:30208\n"
      "ds_read_b64_tr_b16 v[62:63], v13 offset:32256\n"
      "v_add_f32 v17, v17, v206\n"
      "v_cvt_pk_bf16_f32 v29, v202, v203\n"
      "v_add_f32 v18, v18, v207\n"
      "v_cvt_pk_bf16_f32 v30, v204, v205\n"
      "v_mfma_f32_32x32x16_bf16 v[160:175], v[252:255], v[156:159], v[160:175]\n"
      "v_cvt_pk_bf16_f32 v31, v206, v207\n"
      "v_add_f32 v17, v17, v18\n"
      "s_nop 0\n"
      "v_cmp_lt_f32 vcc, s67, v17\n"
      "s_cbranch_vccz .Lok15_%=\n"
      "s_swappc_b64 s[62:63], s[92:93]\n"
      ".Lok15_%=:\n"
      "v_add_f32 v16, v16, v17\n"
      "s_waitcnt lgkmcnt(0)\n"
      "s_barrier\n"
      "v_mfma_f32_32x32x16_bf16 v[64:79], v[32:35], v[24:27], v[64:79]\n"
      "ds_read_b128 v[224:227], v10 offset:40960\n"
      "ds_read_b128 v[228:231], v11 offset:40960\n"
      "ds_read_b128 v[232:235], v10 offset:41472\n"
      "ds_read_b128 v[236:239], v11 offset:41472\n"
      "v_mfma_f32_32x32x16_bf16 v[80:95], v[36:39], v[24:27], v[80:95]\n"
      "v_exp_f32 v192, v160\n"
      "ds_read_b128 v[240:243], v10 offset:41984\n"
      "v_exp_f32 v193, v161\n"
      "ds_read_b128 v[244:247], v11 offset:41984\n"
      "v_mfma_f32_32x32x16_bf16 v[96:111], v[40:43], v[24:27], v[96:111]\n"
      "v_exp_f32 v194, v162\n"
      "ds_read_b128 v[248:251], v10 offset:42496\n"
      "v_exp_f32 v195, v163\n"
      "ds_read_b128 v[252:255], v11 offset:42496\n"
      "v_mfma_f32_32x32x16_bf16 v[112:127], v[44:47], v[24:27], v[112:127]\n"
      "ds_read_b64_tr_b16 a[130:131], v14 offset:16384\n"
      "v_exp_f32 v196, v164\n"
      "ds_read_b64_tr_b16 a[134:135], v14 offset:16392\n"
      "v_add_f32 v17, v192, v194\n"
      "v_add_f32 v18, v193, v195\n"
      "v_mfma_f32_32x32x16_bf16 v[64:79], v[48:51], v[28:31], v[64:79]\n"
      "ds_read_b64_tr_b16 a[138:139], v15 offset:16384\n"
      "v_exp_f32 v197, v165\n"
      "ds_read_b64_tr_b16 a[142:143], v15 offset:16392\n"
      "v_add_f32 v17, v17, v196\n"
      "v_cvt_pk_bf16_f32 v24, v192, v193\n"
      "v_mfma_f32_32x32x16_bf16 v[80:95], v[52:55], v[28:31], v[80:95]\n"
      "v_exp_f32 v198, v166\n"
      "v_add_f32 v18, v18, v197\n"
      "v_exp_f32 v199, v167\n"
      "v_cvt_pk_bf16_f32 v25, v194, v195\n"
      "v_mfma_f32_32x32x16_bf16 v[96:111], v[56:59], v[28:31], v[96:111]\n"
      "v_exp_f32 v200, v168\n"
      "v_add_f32 v17, v17, v198\n"
      "v_exp_f32 v201, v169\n"
      "v_cvt_pk_bf16_f32 v26, v196, v197\n"
      "v_mfma_f32_32x32x16_bf16 v[112:127], v[60:63], v[28:31], v[112:127]\n"
      "v_add_f32 v18, v18, v199\n"
      "v_cvt_pk_bf16_f32 v27, v198, v199\n"
      "s_waitcnt lgkmcnt(0)\n"
      "v_mfma_f32_32x32x16_bf16 v[176:191], v[224:227], v[128:131], v[208:223]\n"
      "ds_read_b64_tr_b16 v[32:33], v12 offset:32768\n"
      "ds_read_b64_tr_b16 v[34:35], v13 offset:34816\n"
      "v_exp_f32 v202, v170\n"
      "ds_read_b64_tr_b16 v[36:37], v12 offset:33280\n"
      "v_add_f32 v17, v17, v200\n"
      "v_mfma_f32_32x32x16_bf16 v[176:191], v[228:231], v[132:135], v[176:191]\n"
      "ds_read_b64_tr_b16 v[38:39], v13 offset:35328\n"
      "ds_read_b64_tr_b16 v[40:41], v12 offset:33792\n"
      "v_exp_f32 v203, v171\n"
      "v_add_f32 v18, v18, v201\n"
      "v_mfma_f32_32x32x16_bf16 v[176:191], v[232:235], v[136:139], v[176:191]\n"
      "ds_read_b64_tr_b16 v[42:43], v13 offset:35840\n"
      "ds_read_b64_tr_b16 v[44:45], v12 offset:34304\n"
      "v_exp_f32 v204, v172\n"
      "ds_read_b64_tr_b16 v[46:47], v13 offset:36352\n"
      "v_add_f32 v17, v17, v202\n"
      "v_mfma_f32_32x32x16_bf16 v[176:191], v[236:239], v[140:143], v[176:191]\n"
      "ds_read_b64_tr_b16 v[48:49], v12 offset:36864\n"
      "ds_read_b64_tr_b16 v[50:51], v13 offset:38912\n"
      "v_exp_f32 v205, v173\n"
      "v_add_f32 v18, v18, v203\n"
      "v_mfma_f32_32x32x16_bf16 v[176:191], v[240:243], v[144:147], v[176:191]\n"
      "ds_read_b64_tr_b16 v[52:53], v12 offset:37376\n"
      "ds_read_b64_tr_b16 v[54:55], v13 offset:39424\n"
      "v_exp_f32 v206, v174\n"
      "v_add_f32 v17, v17, v204\n"
      "v_cvt_pk_bf16_f32 v28, v200, v201\n"
      "v_mfma_f32_32x32x16_bf16 v[176:191], v[244:247], v[148:151], v[176:191]\n"
      "s_waitcnt lgkmcnt(8)\n"
      "ds_read_b64_tr_b16 v[56:57], v12 offset:37888\n"
      "ds_read_b64_tr_b16 v[58:59], v13 offset:39936\n"
      "v_exp_f32 v207, v175\n"
      "v_add_f32 v18, v18, v205\n"
      "v_mfma_f32_32x32x16_bf16 v[176:191], v[248:251], v[152:155], v[176:191]\n"
      "ds_read_b64_tr_b16 v[60:61], v12 offset:38400\n"
      "ds_read_b64_tr_b16 v[62:63], v13 offset:40448\n"
      "v_add_f32 v17, v17, v206\n"
      "v_cvt_pk_bf16_f32 v29, v202, v203\n"
      "v_add_f32 v18, v18, v207\n"
      "v_cvt_pk_bf16_f32 v30, v204, v205\n"
      "v_mfma_f32_32x32x16_bf16 v[176:191], v[252:255], v[156:159], v[176:191]\n"
      "v_cvt_pk_bf16_f32 v31, v206, v207\n"
      "v_add_f32 v17, v17, v18\n"
      "s_nop 0\n"
      "v_cmp_lt_f32 vcc, s67, v17\n"
      "s_cbranch_vccz .Lok16_%=\n"
      "s_swappc_b64 s[62:63], s[64:65]\n"
      ".Lok16_%=:\n"
      "v_add_f32 v16, v16, v17\n"
      "s_waitcnt lgkmcnt(0)\n"
      "s_barrier\n"
      "v_mfma_f32_32x32x16_bf16 v[64:79], v[32:35], v[24:27], v[64:79]\n"
      "ds_read_b128 v[224:227], v10 offset:0\n"
      "ds_read_b128 v[228:231], v11 offset:0\n"
      "ds_read_b128 v[232:235], v10 offset:512\n"
      "ds_read_b128 v[236:239], v11 offset:512\n"
      "v_mfma_f32_32x32x16_bf16 v[80:95], v[36:39], v[24:27], v[80:95]\n"
      "v_exp_f32 v192, v176\n"
      "ds_read_b128 v[240:243], v10 offset:1024\n"
      "v_exp_f32 v193, v177\n"
      "ds_read_b128 v[244:247], v11 offset:1024\n"
      "v_mfma_f32_32x32x16_bf16 v[96:111], v[40:43], v[24:27], v[96:111]\n"
      "v_exp_f32 v194, v178\n"
      "ds_read_b128 v[248:251], v10 offset:1536\n"
      "v_exp_f32 v195, v179\n"
      "ds_read_b128 v[252:255], v11 offset:1536\n"
      "v_mfma_f32_32x32x16_bf16 v[112:127], v[44:47], v[24:27], v[112:127]\n"
      "ds_read_b64_tr_b16 a[144:145], v14 offset:40960\n"
      "v_exp_f32 v196, v180\n"
      "ds_read_b64_tr_b16 a[148:149], v14 offset:40968\n"
      "v_add_f32 v17, v192, v194\n"
      "v_add_f32 v18, v193, v195\n"
      "v_mfma_f32_32x32x16_bf16 v[64:79], v[48:51], v[28:31], v[64:79]\n"
      "ds_read_b64_tr_b16 a[152:153], v15 offset:40960\n"
      "v_exp_f32 v197, v181\n"
      "ds_read_b64_tr_b16 a[156:157], v15 offset:40968\n"
      "v_add_f32 v17, v17, v196\n"
      "v_cvt_pk_bf16_f32 v24, v192, v193\n"
      "v_mfma_f32_32x32x16_bf16 v[80:95], v[52:55], v[28:31], v[80:95]\n"
      "v_exp_f32 v198, v182\n"
      "v_add_f32 v18, v18, v197\n"
      "v_exp_f32 v199, v183\n"
      "v_cvt_pk_bf16_f32 v25, v194, v195\n"
      "v_mfma_f32_32x32x16_bf16 v[96:111], v[56:59], v[28:31], v[96:111]\n"
      "v_exp_f32 v200, v184\n"
      "v_add_f32 v17, v17, v198\n"
      "v_exp_f32 v201, v185\n"
      "v_cvt_pk_bf16_f32 v26, v196, v197\n"
      "v_mfma_f32_32x32x16_bf16 v[112:127], v[60:63], v[28:31], v[112:127]\n"
      "v_add_f32 v18, v18, v199\n"
      "v_cvt_pk_bf16_f32 v27, v198, v199\n"
      "s_waitcnt lgkmcnt(0)\n"
      "v_mfma_f32_32x32x16_bf16 v[160:175], v[224:227], v[128:131], v[208:223]\n"
      "ds_read_b64_tr_b16 v[32:33], v12 offset:40960\n"
      "ds_read_b64_tr_b16 v[34:35], v13 offset:43008\n"
      "v_exp_f32 v202, v186\n"
      "ds_read_b64_tr_b16 v[36:37], v12 offset:41472\n"
      "v_add_f32 v17, v17, v200\n"
      "v_mfma_f32_32x32x16_bf16 v[160:175], v[228:231], v[132:135], v[160:175]\n"
      "ds_read_b64_tr_b16 v[38:39], v13 offset:43520\n"
      "ds_read_b64_tr_b16 v[40:41], v12 offset:41984\n"
      "v_exp_f32 v203, v187\n"
      "v_add_f32 v18, v18, v201\n"
      "v_mfma_f32_32x32x16_bf16 v[160:175], v[232:235], v[136:139], v[160:175]\n"
      "ds_read_b64_tr_b16 v[42:43], v13 offset:44032\n"
      "ds_read_b64_tr_b16 v[44:45], v12 offset:42496\n"
      "v_exp_f32 v204, v188\n"
      "ds_read_b64_tr_b16 v[46:47], v13 offset:44544\n"
      "v_add_f32 v17, v17, v202\n"
      "v_mfma_f32_32x32x16_bf16 v[160:175], v[236:239], v[140:143], v[160:175]\n"
      "ds_read_b64_tr_b16 v[48:49], v12 offset:45056\n"
      "ds_read_b64_tr_b16 v[50:51], v13 offset:47104\n"
      "v_exp_f32 v205, v189\n"
      "v_add_f32 v18, v18, v203\n"
      "v_mfma_f32_32x32x16_bf16 v[160:175], v[240:243], v[144:147], v[160:175]\n"
      "ds_read_b64_tr_b16 v[52:53], v12 offset:45568\n"
      "ds_read_b64_tr_b16 v[54:55], v13 offset:47616\n"
      "v_exp_f32 v206, v190\n"
      "v_add_f32 v17, v17, v204\n"
      "v_cvt_pk_bf16_f32 v28, v200, v201\n"
      "v_mfma_f32_32x32x16_bf16 v[160:175], v[244:247], v[148:151], v[160:175]\n"
      "s_waitcnt lgkmcnt(8)\n"
      "ds_read_b64_tr_b16 v[56:57], v12 offset:46080\n"
      "ds_read_b64_tr_b16 v[58:59], v13 offset:48128\n"
      "v_exp_f32 v207, v191\n"
      "v_add_f32 v18, v18, v205\n"
      "v_mfma_f32_32x32x16_bf16 v[160:175], v[248:251], v[152:155], v[160:175]\n"
      "ds_read_b64_tr_b16 v[60:61], v12 offset:46592\n"
      "ds_read_b64_tr_b16 v[62:63], v13 offset:48640\n"
      "v_add_f32 v17, v17, v206\n"
      "v_cvt_pk_bf16_f32 v29, v202, v203\n"
      "v_add_f32 v18, v18, v207\n"
      "v_cvt_pk_bf16_f32 v30, v204, v205\n"
      "v_mfma_f32_32x32x16_bf16 v[160:175], v[252:255], v[156:159], v[160:175]\n"
      "v_cvt_pk_bf16_f32 v31, v206, v207\n"
      "v_add_f32 v17, v17, v18\n"
      "s_nop 0\n"
      "v_cmp_lt_f32 vcc, s67, v17\n"
      "s_cbranch_vccz .Lok17_%=\n"
      "s_swappc_b64 s[62:63], s[92:93]\n"
      ".Lok17_%=:\n"
      "v_add_f32 v16, v16, v17\n"
      "s_waitcnt lgkmcnt(0)\n"
      "s_barrier\n"
      "v_mfma_f32_32x32x16_bf16 v[64:79], v[32:35], v[24:27], v[64:79]\n"
      "ds_read_b128 v[224:227], v10 offset:8192\n"
      "ds_read_b128 v[228:231], v11 offset:8192\n"
      "ds_read_b128 v[232:235], v10 offset:8704\n"
      "ds_read_b128 v[236:239], v11 offset:8704\n"
      "v_mfma_f32_32x32x16_bf16 v[80:95], v[36:39], v[24:27], v[80:95]\n"
      "v_exp_f32 v192, v160\n"
      "ds_read_b128 v[240:243], v10 offset:9216\n"
      "v_exp_f32 v193, v161\n"
      "ds_read_b128 v[244:247], v11 offset:9216\n"
      "v_mfma_f32_32x32x16_bf16 v[96:111], v[40:43], v[24:27], v[96:111]\n"
      "v_exp_f32 v194, v162\n"
      "ds_read_b128 v[248:251], v10 offset:9728\n"
      "v_exp_f32 v195, v163\n"
      "ds_read_b128 v[252:255], v11 offset:9728\n"
      "v_mfma_f32_32x32x16_bf16 v[112:127], v[44:47], v[24:27], v[112:127]\n"
      "ds_read_b64_tr_b16 a[146:147], v14 offset:24576\n"
      "v_exp_f32 v196, v164\n"
      "ds_read_b64_tr_b16 a[150:151], v14 offset:24584\n"
      "v_add_f32 v17, v192, v194\n"
      "v_add_f32 v18, v193, v195\n"
      "v_mfma_f32_32x32x16_bf16 v[64:79], v[48:51], v[28:31], v[64:79]\n"
      "ds_read_b64_tr_b16 a[154:155], v15 offset:24576\n"
      "v_exp_f32 v197, v165\n"
      "ds_read_b64_tr_b16 a[158:159], v15 offset:24584\n"
      "v_add_f32 v17, v17, v196\n"
      "v_cvt_pk_bf16_f32 v24, v192, v193\n"
      "v_mfma_f32_32x32x16_bf16 v[80:95], v[52:55], v[28:31], v[80:95]\n"
      "v_exp_f32 v198, v166\n"
      "v_add_f32 v18, v18, v197\n"
      "v_exp_f32 v199, v167\n"
      "v_cvt_pk_bf16_f32 v25, v194, v195\n"
      "v_mfma_f32_32x32x16_bf16 v[96:111], v[56:59], v[28:31], v[96:111]\n"
      "v_exp_f32 v200, v168\n"
      "v_add_f32 v17, v17, v198\n"
      "v_exp_f32 v201, v169\n"
      "v_cvt_pk_bf16_f32 v26, v196, v197\n"
      "v_mfma_f32_32x32x16_bf16 v[112:127], v[60:63], v[28:31], v[112:127]\n"
      "v_add_f32 v18, v18, v199\n"
      "v_cvt_pk_bf16_f32 v27, v198, v199\n"
      "s_waitcnt lgkmcnt(0)\n"
      "v_mfma_f32_32x32x16_bf16 v[176:191], v[224:227], v[128:131], v[208:223]\n"
      "ds_read_b64_tr_b16 v[32:33], v12 offset:0\n"
      "ds_read_b64_tr_b16 v[34:35], v13 offset:2048\n"
      "v_exp_f32 v202, v170\n"
      "ds_read_b64_tr_b16 v[36:37], v12 offset:512\n"
      "v_add_f32 v17, v17, v200\n"
      "v_mfma_f32_32x32x16_bf16 v[176:191], v[228:231], v[132:135], v[176:191]\n"
      "ds_read_b64_tr_b16 v[38:39], v13 offset:2560\n"
      "ds_read_b64_tr_b16 v[40:41], v12 offset:1024\n"
      "v_exp_f32 v203, v171\n"
      "v_add_f32 v18, v18, v201\n"
      "v_mfma_f32_32x32x16_bf16 v[176:191], v[232:235], v[136:139], v[176:191]\n"
      "ds_read_b64_tr_b16 v[42:43], v13 offset:3072\n"
      "ds_read_b64_tr_b16 v[44:45], v12 offset:1536\n"
      "v_exp_f32 v204, v172\n"
      "ds_read_b64_tr_b16 v[46:47], v13 offset:3584\n"
      "v_add_f32 v17, v17, v202\n"
      "v_mfma_f32_32x32x16_bf16 v[176:191], v[236:239], v[140:143], v[176:191]\n"
      "ds_read_b64_tr_b16 v[48:49], v12 offset:4096\n"
      "ds_read_b64_tr_b16 v[50:51], v13 offset:6144\n"
      "v_exp_f32 v205, v173\n"
      "v_add_f32 v18, v18, v203\n"
      "v_mfma_f32_32x32x16_bf16 v[176:191], v[240:243], v[144:147], v[176:191]\n"
      "ds_read_b64_tr_b16 v[52:53], v12 offset:4608\n"
      "ds_read_b64_tr_b16 v[54:55], v13 offset:6656\n"
      "v_exp_f32 v206, v174\n"
      "v_add_f32 v17, v17, v204\n"
      "v_cvt_pk_bf16_f32 v28, v200, v201\n"
      "v_mfma_f32_32x32x16_bf16 v[176:191], v[244:247], v[148:151], v[176:191]\n"
      "s_waitcnt lgkmcnt(8)\n"
      "ds_read_b64_tr_b16 v[56:57], v12 offset:5120\n"
      "ds_read_b64_tr_b16 v[58:59], v13 offset:7168\n"
      "v_exp_f32 v207, v175\n"
      "v_add_f32 v18, v18, v205\n"
      "v_mfma_f32_32x32x16_bf16 v[176:191], v[248:251], v[152:155], v[176:191]\n"
      "ds_read_b64_tr_b16 v[60:61], v12 offset:5632\n"
      "ds_read_b64_tr_b16 v[62:63], v13 offset:7680\n"
      "v_add_f32 v17, v17, v206\n"
      "v_cvt_pk_bf16_f32 v29, v202, v203\n"
      "v_add_f32 v18, v18, v207\n"
      "v_cvt_pk_bf16_f32 v30, v204, v205\n"
      "v_mfma_f32_32x32x16_bf16 v[176:191], v[252:255], v[156:159], v[176:191]\n"
      "v_cvt_pk_bf16_f32 v31, v206, v207\n"
      "v_add_f32 v17, v17, v18\n"
      "s_nop 0\n"
      "v_cmp_lt_f32 vcc, s67, v17\n"
      "s_cbranch_vccz .Lok18_%=\n"
      "s_swappc_b64 s[62:63], s[64:65]\n"
      ".Lok18_%=:\n"
      "v_add_f32 v16, v16, v17\n"
      "s_waitcnt lgkmcnt(0)\n"
      "s_barrier\n"
      "v_mfma_f32_32x32x16_bf16 v[64:79], v[32:35], v[24:27], v[64:79]\n"
      "ds_read_b128 v[224:227], v10 offset:16384\n"
      "ds_read_b128 v[228:231], v11 offset:16384\n"
      "ds_read_b128 v[232:235], v10 offset:16896\n"
      "ds_read_b128 v[236:239], v11 offset:16896\n"
      "v_mfma_f32_32x32x16_bf16 v[80:95], v[36:39], v[24:27], v[80:95]\n"
      "v_exp_f32 v192, v176\n"
      "ds_read_b128 v[240:243], v10 offset:17408\n"
      "v_exp_f32 v193, v177\n"
      "ds_read_b128 v[244:247], v11 offset:17408\n"
      "v_mfma_f32_32x32x16_bf16 v[96:111], v[40:43], v[24:27], v[96:111]\n"
      "v_exp_f32 v194, v178\n"
      "ds_read_b128 v[248:251], v10 offset:17920\n"
      "v_exp_f32 v195, v179\n"
      "ds_read_b128 v[252:255], v11 offset:17920\n"
      "v_mfma_f32_32x32x16_bf16 v[112:127], v[44:47], v[24:27], v[112:127]\n"
      "ds_read_b64_tr_b16 a[160:161], v14 offset:32768\n"
      "v_exp_f32 v196, v180\n"
      "ds_read_b64_tr_b16 a[164:165], v14 offset:32776\n"
      "v_add_f32 v17, v192, v194\n"
      "v_add_f32 v18, v193, v195\n"
      "v_mfma_f32_32x32x16_bf16 v[64:79], v[48:51], v[28:31], v[64:79]\n"
      "ds_read_b64_tr_b16 a[168:169], v15 offset:32768\n"
      "v_exp_f32 v197, v181\n"
      "ds_read_b64_tr_b16 a[172:173], v15 offset:32776\n"
      "v_add_f32 v17, v17, v196\n"
      "v_cvt_pk_bf16_f32 v24, v192, v193\n"
      "v_mfma_f32_32x32x16_bf16 v[80:95], v[52:55], v[28:31], v[80:95]\n"
      "v_exp_f32 v198, v182\n"
      "v_add_f32 v18, v18, v197\n"
      "v_exp_f32 v199, v183\n"
      "v_cvt_pk_bf16_f32 v25, v194, v195\n"
      "v_mfma_f32_32x32x16_bf16 v[96:111], v[56:59], v[28:31], v[96:111]\n"
      "v_exp_f32 v200, v184\n"
      "v_add_f32 v17, v17, v198\n"
      "v_exp_f32 v201, v185\n"
      "v_cvt_pk_bf16_f32 v26, v196, v197\n"
      "v_mfma_f32_32x32x16_bf16 v[112:127], v[60:63], v[28:31], v[112:127]\n"
      "v_add_f32 v18, v18, v199\n"
      "v_cvt_pk_bf16_f32 v27, v198, v199\n"
      "s_waitcnt lgkmcnt(0)\n"
      "v_mfma_f32_32x32x16_bf16 v[160:175], v[224:227], v[128:131], v[208:223]\n"
      "ds_read_b64_tr_b16 v[32:33], v12 offset:8192\n"
      "ds_read_b64_tr_b16 v[34:35], v13 offset:10240\n"
      "v_exp_f32 v202, v186\n"
      "ds_read_b64_tr_b16 v[36:37], v12 offset:8704\n"
      "v_add_f32 v17, v17, v200\n"
      "v_mfma_f32_32x32x16_bf16 v[160:175], v[228:231], v[132:135], v[160:175]\n"
      "ds_read_b64_tr_b16 v[38:39], v13 offset:10752\n"
      "ds_read_b64_tr_b16 v[40:41], v12 offset:9216\n"
      "v_exp_f32 v203, v187\n"
      "v_add_f32 v18, v18, v201\n"
      "v_mfma_f32_32x32x16_bf16 v[160:175], v[232:235], v[136:139], v[160:175]\n"
      "ds_read_b64_tr_b16 v[42:43], v13 offset:11264\n"
      "ds_read_b64_tr_b16 v[44:45], v12 offset:9728\n"
      "v_exp_f32 v204, v188\n"
      "ds_read_b64_tr_b16 v[46:47], v13 offset:11776\n"
      "v_add_f32 v17, v17, v202\n"
      "v_mfma_f32_32x32x16_bf16 v[160:175], v[236:239], v[140:143], v[160:175]\n"
      "ds_read_b64_tr_b16 v[48:49], v12 offset:12288\n"
      "ds_read_b64_tr_b16 v[50:51], v13 offset:14336\n"
      "v_exp_f32 v205, v189\n"
      "v_add_f32 v18, v18, v203\n"
      "v_mfma_f32_32x32x16_bf16 v[160:175], v[240:243], v[144:147], v[160:175]\n"
      "ds_read_b64_tr_b16 v[52:53], v12 offset:12800\n"
      "ds_read_b64_tr_b16 v[54:55], v13 offset:14848\n"
      "v_exp_f32 v206, v190\n"
      "v_add_f32 v17, v17, v204\n"
      "v_cvt_pk_bf16_f32 v28, v200, v201\n"
      "v_mfma_f32_32x32x16_bf16 v[160:175], v[244:247], v[148:151], v[160:175]\n"
      "s_waitcnt lgkmcnt(8)\n"
      "ds_read_b64_tr_b16 v[56:57], v12 offset:13312\n"
      "ds_read_b64_tr_b16 v[58:59], v13 offset:15360\n"
      "v_exp_f32 v207, v191\n"
      "v_add_f32 v18, v18, v205\n"
      "v_mfma_f32_32x32x16_bf16 v[160:175], v[248:251], v[152:155], v[160:175]\n"
      "ds_read_b64_tr_b16 v[60:61], v12 offset:13824\n"
      "ds_read_b64_tr_b16 v[62:63], v13 offset:15872\n"
      "v_add_f32 v17, v17, v206\n"
      "v_cvt_pk_bf16_f32 v29, v202, v203\n"
      "v_add_f32 v18, v18, v207\n"
      "v_cvt_pk_bf16_f32 v30, v204, v205\n"
      "v_mfma_f32_32x32x16_bf16 v[160:175], v[252:255], v[156:159], v[160:175]\n"
      "v_cvt_pk_bf16_f32 v31, v206, v207\n"
      "v_add_f32 v17, v17, v18\n"
      "s_nop 0\n"
      "v_cmp_lt_f32 vcc, s67, v17\n"
      "s_cbranch_vccz .Lok19_%=\n"
      "s_swappc_b64 s[62:63], s[92:93]\n"
      ".Lok19_%=:\n"
      "v_add_f32 v16, v16, v17\n"
      "s_waitcnt lgkmcnt(0)\n"
      "s_barrier\n"
      "v_mfma_f32_32x32x16_bf16 v[64:79], v[32:35], v[24:27], v[64:79]\n"
      "ds_read_b128 v[224:227], v10 offset:24576\n"
      "ds_read_b128 v[228:231], v11 offset:24576\n"
      "ds_read_b128 v[232:235], v10 offset:25088\n"
      "ds_read_b128 v[236:239], v11 offset:25088\n"
      "v_mfma_f32_32x32x16_bf16 v[80:95], v[36:39], v[24:27], v[80:95]\n"
      "v_exp_f32 v192, v160\n"
      "ds_read_b128 v[240:243], v10 offset:25600\n"
      "v_exp_f32 v193, v161\n"
      "ds_read_b128 v[244:247], v11 offset:25600\n"
      "v_mfma_f32_32x32x16_bf16 v[96:111], v[40:43], v[24:27], v[96:111]\n"
      "v_exp_f32 v194, v162\n"
      "ds_read_b128 v[248:251], v10 offset:26112\n"
      "v_exp_f32 v195, v163\n"
      "ds_read_b128 v[252:255], v11 offset:26112\n"
      "v_mfma_f32_32x32x16_bf16 v[112:127], v[44:47], v[24:27], v[112:127]\n"
      "ds_read_b64_tr_b16 a[162:163], v14 offset:49152\n"
      "v_exp_f32 v196, v164\n"
      "ds_read_b64_tr_b16 a[166:167], v14 offset:49160\n"
      "v_add_f32 v17, v192, v194\n"
      "v_add_f32 v18, v193, v195\n"
      "v_mfma_f32_32x32x16_bf16 v[64:79], v[48:51], v[28:31], v[64:79]\n"
      "ds_read_b64_tr_b16 a[170:171], v15 offset:49152\n"
      "v_exp_f32 v197, v165\n"
      "ds_read_b64_tr_b16 a[174:175], v15 offset:49160\n"
      "v_add_f32 v17, v17, v196\n"
      "v_cvt_pk_bf16_f32 v24, v192, v193\n"
      "v_mfma_f32_32x32x16_bf16 v[80:95], v[52:55], v[28:31], v[80:95]\n"
      "v_exp_f32 v198, v166\n"
      "v_add_f32 v18, v18, v197\n"
      "v_exp_f32 v199, v167\n"
      "v_cvt_pk_bf16_f32 v25, v194, v195\n"
      "v_mfma_f32_32x32x16_bf16 v[96:111], v[56:59], v[28:31], v[96:111]\n"
      "v_exp_f32 v200, v168\n"
      "v_add_f32 v17, v17, v198\n"
      "v_exp_f32 v201, v169\n"
      "v_cvt_pk_bf16_f32 v26, v196, v197\n"
      "v_mfma_f32_32x32x16_bf16 v[112:127], v[60:63], v[28:31], v[112:127]\n"
      "v_add_f32 v18, v18, v199\n"
      "v_cvt_pk_bf16_f32 v27, v198, v199\n"
      "s_waitcnt lgkmcnt(0)\n"
      "v_mfma_f32_32x32x16_bf16 v[176:191], v[224:227], v[128:131], v[208:223]\n"
      "ds_read_b64_tr_b16 v[32:33], v12 offset:16384\n"
      "ds_read_b64_tr_b16 v[34:35], v13 offset:18432\n"
      "v_exp_f32 v202, v170\n"
      "ds_read_b64_tr_b16 v[36:37], v12 offset:16896\n"
      "v_add_f32 v17, v17, v200\n"
      "v_mfma_f32_32x32x16_bf16 v[176:191], v[228:231], v[132:135], v[176:191]\n"
      "ds_read_b64_tr_b16 v[38:39], v13 offset:18944\n"
      "ds_read_b64_tr_b16 v[40:41], v12 offset:17408\n"
      "v_exp_f32 v203, v171\n"
      "v_add_f32 v18, v18, v201\n"
      "v_mfma_f32_32x32x16_bf16 v[176:191], v[232:235], v[136:139], v[176:191]\n"
      "ds_read_b64_tr_b16 v[42:43], v13 offset:19456\n"
      "ds_read_b64_tr_b16 v[44:45], v12 offset:17920\n"
      "v_exp_f32 v204, v172\n"
      "ds_read_b64_tr_b16 v[46:47], v13 offset:19968\n"
      "v_add_f32 v17, v17, v202\n"
      "v_mfma_f32_32x32x16_bf16 v[176:191], v[236:239], v[140:143], v[176:191]\n"
      "ds_read_b64_tr_b16 v[48:49], v12 offset:20480\n"
      "ds_read_b64_tr_b16 v[50:51], v13 offset:22528\n"
      "v_exp_f32 v205, v173\n"
      "v_add_f32 v18, v18, v203\n"
      "v_mfma_f32_32x32x16_bf16 v[176:191], v[240:243], v[144:147], v[176:191]\n"
      "ds_read_b64_tr_b16 v[52:53], v12 offset:20992\n"
      "ds_read_b64_tr_b16 v[54:55], v13 offset:23040\n"
      "v_exp_f32 v206, v174\n"
      "v_add_f32 v17, v17, v204\n"
      "v_cvt_pk_bf16_f32 v28, v200, v201\n"
      "v_mfma_f32_32x32x16_bf16 v[176:191], v[244:247], v[148:151], v[176:191]\n"
      "s_waitcnt lgkmcnt(8)\n"
      "ds_read_b64_tr_b16 v[56:57], v12 offset:21504\n"
      "ds_read_b64_tr_b16 v[58:59], v13 offset:23552\n"
      "v_exp_f32 v207, v175\n"
      "v_add_f32 v18, v18, v205\n"
      "v_mfma_f32_32x32x16_bf16 v[176:191], v[248:251], v[152:155], v[176:191]\n"
      "ds_read_b64_tr_b16 v[60:61], v12 offset:22016\n"
      "ds_read_b64_tr_b16 v[62:63], v13 offset:24064\n"
      "v_add_f32 v17, v17, v206\n"
      "v_cvt_pk_bf16_f32 v29, v202, v203\n"
      "v_add_f32 v18, v18, v207\n"
      "v_cvt_pk_bf16_f32 v30, v204, v205\n"
      "v_mfma_f32_32x32x16_bf16 v[176:191], v[252:255], v[156:159], v[176:191]\n"
      "v_cvt_pk_bf16_f32 v31, v206, v207\n"
      "v_add_f32 v17, v17, v18\n"
      "s_nop 0\n"
      "v_cmp_lt_f32 vcc, s67, v17\n"
      "s_cbranch_vccz .Lok20_%=\n"
      "s_swappc_b64 s[62:63], s[64:65]\n"
      ".Lok20_%=:\n"
      "v_add_f32 v16, v16, v17\n"
      "s_waitcnt lgkmcnt(0)\n"
      "s_barrier\n"
      "v_mfma_f32_32x32x16_bf16 v[64:79], v[32:35], v[24:27], v[64:79]\n"
      "ds_read_b128 v[224:227], v10 offset:32768\n"
      "ds_read_b128 v[228:231], v11 offset:32768\n"
      "ds_read_b128 v[232:235], v10 offset:33280\n"
      "ds_read_b128 v[236:239], v11 offset:33280\n"
      "v_mfma_f32_32x32x16_bf16 v[80:95], v[36:39], v[24:27], v[80:95]\n"
      "v_exp_f32 v192, v176\n"
      "ds_read_b128 v[240:243], v10 offset:33792\n"
      "v_exp_f32 v193, v177\n"
      "ds_read_b128 v[244:247], v11 offset:33792\n"
      "v_mfma_f32_32x32x16_bf16 v[96:111], v[40:43], v[24:27], v[96:111]\n"
      "v_exp_f32 v194, v178\n"
      "ds_read_b128 v[248:251], v10 offset:34304\n"
      "v_exp_f32 v195, v179\n"
      "ds_read_b128 v[252:255], v11 offset:34304\n"
      "v_mfma_f32_32x32x16_bf16 v[112:127], v[44:47], v[24:27], v[112:127]\n"
      "ds_read_b64_tr_b16 a[176:177], v14 offset:57344\n"
      "v_exp_f32 v196, v180\n"
      "ds_read_b64_tr_b16 a[180:181], v14 offset:57352\n"
      "v_add_f32 v17, v192, v194\n"
      "v_add_f32 v18, v193, v195\n"
      "v_mfma_f32_32x32x16_bf16 v[64:79], v[48:51], v[28:31], v[64:79]\n"
      "ds_read_b64_tr_b16 a[184:185], v15 offset:57344\n"
      "v_exp_f32 v197, v181\n"
      "ds_read_b64_tr_b16 a[188:189], v15 offset:57352\n"
      "v_add_f32 v17, v17, v196\n"
      "v_cvt_pk_bf16_f32 v24, v192, v193\n"
      "v_mfma_f32_32x32x16_bf16 v[80:95], v[52:55], v[28:31], v[80:95]\n"
      "v_exp_f32 v198, v182\n"
      "v_add_f32 v18, v18, v197\n"
      "v_exp_f32 v199, v183\n"
      "v_cvt_pk_bf16_f32 v25, v194, v195\n"
      "v_mfma_f32_32x32x16_bf16 v[96:111], v[56:59], v[28:31], v[96:111]\n"
      "v_exp_f32 v200, v184\n"
      "v_add_f32 v17, v17, v198\n"
      "v_exp_f32 v201, v185\n"
      "v_cvt_pk_bf16_f32 v26, v196, v197\n"
      "v_mfma_f32_32x32x16_bf16 v[112:127], v[60:63], v[28:31], v[112:127]\n"
      "v_add_f32 v18, v18, v199\n"
      "v_cvt_pk_bf16_f32 v27, v198, v199\n"
      "s_waitcnt lgkmcnt(0)\n"
      "v_mfma_f32_32x32x16_bf16 v[160:175], v[224:227], v[128:131], v[208:223]\n"
      "ds_read_b64_tr_b16 v[32:33], v12 offset:24576\n"
      "ds_read_b64_tr_b16 v[34:35], v13 offset:26624\n"
      "v_exp_f32 v202, v186\n"
      "ds_read_b64_tr_b16 v[36:37], v12 offset:25088\n"
      "v_add_f32 v17, v17, v200\n"
      "v_mfma_f32_32x32x16_bf16 v[160:175], v[228:231], v[132:135], v[160:175]\n"
      "ds_read_b64_tr_b16 v[38:39], v13 offset:27136\n"
      "ds_read_b64_tr_b16 v[40:41], v12 offset:25600\n"
      "v_exp_f32 v203, v187\n"
      "v_add_f32 v18, v18, v201\n"
      "v_mfma_f32_32x32x16_bf16 v[160:175], v[232:235], v[136:139], v[160:175]\n"
      "ds_read_b64_tr_b16 v[42:43], v13 offset:27648\n"
      "ds_read_b64_tr_b16 v[44:45], v12 offset:26112\n"
      "v_exp_f32 v204, v188\n"
      "ds_read_b64_tr_b16 v[46:47], v13 offset:28160\n"
      "v_add_f32 v17, v17, v202\n"
      "v_mfma_f32_32x32x16_bf16 v[160:175], v[236:239], v[140:143], v[160:175]\n"
      "ds_read_b64_tr_b16 v[48:49], v12 offset:28672\n"
      "ds_read_b64_tr_b16 v[50:51], v13 offset:30720\n"
      "v_exp_f32 v205, v189\n"
      "v_add_f32 v18, v18, v203\n"
      "v_mfma_f32_32x32x16_bf16 v[160:175], v[240:243], v[144:147], v[160:175]\n"
      "ds_read_b64_tr_b16 v[52:53], v12 offset:29184\n"
      "ds_read_b64_tr_b16 v[54:55], v13 offset:31232\n"
      "v_exp_f32 v206, v190\n"
      "v_add_f32 v17, v17, v204\n"
      "v_cvt_pk_bf16_f32 v28, v200, v201\n"
      "v_mfma_f32_32x32x16_bf16 v[160:175], v[244:247], v[148:151], v[160:175]\n"
      "s_waitcnt lgkmcnt(8)\n"
      "ds_read_b64_tr_b16 v[56:57], v12 offset:29696\n"
      "ds_read_b64_tr_b16 v[58:59], v13 offset:31744\n"
      "v_exp_f32 v207, v191\n"
      "v_add_f32 v18, v18, v205\n"
      "v_mfma_f32_32x32x16_bf16 v[160:175], v[248:251], v[152:155], v[160:175]\n"
      "ds_read_b64_tr_b16 v[60:61], v12 offset:30208\n"
      "ds_read_b64_tr_b16 v[62:63], v13 offset:32256\n"
      "v_add_f32 v17, v17, v206\n"
      "v_cvt_pk_bf16_f32 v29, v202, v203\n"
      "v_add_f32 v18, v18, v207\n"
      "v_cvt_pk_bf16_f32 v30, v204, v205\n"
      "v_mfma_f32_32x32x16_bf16 v[160:175], v[252:255], v[156:159], v[160:175]\n"
      "v_cvt_pk_bf16_f32 v31, v206, v207\n"
      "v_add_f32 v17, v17, v18\n"
      "s_nop 0\n"
      "v_cmp_lt_f32 vcc, s67, v17\n"
      "s_cbranch_vccz .Lok21_%=\n"
      "s_swappc_b64 s[62:63], s[92:93]\n"
      ".Lok21_%=:\n"
      "v_add_f32 v16, v16, v17\n"
      "s_waitcnt lgkmcnt(0)\n"
      "s_barrier\n"
      "v_mfma_f32_32x32x16_bf16 v[64:79], v[32:35], v[24:27], v[64:79]\n"
      "ds_read_b128 v[224:227], v10 offset:40960\n"
      "ds_read_b128 v[228:231], v11 offset:40960\n"
      "ds_read_b128 v[232:235], v10 offset:41472\n"
      "ds_read_b128 v[236:239], v11 offset:41472\n"
      "v_mfma_f32_32x32x16_bf16 v[80:95], v[36:39], v[24:27], v[80:95]\n"
      "v_exp_f32 v192, v160\n"
      "ds_read_b128 v[240:243], v10 offset:41984\n"
      "v_exp_f32 v193, v161\n"
      "ds_read_b128 v[244:247], v11 offset:41984\n"
      "v_mfma_f32_32x32x16_bf16 v[96:111], v[40:43], v[24:27], v[96:111]\n"
      "v_exp_f32 v194, v162\n"
      "ds_read_b128 v[248:251], v10 offset:42496\n"
      "v_exp_f32 v195, v163\n"
      "ds_read_b128 v[252:255], v11 offset:42496\n"
      "v_mfma_f32_32x32x16_bf16 v[112:127], v[44:47], v[24:27], v[112:127]\n"
      "ds_read_b64_tr_b16 a[178:179], v14 offset:0\n"
      "v_exp_f32 v196, v164\n"
      "ds_read_b64_tr_b16 a[182:183], v14 offset:8\n"
      "v_add_f32 v17, v192, v194\n"
      "v_add_f32 v18, v193, v195\n"
      "v_mfma_f32_32x32x16_bf16 v[64:79], v[48:51], v[28:31], v[64:79]\n"
      "ds_read_b64_tr_b16 a[186:187], v15 offset:0\n"
      "v_exp_f32 v197, v165\n"
      "ds_read_b64_tr_b16 a[190:191], v15 offset:8\n"
      "v_add_f32 v17, v17, v196\n"
      "v_cvt_pk_bf16_f32 v24, v192, v193\n"
      "v_mfma_f32_32x32x16_bf16 v[80:95], v[52:55], v[28:31], v[80:95]\n"
      "v_exp_f32 v198, v166\n"
      "v_add_f32 v18, v18, v197\n"
      "v_exp_f32 v199, v167\n"
      "v_cvt_pk_bf16_f32 v25, v194, v195\n"
      "v_mfma_f32_32x32x16_bf16 v[96:111], v[56:59], v[28:31], v[96:111]\n"
      "v_exp_f32 v200, v168\n"
      "v_add_f32 v17, v17, v198\n"
      "v_exp_f32 v201, v169\n"
      "v_cvt_pk_bf16_f32 v26, v196, v197\n"
      "v_mfma_f32_32x32x16_bf16 v[112:127], v[60:63], v[28:31], v[112:127]\n"
      "v_add_f32 v18, v18, v199\n"
      "v_cvt_pk_bf16_f32 v27, v198, v199\n"
      "s_waitcnt lgkmcnt(0)\n"
      "v_mfma_f32_32x32x16_bf16 v[176:191], v[224:227], v[128:131], v[208:223]\n"
      "ds_read_b64_tr_b16 v[32:33], v12 offset:32768\n"
      "ds_read_b64_tr_b16 v[34:35], v13 offset:34816\n"
      "v_exp_f32 v202, v170\n"
      "ds_read_b64_tr_b16 v[36:37], v12 offset:33280\n"
      "v_add_f32 v17, v17, v200\n"
      "v_mfma_f32_32x32x16_bf16 v[176:191], v[228:231], v[132:135], v[176:191]\n"
      "ds_read_b64_tr_b16 v[38:39], v13 offset:35328\n"
      "ds_read_b64_tr_b16 v[40:41], v12 offset:33792\n"
      "v_exp_f32 v203, v171\n"
      "v_add_f32 v18, v18, v201\n"
      "v_mfma_f32_32x32x16_bf16 v[176:191], v[232:235], v[136:139], v[176:191]\n"
      "ds_read_b64_tr_b16 v[42:43], v13 offset:35840\n"
      "ds_read_b64_tr_b16 v[44:45], v12 offset:34304\n"
      "v_exp_f32 v204, v172\n"
      "ds_read_b64_tr_b16 v[46:47], v13 offset:36352\n"
      "v_add_f32 v17, v17, v202\n"
      "v_mfma_f32_32x32x16_bf16 v[176:191], v[236:239], v[140:143], v[176:191]\n"
      "ds_read_b64_tr_b16 v[48:49], v12 offset:36864\n"
      "ds_read_b64_tr_b16 v[50:51], v13 offset:38912\n"
      "v_exp_f32 v205, v173\n"
      "v_add_f32 v18, v18, v203\n"
      "v_mfma_f32_32x32x16_bf16 v[176:191], v[240:243], v[144:147], v[176:191]\n"
      "ds_read_b64_tr_b16 v[52:53], v12 offset:37376\n"
      "ds_read_b64_tr_b16 v[54:55], v13 offset:39424\n"
      "v_exp_f32 v206, v174\n"
      "v_add_f32 v17, v17, v204\n"
      "v_cvt_pk_bf16_f32 v28, v200, v201\n"
      "v_mfma_f32_32x32x16_bf16 v[176:191], v[244:247], v[148:151], v[176:191]\n"
      "s_waitcnt lgkmcnt(8)\n"
      "ds_read_b64_tr_b16 v[56:57], v12 offset:37888\n"
      "ds_read_b64_tr_b16 v[58:59], v13 offset:39936\n"
      "v_exp_f32 v207, v175\n"
      "v_add_f32 v18, v18, v205\n"
      "v_mfma_f32_32x32x16_bf16 v[176:191], v[248:251], v[152:155], v[176:191]\n"
      "ds_read_b64_tr_b16 v[60:61], v12 offset:38400\n"
      "ds_read_b64_tr_b16 v[62:63], v13 offset:40448\n"
      "v_add_f32 v17, v17, v206\n"
      "v_cvt_pk_bf16_f32 v29, v202, v203\n"
      "v_add_f32 v18, v18, v207\n"
      "v_cvt_pk_bf16_f32 v30, v204, v205\n"
      "v_mfma_f32_32x32x16_bf16 v[176:191], v[252:255], v[156:159], v[176:191]\n"
      "v_cvt_pk_bf16_f32 v31, v206, v207\n"
      "v_add_f32 v17, v17, v18\n"
      "s_nop 0\n"
      "v_cmp_lt_f32 vcc, s67, v17\n"
      "s_cbranch_vccz .Lok22_%=\n"
      "s_swappc_b64 s[62:63], s[64:65]\n"
      ".Lok22_%=:\n"
      "v_add_f32 v16, v16, v17\n"
      "s_waitcnt lgkmcnt(0)\n"
      "s_barrier\n"
      "v_mfma_f32_32x32x16_bf16 v[64:79], v[32:35], v[24:27], v[64:79]\n"
      "ds_read_b128 v[224:227], v10 offset:0\n"
      "ds_read_b128 v[228:231], v11 offset:0\n"
      "ds_read_b128 v[232:235], v10 offset:512\n"
      "ds_read_b128 v[236:239], v11 offset:512\n"
      "v_mfma_f32_32x32x16_bf16 v[80:95], v[36:39], v[24:27], v[80:95]\n"
      "v_exp_f32 v192, v176\n"
      "ds_read_b128 v[240:243], v10 offset:1024\n"
      "v_exp_f32 v193, v177\n"
      "ds_read_b128 v[244:247], v11 offset:1024\n"
      "v_mfma_f32_32x32x16_bf16 v[96:111], v[40:43], v[24:27], v[96:111]\n"
      "v_exp_f32 v194, v178\n"
      "ds_read_b128 v[248:251], v10 offset:1536\n"
      "v_exp_f32 v195, v179\n"
      "ds_read_b128 v[252:255], v11 offset:1536\n"
      "v_mfma_f32_32x32x16_bf16 v[112:127], v[44:47], v[24:27], v[112:127]\n"
      "ds_read_b64_tr_b16 a[192:193], v14 offset:8192\n"
      "v_exp_f32 v196, v180\n"
      "ds_read_b64_tr_b16 a[196:197], v14 offset:8200\n"
      "v_add_f32 v17, v192, v194\n"
      "v_add_f32 v18, v193, v195\n"
      "v_mfma_f32_32x32x16_bf16 v[64:79], v[48:51], v[28:31], v[64:79]\n"
      "ds_read_b64_tr_b16 a[200:201], v15 offset:8192\n"
      "v_exp_f32 v197, v181\n"
      "ds_read_b64_tr_b16 a[204:205], v15 offset:8200\n"
      "v_add_f32 v17, v17, v196\n"
      "v_cvt_pk_bf16_f32 v24, v192, v193\n"
      "v_mfma_f32_32x32x16_bf16 v[80:95], v[52:55], v[28:31], v[80:95]\n"
      "v_exp_f32 v198, v182\n"
      "v_add_f32 v18, v18, v197\n"
      "v_exp_f32 v199, v183\n"
      "v_cvt_pk_bf16_f32 v25, v194, v195\n"
      "v_mfma_f32_32x32x16_bf16 v[96:111], v[56:59], v[28:31], v[96:111]\n"
      "v_exp_f32 v200, v184\n"
      "v_add_f32 v17, v17, v198\n"
      "v_exp_f32 v201, v185\n"
      "v_cvt_pk_bf16_f32 v26, v196, v197\n"
      "v_mfma_f32_32x32x16_bf16 v[112:127], v[60:63], v[28:31], v[112:127]\n"
      "v_add_f32 v18, v18, v199\n"
      "v_cvt_pk_bf16_f32 v27, v198, v199\n"
      "s_waitcnt lgkmcnt(0)\n"
      "v_mfma_f32_32x32x16_bf16 v[160:175], v[224:227], v[128:131], v[208:223]\n"
      "ds_read_b64_tr_b16 v[32:33], v12 offset:40960\n"
      "ds_read_b64_tr_b16 v[34:35], v13 offset:43008\n"
      "v_exp_f32 v202, v186\n"
      "ds_read_b64_tr_b16 v[36:37], v12 offset:41472\n"
      "v_add_f32 v17, v17, v200\n"
      "v_mfma_f32_32x32x16_bf16 v[160:175], v[228:231], v[132:135], v[160:175]\n"
      "ds_read_b64_tr_b16 v[38:39], v13 offset:43520\n"
      "ds_read_b64_tr_b16 v[40:41], v12 offset:41984\n"
      "v_exp_f32 v203, v187\n"
      "v_add_f32 v18, v18, v201\n"
      "v_mfma_f32_32x32x16_bf16 v[160:175], v[232:235], v[136:139], v[160:175]\n"
      "ds_read_b64_tr_b16 v[42:43], v13 offset:44032\n"
      "ds_read_b64_tr_b16 v[44:45], v12 offset:42496\n"
      "v_exp_f32 v204, v188\n"
      "ds_read_b64_tr_b16 v[46:47], v13 offset:44544\n"
      "v_add_f32 v17, v17, v202\n"
      "v_mfma_f32_32x32x16_bf16 v[160:175], v[236:239], v[140:143], v[160:175]\n"
      "ds_read_b64_tr_b16 v[48:49], v12 offset:45056\n"
      "ds_read_b64_tr_b16 v[50:51], v13 offset:47104\n"
      "v_exp_f32 v205, v189\n"
      "v_add_f32 v18, v18, v203\n"
      "v_mfma_f32_32x32x16_bf16 v[160:175], v[240:243], v[144:147], v[160:175]\n"
      "ds_read_b64_tr_b16 v[52:53], v12 offset:45568\n"
      "ds_read_b64_tr_b16 v[54:55], v13 offset:47616\n"
      "v_exp_f32 v206, v190\n"
      "v_add_f32 v17, v17, v204\n"
      "v_cvt_pk_bf16_f32 v28, v200, v201\n"
      "v_mfma_f32_32x32x16_bf16 v[160:175], v[244:247], v[148:151], v[160:175]\n"
      "s_waitcnt lgkmcnt(8)\n"
      "ds_read_b64_tr_b16 v[56:57], v12 offset:46080\n"
      "ds_read_b64_tr_b16 v[58:59], v13 offset:48128\n"
      "v_exp_f32 v207, v191\n"
      "v_add_f32 v18, v18, v205\n"
      "v_mfma_f32_32x32x16_bf16 v[160:175], v[248:251], v[152:155], v[160:175]\n"
      "ds_read_b64_tr_b16 v[60:61], v12 offset:46592\n"
      "ds_read_b64_tr_b16 v[62:63], v13 offset:48640\n"
      "v_add_f32 v17, v17, v206\n"
      "v_cvt_pk_bf16_f32 v29, v202, v203\n"
      "v_add_f32 v18, v18, v207\n"
      "v_cvt_pk_bf16_f32 v30, v204, v205\n"
      "v_mfma_f32_32x32x16_bf16 v[160:175], v[252:255], v[156:159], v[160:175]\n"
      "v_cvt_pk_bf16_f32 v31, v206, v207\n"
      "v_add_f32 v17, v17, v18\n"
      "s_nop 0\n"
      "v_cmp_lt_f32 vcc, s67, v17\n"
      "s_cbranch_vccz .Lok23_%=\n"
      "s_swappc_b64 s[62:63], s[92:93]\n"
      ".Lok23_%=:\n"
      "v_add_f32 v16, v16, v17\n"
      "s_waitcnt lgkmcnt(0)\n"
      "s_barrier\n"
      "v_mfma_f32_32x32x16_bf16 v[64:79], v[32:35], v[24:27], v[64:79]\n"
      "ds_read_b128 v[224:227], v10 offset:8192\n"
      "ds_read_b128 v[228:231], v11 offset:8192\n"
      "ds_read_b128 v[232:235], v10 offset:8704\n"
      "ds_read_b128 v[236:239], v11 offset:8704\n"
      "v_mfma_f32_32x32x16_bf16 v[80:95], v[36:39], v[24:27], v[80:95]\n"
      "v_exp_f32 v192, v160\n"
      "ds_read_b128 v[240:243], v10 offset:9216\n"
      "v_exp_f32 v193, v161\n"
      "ds_read_b128 v[244:247], v11 offset:9216\n"
      "v_mfma_f32_32x32x16_bf16 v[96:111], v[40:43], v[24:27], v[96:111]\n"
      "v_exp_f32 v194, v162\n"
      "ds_read_b128 v[248:251], v10 offset:9728\n"
      "v_exp_f32 v195, v163\n"
      "ds_read_b128 v[252:255], v11 offset:9728\n"
      "v_mfma_f32_32x32x16_bf16 v[112:127], v[44:47], v[24:27], v[112:127]\n"
      "ds_read_b64_tr_b16 a[194:195], v14 offset:16384\n"
      "v_exp_f32 v196, v164\n"
      "ds_read_b64_tr_b16 a[198:199], v14 offset:16392\n"
      "v_add_f32 v17, v192, v194\n"
      "v_add_f32 v18, v193, v195\n"
      "v_mfma_f32_32x32x16_bf16 v[64:79], v[48:51], v[28:31], v[64:79]\n"
      "ds_read_b64_tr_b16 a[202:203], v15 offset:16384\n"
      "v_exp_f32 v197, v165\n"
      "ds_read_b64_tr_b16 a[206:207], v15 offset:16392\n"
      "v_add_f32 v17, v17, v196\n"
      "v_cvt_pk_bf16_f32 v24, v192, v193\n"
      "v_mfma_f32_32x32x16_bf16 v[80:95], v[52:55], v[28:31], v[80:95]\n"
      "v_exp_f32 v198, v166\n"
      "v_add_f32 v18, v18, v197\n"
      "v_exp_f32 v199, v167\n"
      "v_cvt_pk_bf16_f32 v25, v194, v195\n"
      "v_mfma_f32_32x32x16_bf16 v[96:111], v[56:59], v[28:31], v[96:111]\n"
      "v_exp_f32 v200, v168\n"
      "v_add_f32 v17, v17, v198\n"
      "v_exp_f32 v201, v169\n"
      "v_cvt_pk_bf16_f32 v26, v196, v197\n"
      "v_mfma_f32_32x32x16_bf16 v[112:127], v[60:63], v[28:31], v[112:127]\n"
      "v_add_f32 v18, v18, v199\n"
      "v_cvt_pk_bf16_f32 v27, v198, v199\n"
      "s_waitcnt lgkmcnt(0)\n"
      "v_mfma_f32_32x32x16_bf16 v[176:191], v[224:227], v[128:131], v[208:223]\n"
      "ds_read_b64_tr_b16 v[32:33], v12 offset:0\n"
      "ds_read_b64_tr_b16 v[34:35], v13 offset:2048\n"
      "v_exp_f32 v202, v170\n"
      "ds_read_b64_tr_b16 v[36:37], v12 offset:512\n"
      "v_add_f32 v17, v17, v200\n"
      "v_mfma_f32_32x32x16_bf16 v[176:191], v[228:231], v[132:135], v[176:191]\n"
      "ds_read_b64_tr_b16 v[38:39], v13 offset:2560\n"
      "ds_read_b64_tr_b16 v[40:41], v12 offset:1024\n"
      "v_exp_f32 v203, v171\n"
      "v_add_f32 v18, v18, v201\n"
      "v_mfma_f32_32x32x16_bf16 v[176:191], v[232:235], v[136:139], v[176:191]\n"
      "ds_read_b64_tr_b16 v[42:43], v13 offset:3072\n"
      "ds_read_b64_tr_b16 v[44:45], v12 offset:1536\n"
      "v_exp_f32 v204, v172\n"
      "ds_read_b64_tr_b16 v[46:47], v13 offset:3584\n"
      "v_add_f32 v17, v17, v202\n"
      "v_mfma_f32_32x32x16_bf16 v[176:191], v[236:239], v[140:143], v[176:191]\n"
      "ds_read_b64_tr_b16 v[48:49], v12 offset:4096\n"
      "ds_read_b64_tr_b16 v[50:51], v13 offset:6144\n"
      "v_exp_f32 v205, v173\n"
      "v_add_f32 v18, v18, v203\n"
      "v_mfma_f32_32x32x16_bf16 v[176:191], v[240:243], v[144:147], v[176:191]\n"
      "ds_read_b64_tr_b16 v[52:53], v12 offset:4608\n"
      "ds_read_b64_tr_b16 v[54:55], v13 offset:6656\n"
      "v_exp_f32 v206, v174\n"
      "v_add_f32 v17, v17, v204\n"
      "v_cvt_pk_bf16_f32 v28, v200, v201\n"
      "v_mfma_f32_32x32x16_bf16 v[176:191], v[244:247], v[148:151], v[176:191]\n"
      "s_waitcnt lgkmcnt(8)\n"
      "ds_read_b64_tr_b16 v[56:57], v12 offset:5120\n"
      "ds_read_b64_tr_b16 v[58:59], v13 offset:7168\n"
      "v_exp_f32 v207, v175\n"
      "v_add_f32 v18, v18, v205\n"
      "v_mfma_f32_32x32x16_bf16 v[176:191], v[248:251], v[152:155], v[176:191]\n"
      "ds_read_b64_tr_b16 v[60:61], v12 offset:5632\n"
      "ds_read_b64_tr_b16 v[62:63], v13 offset:7680\n"
      "v_add_f32 v17, v17, v206\n"
      "v_cvt_pk_bf16_f32 v29, v202, v203\n"
      "v_add_f32 v18, v18, v207\n"
      "v_cvt_pk_bf16_f32 v30, v204, v205\n"
      "v_mfma_f32_32x32x16_bf16 v[176:191], v[252:255], v[156:159], v[176:191]\n"
      "v_cvt_pk_bf16_f32 v31, v206, v207\n"
      "v_add_f32 v17, v17, v18\n"
      "s_nop 0\n"
      "v_cmp_lt_f32 vcc, s67, v17\n"
      "s_cbranch_vccz .Lok24_%=\n"
      "s_swappc_b64 s[62:63], s[64:65]\n"
      ".Lok24_%=:\n"
      "v_add_f32 v16, v16, v17\n"
      "s_waitcnt lgkmcnt(0)\n"
      "s_barrier\n"
      "v_mfma_f32_32x32x16_bf16 v[64:79], v[32:35], v[24:27], v[64:79]\n"
      "ds_read_b128 v[224:227], v10 offset:16384\n"
      "ds_read_b128 v[228:231], v11 offset:16384\n"
      "ds_read_b128 v[232:235], v10 offset:16896\n"
      "ds_read_b128 v[236:239], v11 offset:16896\n"
      "v_mfma_f32_32x32x16_bf16 v[80:95], v[36:39], v[24:27], v[80:95]\n"
      "v_exp_f32 v192, v176\n"
      "ds_read_b128 v[240:243], v10 offset:17408\n"
      "v_exp_f32 v193, v177\n"
      "ds_read_b128 v[244:247], v11 offset:17408\n"
      "v_mfma_f32_32x32x16_bf16 v[96:111], v[40:43], v[24:27], v[96:111]\n"
      "v_exp_f32 v194, v178\n"
      "ds_read_b128 v[248:251], v10 offset:17920\n"
      "v_exp_f32 v195, v179\n"
      "ds_read_b128 v[252:255], v11 offset:17920\n"
      "v_mfma_f32_32x32x16_bf16 v[112:127], v[44:47], v[24:27], v[112:127]\n"
      "ds_read_b64_tr_b16 a[208:209], v14 offset:40960\n"
      "v_exp_f32 v196, v180\n"
      "ds_read_b64_tr_b16 a[212:213], v14 offset:40968\n"
      "v_add_f32 v17, v192, v194\n"
      "v_add_f32 v18, v193, v195\n"
      "v_mfma_f32_32x32x16_bf16 v[64:79], v[48:51], v[28:31], v[64:79]\n"
      "ds_read_b64_tr_b16 a[216:217], v15 offset:40960\n"
      "v_exp_f32 v197, v181\n"
      "ds_read_b64_tr_b16 a[220:221], v15 offset:40968\n"
      "v_add_f32 v17, v17, v196\n"
      "v_cvt_pk_bf16_f32 v24, v192, v193\n"
      "v_mfma_f32_32x32x16_bf16 v[80:95], v[52:55], v[28:31], v[80:95]\n"
      "v_exp_f32 v198, v182\n"
      "v_add_f32 v18, v18, v197\n"
      "v_exp_f32 v199, v183\n"
      "v_cvt_pk_bf16_f32 v25, v194, v195\n"
      "v_mfma_f32_32x32x16_bf16 v[96:111], v[56:59], v[28:31], v[96:111]\n"
      "v_exp_f32 v200, v184\n"
      "v_add_f32 v17, v17, v198\n"
      "v_exp_f32 v201, v185\n"
      "v_cvt_pk_bf16_f32 v26, v196, v197\n"
      "v_mfma_f32_32x32x16_bf16 v[112:127], v[60:63], v[28:31], v[112:127]\n"
      "v_add_f32 v18, v18, v199\n"
      "v_cvt_pk_bf16_f32 v27, v198, v199\n"
      "s_waitcnt lgkmcnt(0)\n"
      "v_mfma_f32_32x32x16_bf16 v[160:175], v[224:227], v[128:131], v[208:223]\n"
      "ds_read_b64_tr_b16 v[32:33], v12 offset:8192\n"
      "ds_read_b64_tr_b16 v[34:35], v13 offset:10240\n"
      "v_exp_f32 v202, v186\n"
      "ds_read_b64_tr_b16 v[36:37], v12 offset:8704\n"
      "v_add_f32 v17, v17, v200\n"
      "v_mfma_f32_32x32x16_bf16 v[160:175], v[228:231], v[132:135], v[160:175]\n"
      "ds_read_b64_tr_b16 v[38:39], v13 offset:10752\n"
      "ds_read_b64_tr_b16 v[40:41], v12 offset:9216\n"
      "v_exp_f32 v203, v187\n"
      "v_add_f32 v18, v18, v201\n"
      "v_mfma_f32_32x32x16_bf16 v[160:175], v[232:235], v[136:139], v[160:175]\n"
      "ds_read_b64_tr_b16 v[42:43], v13 offset:11264\n"
      "ds_read_b64_tr_b16 v[44:45], v12 offset:9728\n"
      "v_exp_f32 v204, v188\n"
      "ds_read_b64_tr_b16 v[46:47], v13 offset:11776\n"
      "v_add_f32 v17, v17, v202\n"
      "v_mfma_f32_32x32x16_bf16 v[160:175], v[236:239], v[140:143], v[160:175]\n"
      "ds_read_b64_tr_b16 v[48:49], v12 offset:12288\n"
      "ds_read_b64_tr_b16 v[50:51], v13 offset:14336\n"
      "v_exp_f32 v205, v189\n"
      "v_add_f32 v18, v18, v203\n"
      "v_mfma_f32_32x32x16_bf16 v[160:175], v[240:243], v[144:147], v[160:175]\n"
      "ds_read_b64_tr_b16 v[52:53], v12 offset:12800\n"
      "ds_read_b64_tr_b16 v[54:55], v13 offset:14848\n"
      "v_exp_f32 v206, v190\n"
      "v_add_f32 v17, v17, v204\n"
      "v_cvt_pk_bf16_f32 v28, v200, v201\n"
      "v_mfma_f32_32x32x16_bf16 v[160:175], v[244:247], v[148:151], v[160:175]\n"
      "s_waitcnt lgkmcnt(8)\n"
      "ds_read_b64_tr_b16 v[56:57], v12 offset:13312\n"
      "ds_read_b64_tr_b16 v[58:59], v13 offset:15360\n"
      "v_exp_f32 v207, v191\n"
      "v_add_f32 v18, v18, v205\n"
      "v_mfma_f32_32x32x16_bf16 v[160:175], v[248:251], v[152:155], v[160:175]\n"
      "ds_read_b64_tr_b16 v[60:61], v12 offset:13824\n"
      "ds_read_b64_tr_b16 v[62:63], v13 offset:15872\n"
      "v_add_f32 v17, v17, v206\n"
      "v_cvt_pk_bf16_f32 v29, v202, v203\n"
      "v_add_f32 v18, v18, v207\n"
      "v_cvt_pk_bf16_f32 v30, v204, v205\n"
      "v_mfma_f32_32x32x16_bf16 v[160:175], v[252:255], v[156:159], v[160:175]\n"
      "v_cvt_pk_bf16_f32 v31, v206, v207\n"
      "v_add_f32 v17, v17, v18\n"
      "s_nop 0\n"
      "v_cmp_lt_f32 vcc, s67, v17\n"
      "s_cbranch_vccz .Lok25_%=\n"
      "s_swappc_b64 s[62:63], s[92:93]\n"
      ".Lok25_%=:\n"
      "v_add_f32 v16, v16, v17\n"
      "s_waitcnt lgkmcnt(0)\n"
      "s_barrier\n"
      "v_mfma_f32_32x32x16_bf16 v[64:79], v[32:35], v[24:27], v[64:79]\n"
      "ds_read_b128 v[224:227], v10 offset:24576\n"
      "ds_read_b128 v[228:231], v11 offset:24576\n"
      "ds_read_b128 v[232:235], v10 offset:25088\n"
      "ds_read_b128 v[236:239], v11 offset:25088\n"
      "v_mfma_f32_32x32x16_bf16 v[80:95], v[36:39], v[24:27], v[80:95]\n"
      "v_exp_f32 v192, v160\n"
      "ds_read_b128 v[240:243], v10 offset:25600\n"
      "v_exp_f32 v193, v161\n"
      "ds_read_b128 v[244:247], v11 offset:25600\n"
      "v_mfma_f32_32x32x16_bf16 v[96:111], v[40:43], v[24:27], v[96:111]\n"
      "v_exp_f32 v194, v162\n"
      "ds_read_b128 v[248:251], v10 offset:26112\n"
      "v_exp_f32 v195, v163\n"
      "ds_read_b128 v[252:255], v11 offset:26112\n"
      "v_mfma_f32_32x32x16_bf16 v[112:127], v[44:47], v[24:27], v[112:127]\n"
      "ds_read_b64_tr_b16 a[210:211], v14 offset:24576\n"
      "v_exp_f32 v196, v164\n"
      "ds_read_b64_tr_b16 a[214:215], v14 offset:24584\n"
      "v_add_f32 v17, v192, v194\n"
      "v_add_f32 v18, v193, v195\n"
      "v_mfma_f32_32x32x16_bf16 v[64:79], v[48:51], v[28:31], v[64:79]\n"
      "ds_read_b64_tr_b16 a[218:219], v15 offset:24576\n"
      "v_exp_f32 v197, v165\n"
      "ds_read_b64_tr_b16 a[222:223], v15 offset:24584\n"
      "v_add_f32 v17, v17, v196\n"
      "v_cvt_pk_bf16_f32 v24, v192, v193\n"
      "v_mfma_f32_32x32x16_bf16 v[80:95], v[52:55], v[28:31], v[80:95]\n"
      "v_exp_f32 v198, v166\n"
      "v_add_f32 v18, v18, v197\n"
      "v_exp_f32 v199, v167\n"
      "v_cvt_pk_bf16_f32 v25, v194, v195\n"
      "v_mfma_f32_32x32x16_bf16 v[96:111], v[56:59], v[28:31], v[96:111]\n"
      "v_exp_f32 v200, v168\n"
      "v_add_f32 v17, v17, v198\n"
      "v_exp_f32 v201, v169\n"
      "v_cvt_pk_bf16_f32 v26, v196, v197\n"
      "v_mfma_f32_32x32x16_bf16 v[112:127], v[60:63], v[28:31], v[112:127]\n"
      "v_add_f32 v18, v18, v199\n"
      "v_cvt_pk_bf16_f32 v27, v198, v199\n"
      "s_waitcnt lgkmcnt(0)\n"
      "v_mfma_f32_32x32x16_bf16 v[176:191], v[224:227], v[128:131], v[208:223]\n"
      "ds_read_b64_tr_b16 v[32:33], v12 offset:16384\n"
      "ds_read_b64_tr_b16 v[34:35], v13 offset:18432\n"
      "v_exp_f32 v202, v170\n"
      "ds_read_b64_tr_b16 v[36:37], v12 offset:16896\n"
      "v_add_f32 v17, v17, v200\n"
      "v_mfma_f32_32x32x16_bf16 v[176:191], v[228:231], v[132:135], v[176:191]\n"
      "ds_read_b64_tr_b16 v[38:39], v13 offset:18944\n"
      "ds_read_b64_tr_b16 v[40:41], v12 offset:17408\n"
      "v_exp_f32 v203, v171\n"
      "v_add_f32 v18, v18, v201\n"
      "v_mfma_f32_32x32x16_bf16 v[176:191], v[232:235], v[136:139], v[176:191]\n"
      "ds_read_b64_tr_b16 v[42:43], v13 offset:19456\n"
      "ds_read_b64_tr_b16 v[44:45], v12 offset:17920\n"
      "v_exp_f32 v204, v172\n"
      "ds_read_b64_tr_b16 v[46:47], v13 offset:19968\n"
      "v_add_f32 v17, v17, v202\n"
      "v_mfma_f32_32x32x16_bf16 v[176:191], v[236:239], v[140:143], v[176:191]\n"
      "ds_read_b64_tr_b16 v[48:49], v12 offset:20480\n"
      "ds_read_b64_tr_b16 v[50:51], v13 offset:22528\n"
      "v_exp_f32 v205, v173\n"
      "v_add_f32 v18, v18, v203\n"
      "v_mfma_f32_32x32x16_bf16 v[176:191], v[240:243], v[144:147], v[176:191]\n"
      "ds_read_b64_tr_b16 v[52:53], v12 offset:20992\n"
      "ds_read_b64_tr_b16 v[54:55], v13 offset:23040\n"
      "v_exp_f32 v206, v174\n"
      "v_add_f32 v17, v17, v204\n"
      "v_cvt_pk_bf16_f32 v28, v200, v201\n"
      "v_mfma_f32_32x32x16_bf16 v[176:191], v[244:247], v[148:151], v[176:191]\n"
      "s_waitcnt lgkmcnt(8)\n"
      "ds_read_b64_tr_b16 v[56:57], v12 offset:21504\n"
      "ds_read_b64_tr_b16 v[58:59], v13 offset:23552\n"
      "v_exp_f32 v207, v175\n"
      "v_add_f32 v18, v18, v205\n"
      "v_mfma_f32_32x32x16_bf16 v[176:191], v[248:251], v[152:155], v[176:191]\n"
      "ds_read_b64_tr_b16 v[60:61], v12 offset:22016\n"
      "ds_read_b64_tr_b16 v[62:63], v13 offset:24064\n"
      "v_add_f32 v17, v17, v206\n"
      "v_cvt_pk_bf16_f32 v29, v202, v203\n"
      "v_add_f32 v18, v18, v207\n"
      "v_cvt_pk_bf16_f32 v30, v204, v205\n"
      "v_mfma_f32_32x32x16_bf16 v[176:191], v[252:255], v[156:159], v[176:191]\n"
      "v_cvt_pk_bf16_f32 v31, v206, v207\n"
      "v_add_f32 v17, v17, v18\n"
      "s_nop 0\n"
      "v_cmp_lt_f32 vcc, s67, v17\n"
      "s_cbranch_vccz .Lok26_%=\n"
      "s_swappc_b64 s[62:63], s[64:65]\n"
      ".Lok26_%=:\n"
      "v_add_f32 v16, v16, v17\n"
      "s_waitcnt lgkmcnt(0)\n"
      "s_barrier\n"
      "v_mfma_f32_32x32x16_bf16 v[64:79], v[32:35], v[24:27], v[64:79]\n"
      "ds_read_b128 v[224:227], v10 offset:32768\n"
      "ds_read_b128 v[228:231], v11 offset:32768\n"
      "ds_read_b128 v[232:235], v10 offset:33280\n"
      "ds_read_b128 v[236:239], v11 offset:33280\n"
      "v_mfma_f32_32x32x16_bf16 v[80:95], v[36:39], v[24:27], v[80:95]\n"
      "v_exp_f32 v192, v176\n"
      "ds_read_b128 v[240:243], v10 offset:33792\n"
      "v_exp_f32 v193, v177\n"
      "ds_read_b128 v[244:247], v11 offset:33792\n"
      "v_mfma_f32_32x32x16_bf16 v[96:111], v[40:43], v[24:27], v[96:111]\n"
      "v_exp_f32 v194, v178\n"
      "ds_read_b128 v[248:251], v10 offset:34304\n"
      "v_exp_f32 v195, v179\n"
      "ds_read_b128 v[252:255], v11 offset:34304\n"
      "v_mfma_f32_32x32x16_bf16 v[112:127], v[44:47], v[24:27], v[112:127]\n"
      "ds_read_b64_tr_b16 a[224:225], v14 offset:32768\n"
      "v_exp_f32 v196, v180\n"
      "ds_read_b64_tr_b16 a[228:229], v14 offset:32776\n"
      "v_add_f32 v17, v192, v194\n"
      "v_add_f32 v18, v193, v195\n"
      "v_mfma_f32_32x32x16_bf16 v[64:79], v[48:51], v[28:31], v[64:79]\n"
      "ds_read_b64_tr_b16 a[232:233], v15 offset:32768\n"
      "v_exp_f32 v197, v181\n"
      "ds_read_b64_tr_b16 a[236:237], v15 offset:32776\n"
      "v_add_f32 v17, v17, v196\n"
      "v_cvt_pk_bf16_f32 v24, v192, v193\n"
      "v_mfma_f32_32x32x16_bf16 v[80:95], v[52:55], v[28:31], v[80:95]\n"
      "v_exp_f32 v198, v182\n"
      "v_add_f32 v18, v18, v197\n"
      "v_exp_f32 v199, v183\n"
      "v_cvt_pk_bf16_f32 v25, v194, v195\n"
      "v_mfma_f32_32x32x16_bf16 v[96:111], v[56:59], v[28:31], v[96:111]\n"
      "v_exp_f32 v200, v184\n"
      "v_add_f32 v17, v17, v198\n"
      "v_exp_f32 v201, v185\n"
      "v_cvt_pk_bf16_f32 v26, v196, v197\n"
      "v_mfma_f32_32x32x16_bf16 v[112:127], v[60:63], v[28:31], v[112:127]\n"
      "v_add_f32 v18, v18, v199\n"
      "v_cvt_pk_bf16_f32 v27, v198, v199\n"
      "s_waitcnt lgkmcnt(0)\n"
      "v_mfma_f32_32x32x16_bf16 v[160:175], v[224:227], v[128:131], v[208:223]\n"
      "ds_read_b64_tr_b16 v[32:33], v12 offset:24576\n"
      "ds_read_b64_tr_b16 v[34:35], v13 offset:26624\n"
      "v_exp_f32 v202, v186\n"
      "ds_read_b64_tr_b16 v[36:37], v12 offset:25088\n"
      "v_add_f32 v17, v17, v200\n"
      "v_mfma_f32_32x32x16_bf16 v[160:175], v[228:231], v[132:135], v[160:175]\n"
      "ds_read_b64_tr_b16 v[38:39], v13 offset:27136\n"
      "ds_read_b64_tr_b16 v[40:41], v12 offset:25600\n"
      "v_exp_f32 v203, v187\n"
      "v_add_f32 v18, v18, v201\n"
      "v_mfma_f32_32x32x16_bf16 v[160:175], v[232:235], v[136:139], v[160:175]\n"
      "ds_read_b64_tr_b16 v[42:43], v13 offset:27648\n"
      "ds_read_b64_tr_b16 v[44:45], v12 offset:26112\n"
      "v_exp_f32 v204, v188\n"
      "ds_read_b64_tr_b16 v[46:47], v13 offset:28160\n"
      "v_add_f32 v17, v17, v202\n"
      "v_mfma_f32_32x32x16_bf16 v[160:175], v[236:239], v[140:143], v[160:175]\n"
      "ds_read_b64_tr_b16 v[48:49], v12 offset:28672\n"
      "ds_read_b64_tr_b16 v[50:51], v13 offset:30720\n"
      "v_exp_f32 v205, v189\n"
      "v_add_f32 v18, v18, v203\n"
      "v_mfma_f32_32x32x16_bf16 v[160:175], v[240:243], v[144:147], v[160:175]\n"
      "ds_read_b64_tr_b16 v[52:53], v12 offset:29184\n"
      "ds_read_b64_tr_b16 v[54:55], v13 offset:31232\n"
      "v_exp_f32 v206, v190\n"
      "v_add_f32 v17, v17, v204\n"
      "v_cvt_pk_bf16_f32 v28, v200, v201\n"
      "v_mfma_f32_32x32x16_bf16 v[160:175], v[244:247], v[148:151], v[160:175]\n"
      "s_waitcnt lgkmcnt(8)\n"
      "ds_read_b64_tr_b16 v[56:57], v12 offset:29696\n"
      "ds_read_b64_tr_b16 v[58:59], v13 offset:31744\n"
      "v_exp_f32 v207, v191\n"
      "v_add_f32 v18, v18, v205\n"
      "v_mfma_f32_32x32x16_bf16 v[160:175], v[248:251], v[152:155], v[160:175]\n"
      "ds_read_b64_tr_b16 v[60:61], v12 offset:30208\n"
      "ds_read_b64_tr_b16 v[62:63], v13 offset:32256\n"
      "v_add_f32 v17, v17, v206\n"
      "v_cvt_pk_bf16_f32 v29, v202, v203\n"
      "v_add_f32 v18, v18, v207\n"
      "v_cvt_pk_bf16_f32 v30, v204, v205\n"
      "v_mfma_f32_32x32x16_bf16 v[160:175], v[252:255], v[156:159], v[160:175]\n"
      "v_cvt_pk_bf16_f32 v31, v206, v207\n"
      "v_add_f32 v17, v17, v18\n"
      "s_nop 0\n"
      "v_cmp_lt_f32 vcc, s67, v17\n"
      "s_cbranch_vccz .Lok27_%=\n"
      "s_swappc_b64 s[62:63], s[92:93]\n"
      ".Lok27_%=:\n"
      "v_add_f32 v16, v16, v17\n"
      "s_waitcnt lgkmcnt(0)\n"
      "s_barrier\n"
      "v_mfma_f32_32x32x16_bf16 v[64:79], v[32:35], v[24:27], v[64:79]\n"
      "ds_read_b128 v[224:227], v10 offset:40960\n"
      "ds_read_b128 v[228:231], v11 offset:40960\n"
      "ds_read_b128 v[232:235], v10 offset:41472\n"
      "ds_read_b128 v[236:239], v11 offset:41472\n"
      "v_mfma_f32_32x32x16_bf16 v[80:95], v[36:39], v[24:27], v[80:95]\n"
      "v_exp_f32 v192, v160\n"
      "ds_read_b128 v[240:243], v10 offset:41984\n"
      "v_exp_f32 v193, v161\n"
      "ds_read_b128 v[244:247], v11 offset:41984\n"
      "v_mfma_f32_32x32x16_bf16 v[96:111], v[40:43], v[24:27], v[96:111]\n"
      "v_exp_f32 v194, v162\n"
      "ds_read_b128 v[248:251], v10 offset:42496\n"
      "v_exp_f32 v195, v163\n"
      "ds_read_b128 v[252:255], v11 offset:42496\n"
      "v_mfma_f32_32x32x16_bf16 v[112:127], v[44:47], v[24:27], v[112:127]\n"
      "ds_read_b64_tr_b16 a[226:227], v14 offset:49152\n"
      "v_exp_f32 v196, v164\n"
      "ds_read_b64_tr_b16 a[230:231], v14 offset:49160\n"
      "v_add_f32 v17, v192, v194\n"
      "v_add_f32 v18, v193, v195\n"
      "v_mfma_f32_32x32x16_bf16 v[64:79], v[48:51], v[28:31], v[64:79]\n"
      "ds_read_b64_tr_b16 a[234:235], v15 offset:49152\n"
      "v_exp_f32 v197, v165\n"
      "ds_read_b64_tr_b16 a[238:239], v15 offset:49160\n"
      "v_add_f32 v17, v17, v196\n"
      "v_cvt_pk_bf16_f32 v24, v192, v193\n"
      "v_mfma_f32_32x32x16_bf16 v[80:95], v[52:55], v[28:31], v[80:95]\n"
      "v_exp_f32 v198, v166\n"
      "v_add_f32 v18, v18, v197\n"
      "v_exp_f32 v199, v167\n"
      "v_cvt_pk_bf16_f32 v25, v194, v195\n"
      "v_mfma_f32_32x32x16_bf16 v[96:111], v[56:59], v[28:31], v[96:111]\n"
      "v_exp_f32 v200, v168\n"
      "v_add_f32 v17, v17, v198\n"
      "v_exp_f32 v201, v169\n"
      "v_cvt_pk_bf16_f32 v26, v196, v197\n"
      "v_mfma_f32_32x32x16_bf16 v[112:127], v[60:63], v[28:31], v[112:127]\n"
      "v_add_f32 v18, v18, v199\n"
      "v_cvt_pk_bf16_f32 v27, v198, v199\n"
      "s_waitcnt lgkmcnt(0)\n"
      "v_mfma_f32_32x32x16_bf16 v[176:191], v[224:227], v[128:131], v[208:223]\n"
      "ds_read_b64_tr_b16 v[32:33], v12 offset:32768\n"
      "ds_read_b64_tr_b16 v[34:35], v13 offset:34816\n"
      "v_exp_f32 v202, v170\n"
      "ds_read_b64_tr_b16 v[36:37], v12 offset:33280\n"
      "v_add_f32 v17, v17, v200\n"
      "v_mfma_f32_32x32x16_bf16 v[176:191], v[228:231], v[132:135], v[176:191]\n"
      "ds_read_b64_tr_b16 v[38:39], v13 offset:35328\n"
      "ds_read_b64_tr_b16 v[40:41], v12 offset:33792\n"
      "v_exp_f32 v203, v171\n"
      "v_add_f32 v18, v18, v201\n"
      "v_mfma_f32_32x32x16_bf16 v[176:191], v[232:235], v[136:139], v[176:191]\n"
      "ds_read_b64_tr_b16 v[42:43], v13 offset:35840\n"
      "ds_read_b64_tr_b16 v[44:45], v12 offset:34304\n"
      "v_exp_f32 v204, v172\n"
      "ds_read_b64_tr_b16 v[46:47], v13 offset:36352\n"
      "v_add_f32 v17, v17, v202\n"
      "v_mfma_f32_32x32x16_bf16 v[176:191], v[236:239], v[140:143], v[176:191]\n"
      "ds_read_b64_tr_b16 v[48:49], v12 offset:36864\n"
      "ds_read_b64_tr_b16 v[50:51], v13 offset:38912\n"
      "v_exp_f32 v205, v173\n"
      "v_add_f32 v18, v18, v203\n"
      "v_mfma_f32_32x32x16_bf16 v[176:191], v[240:243], v[144:147], v[176:191]\n"
      "ds_read_b64_tr_b16 v[52:53], v12 offset:37376\n"
      "ds_read_b64_tr_b16 v[54:55], v13 offset:39424\n"
      "v_exp_f32 v206, v174\n"
      "v_add_f32 v17, v17, v204\n"
      "v_cvt_pk_bf16_f32 v28, v200, v201\n"
      "v_mfma_f32_32x32x16_bf16 v[176:191], v[244:247], v[148:151], v[176:191]\n"
      "s_waitcnt lgkmcnt(8)\n"
      "ds_read_b64_tr_b16 v[56:57], v12 offset:37888\n"
      "ds_read_b64_tr_b16 v[58:59], v13 offset:39936\n"
      "v_exp_f32 v207, v175\n"
      "v_add_f32 v18, v18, v205\n"
      "v_mfma_f32_32x32x16_bf16 v[176:191], v[248:251], v[152:155], v[176:191]\n"
      "ds_read_b64_tr_b16 v[60:61], v12 offset:38400\n"
      "ds_read_b64_tr_b16 v[62:63], v13 offset:40448\n"
      "v_add_f32 v17, v17, v206\n"
      "v_cvt_pk_bf16_f32 v29, v202, v203\n"
      "v_add_f32 v18, v18, v207\n"
      "v_cvt_pk_bf16_f32 v30, v204, v205\n"
      "v_mfma_f32_32x32x16_bf16 v[176:191], v[252:255], v[156:159], v[176:191]\n"
      "v_cvt_pk_bf16_f32 v31, v206, v207\n"
      "v_add_f32 v17, v17, v18\n"
      "s_nop 0\n"
      "v_cmp_lt_f32 vcc, s67, v17\n"
      "s_cbranch_vccz .Lok28_%=\n"
      "s_swappc_b64 s[62:63], s[64:65]\n"
      ".Lok28_%=:\n"
      "v_add_f32 v16, v16, v17\n"
      "s_waitcnt lgkmcnt(0)\n"
      "s_barrier\n"
      "v_mfma_f32_32x32x16_bf16 v[64:79], v[32:35], v[24:27], v[64:79]\n"
      "ds_read_b128 v[224:227], v10 offset:0\n"
      "ds_read_b128 v[228:231], v11 offset:0\n"
      "ds_read_b128 v[232:235], v10 offset:512\n"
      "ds_read_b128 v[236:239], v11 offset:512\n"
      "v_mfma_f32_32x32x16_bf16 v[80:95], v[36:39], v[24:27], v[80:95]\n"
      "v_exp_f32 v192, v176\n"
      "ds_read_b128 v[240:243], v10 offset:1024\n"
      "v_exp_f32 v193, v177\n"
      "ds_read_b128 v[244:247], v11 offset:1024\n"
      "v_mfma_f32_32x32x16_bf16 v[96:111], v[40:43], v[24:27], v[96:111]\n"
      "v_exp_f32 v194, v178\n"
      "ds_read_b128 v[248:251], v10 offset:1536\n"
      "v_exp_f32 v195, v179\n"
      "ds_read_b128 v[252:255], v11 offset:1536\n"
      "v_mfma_f32_32x32x16_bf16 v[112:127], v[44:47], v[24:27], v[112:127]\n"
      "ds_read_b64_tr_b16 a[240:241], v14 offset:57344\n"
      "v_exp_f32 v196, v180\n"
      "ds_read_b64_tr_b16 a[244:245], v14 offset:57352\n"
      "v_add_f32 v17, v192, v194\n"
      "v_add_f32 v18, v193, v195\n"
      "v_mfma_f32_32x32x16_bf16 v[64:79], v[48:51], v[28:31], v[64:79]\n"
      "ds_read_b64_tr_b16 a[248:249], v15 offset:57344\n"
      "v_exp_f32 v197, v181\n"
      "ds_read_b64_tr_b16 a[252:253], v15 offset:57352\n"
      "v_add_f32 v17, v17, v196\n"
      "v_cvt_pk_bf16_f32 v24, v192, v193\n"
      "v_mfma_f32_32x32x16_bf16 v[80:95], v[52:55], v[28:31], v[80:95]\n"
      "v_exp_f32 v198, v182\n"
      "v_add_f32 v18, v18, v197\n"
      "v_exp_f32 v199, v183\n"
      "v_cvt_pk_bf16_f32 v25, v194, v195\n"
      "v_mfma_f32_32x32x16_bf16 v[96:111], v[56:59], v[28:31], v[96:111]\n"
      "v_exp_f32 v200, v184\n"
      "v_add_f32 v17, v17, v198\n"
      "v_exp_f32 v201, v185\n"
      "v_cvt_pk_bf16_f32 v26, v196, v197\n"
      "v_mfma_f32_32x32x16_bf16 v[112:127], v[60:63], v[28:31], v[112:127]\n"
      "v_add_f32 v18, v18, v199\n"
      "v_cvt_pk_bf16_f32 v27, v198, v199\n"
      "s_waitcnt lgkmcnt(0)\n"
      "v_mfma_f32_32x32x16_bf16 v[160:175], v[224:227], v[128:131], v[208:223]\n"
      "ds_read_b64_tr_b16 v[32:33], v12 offset:40960\n"
      "ds_read_b64_tr_b16 v[34:35], v13 offset:43008\n"
      "v_exp_f32 v202, v186\n"
      "ds_read_b64_tr_b16 v[36:37], v12 offset:41472\n"
      "v_add_f32 v17, v17, v200\n"
      "v_mfma_f32_32x32x16_bf16 v[160:175], v[228:231], v[132:135], v[160:175]\n"
      "ds_read_b64_tr_b16 v[38:39], v13 offset:43520\n"
      "ds_read_b64_tr_b16 v[40:41], v12 offset:41984\n"
      "v_exp_f32 v203, v187\n"
      "v_add_f32 v18, v18, v201\n"
      "v_mfma_f32_32x32x16_bf16 v[160:175], v[232:235], v[136:139], v[160:175]\n"
      "ds_read_b64_tr_b16 v[42:43], v13 offset:44032\n"
      "ds_read_b64_tr_b16 v[44:45], v12 offset:42496\n"
      "v_exp_f32 v204, v188\n"
      "ds_read_b64_tr_b16 v[46:47], v13 offset:44544\n"
      "v_add_f32 v17, v17, v202\n"
      "v_mfma_f32_32x32x16_bf16 v[160:175], v[236:239], v[140:143], v[160:175]\n"
      "ds_read_b64_tr_b16 v[48:49], v12 offset:45056\n"
      "ds_read_b64_tr_b16 v[50:51], v13 offset:47104\n"
      "v_exp_f32 v205, v189\n"
      "v_add_f32 v18, v18, v203\n"
      "v_mfma_f32_32x32x16_bf16 v[160:175], v[240:243], v[144:147], v[160:175]\n"
      "ds_read_b64_tr_b16 v[52:53], v12 offset:45568\n"
      "ds_read_b64_tr_b16 v[54:55], v13 offset:47616\n"
      "v_exp_f32 v206, v190\n"
      "v_add_f32 v17, v17, v204\n"
      "v_cvt_pk_bf16_f32 v28, v200, v201\n"
      "v_mfma_f32_32x32x16_bf16 v[160:175], v[244:247], v[148:151], v[160:175]\n"
      "s_waitcnt lgkmcnt(8)\n"
      "ds_read_b64_tr_b16 v[56:57], v12 offset:46080\n"
      "ds_read_b64_tr_b16 v[58:59], v13 offset:48128\n"
      "v_exp_f32 v207, v191\n"
      "v_add_f32 v18, v18, v205\n"
      "v_mfma_f32_32x32x16_bf16 v[160:175], v[248:251], v[152:155], v[160:175]\n"
      "ds_read_b64_tr_b16 v[60:61], v12 offset:46592\n"
      "ds_read_b64_tr_b16 v[62:63], v13 offset:48640\n"
      "v_add_f32 v17, v17, v206\n"
      "v_cvt_pk_bf16_f32 v29, v202, v203\n"
      "v_add_f32 v18, v18, v207\n"
      "v_cvt_pk_bf16_f32 v30, v204, v205\n"
      "v_mfma_f32_32x32x16_bf16 v[160:175], v[252:255], v[156:159], v[160:175]\n"
      "v_cvt_pk_bf16_f32 v31, v206, v207\n"
      "v_add_f32 v17, v17, v18\n"
      "s_nop 0\n"
      "v_cmp_lt_f32 vcc, s67, v17\n"
      "s_cbranch_vccz .Lok29_%=\n"
      "s_swappc_b64 s[62:63], s[92:93]\n"
      ".Lok29_%=:\n"
      "v_add_f32 v16, v16, v17\n"
      "s_waitcnt lgkmcnt(0)\n"
      "s_barrier\n"
      "v_mfma_f32_32x32x16_bf16 v[64:79], v[32:35], v[24:27], v[64:79]\n"
      "ds_read_b128 v[224:227], v10 offset:8192\n"
      "ds_read_b128 v[228:231], v11 offset:8192\n"
      "ds_read_b128 v[232:235], v10 offset:8704\n"
      "ds_read_b128 v[236:239], v11 offset:8704\n"
      "v_mfma_f32_32x32x16_bf16 v[80:95], v[36:39], v[24:27], v[80:95]\n"
      "v_exp_f32 v192, v160\n"
      "ds_read_b128 v[240:243], v10 offset:9216\n"
      "v_exp_f32 v193, v161\n"
      "ds_read_b128 v[244:247], v11 offset:9216\n"
      "v_mfma_f32_32x32x16_bf16 v[96:111], v[40:43], v[24:27], v[96:111]\n"
      "v_exp_f32 v194, v162\n"
      "ds_read_b128 v[248:251], v10 offset:9728\n"
      "v_exp_f32 v195, v163\n"
      "ds_read_b128 v[252:255], v11 offset:9728\n"
      "v_mfma_f32_32x32x16_bf16 v[112:127], v[44:47], v[24:27], v[112:127]\n"
      "ds_read_b64_tr_b16 a[242:243], v14 offset:0\n"
      "v_exp_f32 v196, v164\n"
      "ds_read_b64_tr_b16 a[246:247], v14 offset:8\n"
      "v_add_f32 v17, v192, v194\n"
      "v_add_f32 v18, v193, v195\n"
      "v_mfma_f32_32x32x16_bf16 v[64:79], v[48:51], v[28:31], v[64:79]\n"
      "ds_read_b64_tr_b16 a[250:251], v15 offset:0\n"
      "v_exp_f32 v197, v165\n"
      "ds_read_b64_tr_b16 a[254:255], v15 offset:8\n"
      "v_add_f32 v17, v17, v196\n"
      "v_cvt_pk_bf16_f32 v24, v192, v193\n"
      "v_mfma_f32_32x32x16_bf16 v[80:95], v[52:55], v[28:31], v[80:95]\n"
      "v_exp_f32 v198, v166\n"
      "v_add_f32 v18, v18, v197\n"
      "v_exp_f32 v199, v167\n"
      "v_cvt_pk_bf16_f32 v25, v194, v195\n"
      "v_mfma_f32_32x32x16_bf16 v[96:111], v[56:59], v[28:31], v[96:111]\n"
      "v_exp_f32 v200, v168\n"
      "v_add_f32 v17, v17, v198\n"
      "v_exp_f32 v201, v169\n"
      "v_cvt_pk_bf16_f32 v26, v196, v197\n"
      "v_mfma_f32_32x32x16_bf16 v[112:127], v[60:63], v[28:31], v[112:127]\n"
      "v_add_f32 v18, v18, v199\n"
      "v_cvt_pk_bf16_f32 v27, v198, v199\n"
      "s_waitcnt lgkmcnt(0)\n"
      "v_mfma_f32_32x32x16_bf16 v[176:191], v[224:227], v[128:131], v[208:223]\n"
      "v_exp_f32 v202, v170\n"
      "v_add_f32 v17, v17, v200\n"
      "v_mfma_f32_32x32x16_bf16 v[176:191], v[228:231], v[132:135], v[176:191]\n"
      "v_exp_f32 v203, v171\n"
      "v_add_f32 v18, v18, v201\n"
      "v_mfma_f32_32x32x16_bf16 v[176:191], v[232:235], v[136:139], v[176:191]\n"
      "v_exp_f32 v204, v172\n"
      "v_add_f32 v17, v17, v202\n"
      "v_mfma_f32_32x32x16_bf16 v[176:191], v[236:239], v[140:143], v[176:191]\n"
      "v_exp_f32 v205, v173\n"
      "v_add_f32 v18, v18, v203\n"
      "v_mfma_f32_32x32x16_bf16 v[176:191], v[240:243], v[144:147], v[176:191]\n"
      "v_exp_f32 v206, v174\n"
      "v_add_f32 v17, v17, v204\n"
      "v_cvt_pk_bf16_f32 v28, v200, v201\n"
      "v_mfma_f32_32x32x16_bf16 v[176:191], v[244:247], v[148:151], v[176:191]\n"
      "s_waitcnt lgkmcnt(8)\n"
      "v_exp_f32 v207, v175\n"
      "v_add_f32 v18, v18, v205\n"
      "v_mfma_f32_32x32x16_bf16 v[176:191], v[248:251], v[152:155], v[176:191]\n"
      "v_add_f32 v17, v17, v206\n"
      "v_cvt_pk_bf16_f32 v29, v202, v203\n"
      "v_add_f32 v18, v18, v207\n"
      "v_cvt_pk_bf16_f32 v30, v204, v205\n"
      "v_mfma_f32_32x32x16_bf16 v[176:191], v[252:255], v[156:159], v[176:191]\n"
      "v_cvt_pk_bf16_f32 v31, v206, v207\n"
      "v_add_f32 v17, v17, v18\n"
      "s_nop 0\n"
      "v_cmp_lt_f32 vcc, s67, v17\n"
      "s_cbranch_vccz .Lok30_%=\n"
      "s_swappc_b64 s[62:63], s[64:65]\n"
      ".Lok30_%=:\n"
      "v_add_f32 v16, v16, v17\n"
      "s_waitcnt lgkmcnt(0)\n"
      "s_barrier\n"
      "ds_read_b64_tr_b16 v[32:33], v12 offset:0\n"
      "ds_read_b64_tr_b16 v[34:35], v13 offset:2048\n"
      "ds_read_b64_tr_b16 v[36:37], v12 offset:512\n"
      "ds_read_b64_tr_b16 v[38:39], v13 offset:2560\n"
      "ds_read_b64_tr_b16 v[40:41], v12 offset:1024\n"
      "ds_read_b64_tr_b16 v[42:43], v13 offset:3072\n"
      "ds_read_b64_tr_b16 v[44:45], v12 offset:1536\n"
      "ds_read_b64_tr_b16 v[46:47], v13 offset:3584\n"
      "ds_read_b64_tr_b16 v[48:49], v12 offset:4096\n"
      "ds_read_b64_tr_b16 v[50:51], v13 offset:6144\n"
      "ds_read_b64_tr_b16 v[52:53], v12 offset:4608\n"
      "ds_read_b64_tr_b16 v[54:55], v13 offset:6656\n"
      "v_exp_f32 v192, v176\n"
      "v_exp_f32 v193, v177\n"
      "v_exp_f32 v194, v178\n"
      "v_exp_f32 v195, v179\n"
      "v_exp_f32 v196, v180\n"
      "v_exp_f32 v197, v181\n"
      "s_waitcnt lgkmcnt(10)\n"
      "v_mfma_f32_32x32x16_bf16 v[64:79], v[32:35], v[24:27], v[64:79]\n"
      "ds_read_b64_tr_b16 v[56:57], v12 offset:5120\n"
      "ds_read_b64_tr_b16 v[58:59], v13 offset:7168\n"
      "v_exp_f32 v198, v182\n"
      "v_add_f32 v17, v192, v194\n"
      "s_waitcnt lgkmcnt(10)\n"
      "v_mfma_f32_32x32x16_bf16 v[80:95], v[36:39], v[24:27], v[80:95]\n"
      "ds_read_b64_tr_b16 v[60:61], v12 offset:5632\n"
      "ds_read_b64_tr_b16 v[62:63], v13 offset:7680\n"
      "v_exp_f32 v199, v183\n"
      "v_add_f32 v18, v193, v195\n"
      "s_waitcnt lgkmcnt(10)\n"
      "v_mfma_f32_32x32x16_bf16 v[96:111], v[40:43], v[24:27], v[96:111]\n"
      "v_exp_f32 v200, v184\n"
      "v_add_f32 v17, v17, v196\n"
      "v_exp_f32 v201, v185\n"
      "v_add_f32 v18, v18, v197\n"
      "s_waitcnt lgkmcnt(8)\n"
      "v_mfma_f32_32x32x16_bf16 v[112:127], v[44:47], v[24:27], v[112:127]\n"
      "v_exp_f32 v202, v186\n"
      "v_add_f32 v17, v17, v198\n"
      "v_exp_f32 v203, v187\n"
      "v_add_f32 v18, v18, v199\n"
      "s_waitcnt lgkmcnt(6)\n"
      "v_mfma_f32_32x32x16_bf16 v[64:79], v[48:51], v[28:31], v[64:79]\n"
      "v_exp_f32 v204, v188\n"
      "v_add_f32 v17, v17, v200\n"
      "v_exp_f32 v205, v189\n"
      "v_add_f32 v18, v18, v201\n"
      "s_waitcnt lgkmcnt(4)\n"
      "v_mfma_f32_32x32x16_bf16 v[80:95], v[52:55], v[28:31], v[80:95]\n"
      "v_exp_f32 v206, v190\n"
      "v_add_f32 v17, v17, v202\n"
      "v_exp_f32 v207, v191\n"
      "v_add_f32 v18, v18, v203\n"
      "s_waitcnt lgkmcnt(2)\n"
      "v_mfma_f32_32x32x16_bf16 v[96:111], v[56:59], v[28:31], v[96:111]\n"
      "v_add_f32 v17, v17, v204\n"
      "v_add_f32 v18, v18, v205\n"
      "v_add_f32 v17, v17, v206\n"
      "v_add_f32 v18, v18, v207\n"
      "s_waitcnt lgkmcnt(0)\n"
      "v_mfma_f32_32x32x16_bf16 v[112:127], v[60:63], v[28:31], v[112:127]\n"
      "v_add_f32 v17, v17, v18\n"
      "s_nop 0\n"
      "v_cmp_lt_f32 vcc, s67, v17\n"
      "s_cbranch_vccz .Lok31_%=\n"
      "s_swappc_b64 s[62:63], s[92:93]\n"
      ".Lok31_%=:\n"
      "v_add_f32 v16, v16, v17\n"
      "v_mov_b32 v2, v16\n"
      "v_mov_b32 v3, v16\n"
      "s_nop 1\n"
      "v_permlane32_swap_b32 v2, v3\n"
      "v_add_f32 v2, v2, v3\n"
      "v_rcp_f32 v6, v2\n"
      "v_rcp_f32 v7, v2\n"
      "s_nop 0\n"
      "v_pk_mul_f32 v[192:193], v[192:193], v[6:7]\n"
      "v_pk_mul_f32 v[194:195], v[194:195], v[6:7]\n"
      "v_pk_mul_f32 v[196:197], v[196:197], v[6:7]\n"
      "v_pk_mul_f32 v[198:199], v[198:199], v[6:7]\n"
      "v_pk_mul_f32 v[200:201], v[200:201], v[6:7]\n"
      "v_pk_mul_f32 v[202:203], v[202:203], v[6:7]\n"
      "v_pk_mul_f32 v[204:205], v[204:205], v[6:7]\n"
      "v_pk_mul_f32 v[206:207], v[206:207], v[6:7]\n"
      "v_cvt_pk_bf16_f32 v24, v192, v193\n"
      "v_cvt_pk_bf16_f32 v25, v194, v195\n"
      "v_cvt_pk_bf16_f32 v26, v196, v197\n"
      "v_cvt_pk_bf16_f32 v27, v198, v199\n"
      "v_cvt_pk_bf16_f32 v28, v200, v201\n"
      "v_cvt_pk_bf16_f32 v29, v202, v203\n"
      "v_cvt_pk_bf16_f32 v30, v204, v205\n"
      "v_cvt_pk_bf16_f32 v31, v206, v207\n"
      "v_pk_mul_f32 v[64:65], v[64:65], v[6:7]\n"
      "v_pk_mul_f32 v[66:67], v[66:67], v[6:7]\n"
      "v_pk_mul_f32 v[68:69], v[68:69], v[6:7]\n"
      "v_pk_mul_f32 v[70:71], v[70:71], v[6:7]\n"
      "v_pk_mul_f32 v[72:73], v[72:73], v[6:7]\n"
      "v_pk_mul_f32 v[74:75], v[74:75], v[6:7]\n"
      "v_pk_mul_f32 v[76:77], v[76:77], v[6:7]\n"
      "v_pk_mul_f32 v[78:79], v[78:79], v[6:7]\n"
      "v_pk_mul_f32 v[80:81], v[80:81], v[6:7]\n"
      "v_pk_mul_f32 v[82:83], v[82:83], v[6:7]\n"
      "v_pk_mul_f32 v[84:85], v[84:85], v[6:7]\n"
      "v_pk_mul_f32 v[86:87], v[86:87], v[6:7]\n"
      "v_pk_mul_f32 v[88:89], v[88:89], v[6:7]\n"
      "v_pk_mul_f32 v[90:91], v[90:91], v[6:7]\n"
      "v_pk_mul_f32 v[92:93], v[92:93], v[6:7]\n"
      "v_pk_mul_f32 v[94:95], v[94:95], v[6:7]\n"
      "v_pk_mul_f32 v[96:97], v[96:97], v[6:7]\n"
      "v_pk_mul_f32 v[98:99], v[98:99], v[6:7]\n"
      "v_pk_mul_f32 v[100:101], v[100:101], v[6:7]\n"
      "v_pk_mul_f32 v[102:103], v[102:103], v[6:7]\n"
      "v_pk_mul_f32 v[104:105], v[104:105], v[6:7]\n"
      "v_pk_mul_f32 v[106:107], v[106:107], v[6:7]\n"
      "v_pk_mul_f32 v[108:109], v[108:109], v[6:7]\n"
      "v_pk_mul_f32 v[110:111], v[110:111], v[6:7]\n"
      "v_pk_mul_f32 v[112:113], v[112:113], v[6:7]\n"
      "v_pk_mul_f32 v[114:115], v[114:115], v[6:7]\n"
      "v_pk_mul_f32 v[116:117], v[116:117], v[6:7]\n"
      "v_pk_mul_f32 v[118:119], v[118:119], v[6:7]\n"
      "v_pk_mul_f32 v[120:121], v[120:121], v[6:7]\n"
      "v_pk_mul_f32 v[122:123], v[122:123], v[6:7]\n"
      "v_pk_mul_f32 v[124:125], v[124:125], v[6:7]\n"
      "v_pk_mul_f32 v[126:127], v[126:127], v[6:7]\n"
      "s_sub_u32 s29, s76, s26\n"
      "s_mul_i32 s29, s29, 12288\n"
      "s_mul_i32 s30, s26, 12288\n"
      "v_subrev_u32 v2, s30, v19\n"
      "v_add_u32 v9, s29, v19\n"
      "v_add_u32 v3, 98304, v19\n"
      "v_cmp_gt_u32 vcc, 0x3000, v2\n"
      "s_nop 1\n"
      "v_cndmask_b32 v9, v3, v9, vcc\n"
      "s_lshl_b32 s29, s20, 1\n"
      "s_add_u32 s29, s29, 0\n"
      "s_sub_u32 s30, s21, s29\n"
      "s_and_b32 s30, s30, 7\n"
      "s_lshl_b32 s31, s28, 3\n"
      "s_add_u32 s29, s29, s31\n"
      "s_mul_i32 s29, s29, 26880\n"
      "s_mul_i32 s31, s30, 3840\n"
      "s_add_u32 s29, s29, s31\n"
      "s_add_u32 s45, s29, 8384768\n"
      "s_cmp_eq_u32 s30, 0\n"
      "s_cselect_b32 s31, 0x10000000, 0\n"
      "v_add_u32 v20, s31, v21\n"
      "v_lshrrev_b32 v22, 2, v20\n"
      "s_lshl_b32 s29, s20, 1\n"
      "s_add_u32 s29, s29, 1\n"
      "s_sub_u32 s30, s21, s29\n"
      "s_and_b32 s30, s30, 7\n"
      "s_lshl_b32 s31, s28, 3\n"
      "s_add_u32 s29, s29, s31\n"
      "s_mul_i32 s29, s29, 26880\n"
      "s_mul_i32 s31, s30, 3840\n"
      "s_add_u32 s29, s29, s31\n"
      "s_add_u32 s47, s29, 8384768\n"
      "s_cmp_eq_u32 s30, 0\n"
      "s_cselect_b32 s31, 0x10000000, 0\n"
      "v_add_u32 v4, s31, v21\n"
      "v_lshrrev_b32 v23, 2, v4\n"
      "s_mov_b32 s46, 0xffffff00\n"
      "s_mov_b32 s40, 15\n"
      "s_mov_b32 s42, 0\n"
      "s_lshl_b32 s29, s20, 2\n"
      "s_add_u32 s44, s29, 49168\n"
      "s_mul_i32 s30, %1, 26880\n"
      "s_mul_i32 s31, s20, 1280\n"
      "s_add_u32 s30, s30, s31\n"
      "s_add_u32 s48, s30, 8388608\n"
      "s_and_b32 s71, s60, -1\n"
      "v_mov_b32 v5, s71\n"
      "s_lshl_b32 s43, 1, s76\n"
      "s_mov_b32 s41, 1\n"
      "s_add_u32 s29, s76, s20\n"
      "s_add_u32 s29, s29, 1\n"
      "s_and_b32 s38, s29, 3\n"
      "s_mul_i32 s39, s38, 12288\n"
      "s_add_u32 s29, s22, s20\n"
      "s_add_u32 s29, s29, 1\n"
      "s_and_b32 s29, s29, 3\n"
      "s_sub_u32 s31, s25, s22\n"
      "s_add_u32 s31, s31, s29\n"
      "s_mul_i32 s29, s31, 24576\n"
      "s_mul_i32 s30, s26, 12288\n"
      "s_add_u32 s29, s29, s30\n"
      "s_add_u32 s36, s29, 1048576\n"
      "s_lshl_b32 s31, s31, 7\n"
      "s_add_u32 s37, s31, 4096\n"
      "s_lshl_b32 s69, 3, s26\n"
      "v_and_b32 v8, 3, v1\n"
      "v_lshlrev_b32 v8, 3, v8\n"
      "s_barrier\n"
      "ds_read_b64_tr_b16 v[32:33], v12 offset:8192\n"
      "ds_read_b64_tr_b16 v[34:35], v13 offset:10240\n"
      "ds_read_b64_tr_b16 v[36:37], v12 offset:12288\n"
      "ds_read_b64_tr_b16 v[38:39], v13 offset:14336\n"
      "ds_read_b64_tr_b16 v[40:41], v12 offset:8704\n"
      "ds_read_b64_tr_b16 v[42:43], v13 offset:10752\n"
      "ds_read_b64_tr_b16 v[44:45], v12 offset:12800\n"
      "ds_read_b64_tr_b16 v[46:47], v13 offset:14848\n"
      "ds_read_b64_tr_b16 v[48:49], v12 offset:9216\n"
      "ds_read_b64_tr_b16 v[50:51], v13 offset:11264\n"
      "ds_read_b64_tr_b16 v[52:53], v12 offset:13312\n"
      "ds_read_b64_tr_b16 v[54:55], v13 offset:15360\n"
      "ds_read_b64_tr_b16 v[56:57], v12 offset:9728\n"
      "ds_read_b64_tr_b16 v[58:59], v13 offset:11776\n"
      "s_waitcnt lgkmcnt(12)\n"
      "v_mfma_f32_32x32x16_bf16 v[64:79], v[32:35], v[24:27], v[64:79]\n"
      "ds_read_b64_tr_b16 v[60:61], v12 offset:13824\n"
      "ds_read_b64_tr_b16 v[62:63], v13 offset:15872\n"
      "s_waitcnt lgkmcnt(12)\n"
      "v_mfma_f32_32x32x16_bf16 v[64:79], v[36:39], v[28:31], v[64:79]\n"
      "s_waitcnt lgkmcnt(10)\n"
      "v_mfma_f32_32x32x16_bf16 v[80:95], v[40:43], v[24:27], v[80:95]\n"
      "s_waitcnt lgkmcnt(8)\n"
      "v_mfma_f32_32x32x16_bf16 v[80:95], v[44:47], v[28:31], v[80:95]\n"
      "s_waitcnt lgkmcnt(6)\n"
      "v_mfma_f32_32x32x16_bf16 v[96:111], v[48:51], v[24:27], v[96:111]\n"
      "s_waitcnt lgkmcnt(4)\n"
      "v_mfma_f32_32x32x16_bf16 v[96:111], v[52:55], v[28:31], v[96:111]\n"
      "s_waitcnt lgkmcnt(2)\n"
      "v_mfma_f32_32x32x16_bf16 v[112:127], v[56:59], v[24:27], v[112:127]\n"
      "s_waitcnt lgkmcnt(0)\n"
      "v_mfma_f32_32x32x16_bf16 v[112:127], v[60:63], v[28:31], v[112:127]\n"
      "s_nop 1\n"
      "v_cvt_pk_bf16_f32 v224, v64, v65\n"
      "v_cvt_pk_bf16_f32 v225, v66, v67\n"
      "v_cvt_pk_bf16_f32 v226, v68, v69\n"
      "v_cvt_pk_bf16_f32 v227, v70, v71\n"
      "v_cvt_pk_bf16_f32 v228, v72, v73\n"
      "v_cvt_pk_bf16_f32 v229, v74, v75\n"
      "v_cvt_pk_bf16_f32 v230, v76, v77\n"
      "v_cvt_pk_bf16_f32 v231, v78, v79\n"
      "buffer_store_dwordx4 v[224:227], v19, s[52:55], s78 offen offset:0 sc1\n"
      "buffer_store_dwordx4 v[228:231], v19, s[52:55], s78 offen offset:512 sc1\n"
      "ds_write_b128 v9, v[224:227] offset:0\n"
      "ds_write_b128 v9, v[228:231] offset:512\n"
      "v_cvt_pk_bf16_f32 v232, v80, v81\n"
      "v_cvt_pk_bf16_f32 v233, v82, v83\n"
      "v_cvt_pk_bf16_f32 v234, v84, v85\n"
      "v_cvt_pk_bf16_f32 v235, v86, v87\n"
      "v_cvt_pk_bf16_f32 v236, v88, v89\n"
      "v_cvt_pk_bf16_f32 v237, v90, v91\n"
      "v_cvt_pk_bf16_f32 v238, v92, v93\n"
      "v_cvt_pk_bf16_f32 v239, v94, v95\n"
      "buffer_store_dwordx4 v[232:235], v19, s[52:55], s78 offen offset:1024 sc1\n"
      "buffer_store_dwordx4 v[236:239], v19, s[52:55], s78 offen offset:1536 sc1\n"
      "ds_write_b128 v9, v[232:235] offset:1024\n"
      "ds_write_b128 v9, v[236:239] offset:1536\n"
      "v_cvt_pk_bf16_f32 v240, v96, v97\n"
      "v_cvt_pk_bf16_f32 v241, v98, v99\n"
      "v_cvt_pk_bf16_f32 v242, v100, v101\n"
      "v_cvt_pk_bf16_f32 v243, v102, v103\n"
      "v_cvt_pk_bf16_f32 v244, v104, v105\n"
      "v_cvt_pk_bf16_f32 v245, v106, v107\n"
      "v_cvt_pk_bf16_f32 v246, v108, v109\n"
      "v_cvt_pk_bf16_f32 v247, v110, v111\n"
      "buffer_store_dwordx4 v[240:243], v19, s[52:55], s78 offen offset:2048 sc1\n"
      "buffer_store_dwordx4 v[244:247], v19, s[52:55], s78 offen offset:2560 sc1\n"
      "ds_write_b128 v9, v[240:243] offset:2048\n"
      "ds_write_b128 v9, v[244:247] offset:2560\n"
      "v_cvt_pk_bf16_f32 v248, v112, v113\n"
      "v_cvt_pk_bf16_f32 v249, v114, v115\n"
      "v_cvt_pk_bf16_f32 v250, v116, v117\n"
      "v_cvt_pk_bf16_f32 v251, v118, v119\n"
      "v_cvt_pk_bf16_f32 v252, v120, v121\n"
      "v_cvt_pk_bf16_f32 v253, v122, v123\n"
      "v_cvt_pk_bf16_f32 v254, v124, v125\n"
      "v_cvt_pk_bf16_f32 v255, v126, v127\n"
      "buffer_store_dwordx4 v[248:251], v19, s[52:55], s78 offen offset:3072 sc1\n"
      "buffer_store_dwordx4 v[252:255], v19, s[52:55], s78 offen offset:3584 sc1\n"
      "ds_write_b128 v9, v[248:251] offset:3072\n"
      "ds_write_b128 v9, v[252:255] offset:3584\n"
      ".Ltail_%=:\n"
      "s_waitcnt lgkmcnt(0)\n"
      "s_barrier\n"
      "s_branch .Lr_run_%=\n"
      ".Lround_%=:\n"
      "s_cmp_eq_u32 s41, 0\n"
      "s_cbranch_scc1 .Lr_state_%=\n"
      "s_mov_b32 s41, 0\n"
      "s_lshl_b32 s49, 1, s38\n"
      "v_mov_b32 v2, s60\n"
      "v_mov_b32 v3, s61\n"
      "v_mov_b32 v6, 49152\n"
      "ds_read_b32 v6, v6\n"
      "s_waitcnt vmcnt(0)\n"
      "s_mov_b64 exec, 1\n"
      "buffer_store_dwordx2 v[2:3], v21, s[52:55], s79 offen sc1\n"
      "s_mov_b64 exec, -1\n"
      "s_waitcnt lgkmcnt(0)\n"
      "v_readfirstlane_b32 s29, v6\n"
      "s_nop 1\n"
      "s_and_b32 s29, s29, s49\n"
      "s_cselect_b32 s75, 0, 1\n"
      "s_cselect_b32 s49, 0, s49\n"
      ".Lr_state_%=:\n"
      "s_cmp_eq_u32 s75, 0\n"
      "s_cbranch_scc1 .Lr_wait_%=\n"
      ".Lr_poll_%=:\n"
      "buffer_load_dwordx2 v[6:7], v8, s[52:55], s37 offen sc1\n"
      "s_add_u32 s68, s68, 1\n"
      "s_waitcnt vmcnt(0)\n"
      "v_cmp_eq_u64 vcc, s[60:61], v[6:7]\n"
      "s_cmp_gt_u32 s68, 0x20000\n"
      "s_cselect_b32 s71, -1, 0\n"
      "s_or_b32 s71, s71, vcc_lo\n"
      "s_and_b32 s29, s71, s69\n"
      "s_cmp_lg_u32 s29, s69\n"
      "s_cbranch_scc1 .Lf0_%=\n"
      "s_mov_b32 s75, 0\n"
      "s_add_u32 m0, s39, 0\n"
      "s_add_u32 s66, s36, 0\n"
      "buffer_load_dwordx4 v21, s[52:55], s66 offen sc1 lds\n"
      "buffer_load_dwordx4 v21, s[52:55], s66 offen offset:1024 sc1 lds\n"
      "buffer_load_dwordx4 v21, s[52:55], s66 offen offset:2048 sc1 lds\n"
      "buffer_load_dwordx4 v21, s[52:55], s66 offen offset:3072 sc1 lds\n"
      "s_add_u32 m0, s39, 4096\n"
      "s_add_u32 s66, s36, 4096\n"
      "buffer_load_dwordx4 v21, s[52:55], s66 offen sc1 lds\n"
      "buffer_load_dwordx4 v21, s[52:55], s66 offen offset:1024 sc1 lds\n"
      "buffer_load_dwordx4 v21, s[52:55], s66 offen offset:2048 sc1 lds\n"
      "buffer_load_dwordx4 v21, s[52:55], s66 offen offset:3072 sc1 lds\n"
      "s_add_u32 m0, s39, 8192\n"
      "s_add_u32 s66, s36, 8192\n"
      "buffer_load_dwordx4 v21, s[52:55], s66 offen sc1 lds\n"
      "buffer_load_dwordx4 v21, s[52:55], s66 offen offset:1024 sc1 lds\n"
      "buffer_load_dwordx4 v21, s[52:55], s66 offen offset:2048 sc1 lds\n"
      "buffer_load_dwordx4 v21, s[52:55], s66 offen offset:3072 sc1 lds\n"
      ".Lf0_%=:\n"
      "s_branch .Lr_pub_%=\n"
      ".Lr_wait_%=:\n"
      "s_waitcnt vmcnt(0)\n"
      "s_mov_b32 s42, s49\n"
      ".Lr_pub_%=:\n"
      "v_mov_b32 v2, s44\n"
      "v_mov_b32 v3, s42\n"
      "ds_write_b32 v2, v3\n"
      "v_and_b32 v2, 0xfffffff0, v2\n"
      "s_xor_b32 s44, s44, 48\n"
      "s_waitcnt lgkmcnt(0)\n"
      "s_barrier\n"
      "ds_read_b128 v[24:27], v2\n"
      "s_waitcnt lgkmcnt(0)\n"
      "v_or3_b32 v24, v24, v25, v26\n"
      "v_or_b32 v24, v24, v27\n"
      "s_nop 0\n"
      "v_readfirstlane_b32 s43, v24\n"
      "s_nop 1\n"
      "s_and_b32 s43, s43, s40\n"
      ".Lr_run_%=:\n"
      "s_bitcmp0_b32 s43, 0\n"
      "s_cbranch_scc1 .Lr_skip0_%=\n"
      "s_bitset0_b32 s40, 0\n"
      "s_cmp_eq_u32 s40, 0\n"
      "s_cbranch_scc1 .Lr_last0_%=\n"
      "ds_read_b128 v[24:27], v21 offset:0\n"
      "ds_read_b128 v[28:31], v21 offset:1024\n"
      "ds_read_b128 v[32:35], v21 offset:2048\n"
      "ds_read_b128 v[36:39], v21 offset:3072\n"
      "ds_read_b128 v[40:43], v21 offset:4096\n"
      "ds_read_b128 v[44:47], v21 offset:5120\n"
      "ds_read_b128 v[48:51], v21 offset:6144\n"
      "ds_read_b128 v[52:55], v21 offset:7168\n"
      "ds_read_b128 v[56:59], v21 offset:8192\n"
      "ds_read_b128 v[60:63], v21 offset:9216\n"
      "ds_read_b128 v[10:13], v21 offset:10240\n"
      "ds_read_b128 v[16:19], v21 offset:11264\n"
      "s_waitcnt lgkmcnt(11)\n"
      "v_mfma_f32_16x16x32_bf16 v[64:67], a[0:3], v[24:27], 0\n"
      "v_mfma_f32_16x16x32_bf16 v[68:71], a[4:7], v[24:27], 0\n"
      "v_mfma_f32_16x16x32_bf16 v[72:75], a[8:11], v[24:27], 0\n"
      "v_mfma_f32_16x16x32_bf16 v[76:79], a[12:15], v[24:27], 0\n"
      "s_waitcnt lgkmcnt(10)\n"
      "v_mfma_f32_16x16x32_bf16 v[64:67], a[16:19], v[28:31], v[64:67]\n"
      "v_mfma_f32_16x16x32_bf16 v[68:71], a[20:23], v[28:31], v[68:71]\n"
      "v_mfma_f32_16x16x32_bf16 v[72:75], a[24:27], v[28:31], v[72:75]\n"
      "v_mfma_f32_16x16x32_bf16 v[76:79], a[28:31], v[28:31], v[76:79]\n"
      "s_waitcnt lgkmcnt(9)\n"
      "v_mfma_f32_16x16x32_bf16 v[64:67], a[32:35], v[32:35], v[64:67]\n"
      "v_mfma_f32_16x16x32_bf16 v[68:71], a[36:39], v[32:35], v[68:71]\n"
      "v_mfma_f32_16x16x32_bf16 v[72:75], a[40:43], v[32:35], v[72:75]\n"
      "v_mfma_f32_16x16x32_bf16 v[76:79], a[44:47], v[32:35], v[76:79]\n"
      "s_waitcnt lgkmcnt(8)\n"
      "v_mfma_f32_16x16x32_bf16 v[64:67], a[48:51], v[36:39], v[64:67]\n"
      "v_mfma_f32_16x16x32_bf16 v[68:71], a[52:55], v[36:39], v[68:71]\n"
      "v_mfma_f32_16x16x32_bf16 v[72:75], a[56:59], v[36:39], v[72:75]\n"
      "v_mfma_f32_16x16x32_bf16 v[76:79], a[60:63], v[36:39], v[76:79]\n"
      "s_waitcnt lgkmcnt(7)\n"
      "v_mfma_f32_16x16x32_bf16 v[80:83], a[0:3], v[40:43], 0\n"
      "v_mfma_f32_16x16x32_bf16 v[84:87], a[4:7], v[40:43], 0\n"
      "v_mfma_f32_16x16x32_bf16 v[88:91], a[8:11], v[40:43], 0\n"
      "v_mfma_f32_16x16x32_bf16 v[92:95], a[12:15], v[40:43], 0\n"
      "s_waitcnt lgkmcnt(6)\n"
      "v_mfma_f32_16x16x32_bf16 v[80:83], a[16:19], v[44:47], v[80:83]\n"
      "v_mfma_f32_16x16x32_bf16 v[84:87], a[20:23], v[44:47], v[84:87]\n"
      "v_mfma_f32_16x16x32_bf16 v[88:91], a[24:27], v[44:47], v[88:91]\n"
      "v_mfma_f32_16x16x32_bf16 v[92:95], a[28:31], v[44:47], v[92:95]\n"
      "s_waitcnt lgkmcnt(5)\n"
      "v_mfma_f32_16x16x32_bf16 v[80:83], a[32:35], v[48:51], v[80:83]\n"
      "v_mfma_f32_16x16x32_bf16 v[84:87], a[36:39], v[48:51], v[84:87]\n"
      "v_mfma_f32_16x16x32_bf16 v[88:91], a[40:43], v[48:51], v[88:91]\n"
      "v_mfma_f32_16x16x32_bf16 v[92:95], a[44:47], v[48:51], v[92:95]\n"
      "s_waitcnt lgkmcnt(4)\n"
      "v_mfma_f32_16x16x32_bf16 v[80:83], a[48:51], v[52:55], v[80:83]\n"
      "v_mfma_f32_16x16x32_bf16 v[84:87], a[52:55], v[52:55], v[84:87]\n"
      "v_mfma_f32_16x16x32_bf16 v[88:91], a[56:59], v[52:55], v[88:91]\n"
      "v_mfma_f32_16x16x32_bf16 v[92:95], a[60:63], v[52:55], v[92:95]\n"
      "s_waitcnt lgkmcnt(3)\n"
      "v_mfma_f32_16x16x32_bf16 v[96:99], a[0:3], v[56:59], 0\n"
      "v_mfma_f32_16x16x32_bf16 v[100:103], a[4:7], v[56:59], 0\n"
      "v_mfma_f32_16x16x32_bf16 v[104:107], a[8:11], v[56:59], 0\n"
      "v_mfma_f32_16x16x32_bf16 v[108:111], a[12:15], v[56:59], 0\n"
      "s_waitcnt lgkmcnt(2)\n"
      "v_mfma_f32_16x16x32_bf16 v[96:99], a[16:19], v[60:63], v[96:99]\n"
      "v_mfma_f32_16x16x32_bf16 v[100:103], a[20:23], v[60:63], v[100:103]\n"
      "v_mfma_f32_16x16x32_bf16 v[104:107], a[24:27], v[60:63], v[104:107]\n"
      "v_mfma_f32_16x16x32_bf16 v[108:111], a[28:31], v[60:63], v[108:111]\n"
      "s_waitcnt lgkmcnt(1)\n"
      "v_mfma_f32_16x16x32_bf16 v[96:99], a[32:35], v[10:13], v[96:99]\n"
      "v_mfma_f32_16x16x32_bf16 v[100:103], a[36:39], v[10:13], v[100:103]\n"
      "v_mfma_f32_16x16x32_bf16 v[104:107], a[40:43], v[10:13], v[104:107]\n"
      "v_mfma_f32_16x16x32_bf16 v[108:111], a[44:47], v[10:13], v[108:111]\n"
      "s_waitcnt lgkmcnt(0)\n"
      "v_mfma_f32_16x16x32_bf16 v[96:99], a[48:51], v[16:19], v[96:99]\n"
      "v_mfma_f32_16x16x32_bf16 v[100:103], a[52:55], v[16:19], v[100:103]\n"
      "v_mfma_f32_16x16x32_bf16 v[104:107], a[56:59], v[16:19], v[104:107]\n"
      "v_mfma_f32_16x16x32_bf16 v[108:111], a[60:63], v[16:19], v[108:111]\n"
      ".Lr_skip0_%=:\n"
      "s_bitcmp0_b32 s43, 1\n"
      "s_cbranch_scc1 .Lr_skip1_%=\n"
      "s_bitset0_b32 s40, 1\n"
      "s_cmp_eq_u32 s40, 0\n"
      "s_cbranch_scc1 .Lr_last1_%=\n"
      "ds_read_b128 v[24:27], v21 offset:12288\n"
      "ds_read_b128 v[28:31], v21 offset:13312\n"
      "ds_read_b128 v[32:35], v21 offset:14336\n"
      "ds_read_b128 v[36:39], v21 offset:15360\n"
      "ds_read_b128 v[40:43], v21 offset:16384\n"
      "ds_read_b128 v[44:47], v21 offset:17408\n"
      "ds_read_b128 v[48:51], v21 offset:18432\n"
      "ds_read_b128 v[52:55], v21 offset:19456\n"
      "ds_read_b128 v[56:59], v21 offset:20480\n"
      "ds_read_b128 v[60:63], v21 offset:21504\n"
      "ds_read_b128 v[10:13], v21 offset:22528\n"
      "ds_read_b128 v[16:19], v21 offset:23552\n"
      "s_waitcnt lgkmcnt(11)\n"
      "v_mfma_f32_16x16x32_bf16 v[112:115], a[64:67], v[24:27], 0\n"
      "v_mfma_f32_16x16x32_bf16 v[116:119], a[68:71], v[24:27], 0\n"
      "v_mfma_f32_16x16x32_bf16 v[120:123], a[72:75], v[24:27], 0\n"
      "v_mfma_f32_16x16x32_bf16 v[124:127], a[76:79], v[24:27], 0\n"
      "s_waitcnt lgkmcnt(10)\n"
      "v_mfma_f32_16x16x32_bf16 v[112:115], a[80:83], v[28:31], v[112:115]\n"
      "v_mfma_f32_16x16x32_bf16 v[116:119], a[84:87], v[28:31], v[116:119]\n"
      "v_mfma_f32_16x16x32_bf16 v[120:123], a[88:91], v[28:31], v[120:123]\n"
      "v_mfma_f32_16x16x32_bf16 v[124:127], a[92:95], v[28:31], v[124:127]\n"
      "s_waitcnt lgkmcnt(9)\n"
      "v_mfma_f32_16x16x32_bf16 v[112:115], a[96:99], v[32:35], v[112:115]\n"
      "v_mfma_f32_16x16x32_bf16 v[116:119], a[100:103], v[32:35], v[116:119]\n"
      "v_mfma_f32_16x16x32_bf16 v[120:123], a[104:107], v[32:35], v[120:123]\n"
      "v_mfma_f32_16x16x32_bf16 v[124:127], a[108:111], v[32:35], v[124:127]\n"
      "s_waitcnt lgkmcnt(8)\n"
      "v_mfma_f32_16x16x32_bf16 v[112:115], a[112:115], v[36:39], v[112:115]\n"
      "v_mfma_f32_16x16x32_bf16 v[116:119], a[116:119], v[36:39], v[116:119]\n"
      "v_mfma_f32_16x16x32_bf16 v[120:123], a[120:123], v[36:39], v[120:123]\n"
      "v_mfma_f32_16x16x32_bf16 v[124:127], a[124:127], v[36:39], v[124:127]\n"
      "s_waitcnt lgkmcnt(7)\n"
      "v_mfma_f32_16x16x32_bf16 v[128:131], a[64:67], v[40:43], 0\n"
      "v_mfma_f32_16x16x32_bf16 v[132:135], a[68:71], v[40:43], 0\n"
      "v_mfma_f32_16x16x32_bf16 v[136:139], a[72:75], v[40:43], 0\n"
      "v_mfma_f32_16x16x32_bf16 v[140:143], a[76:79], v[40:43], 0\n"
      "s_waitcnt lgkmcnt(6)\n"
      "v_mfma_f32_16x16x32_bf16 v[128:131], a[80:83], v[44:47], v[128:131]\n"
      "v_mfma_f32_16x16x32_bf16 v[132:135], a[84:87], v[44:47], v[132:135]\n"
      "v_mfma_f32_16x16x32_bf16 v[136:139], a[88:91], v[44:47], v[136:139]\n"
      "v_mfma_f32_16x16x32_bf16 v[140:143], a[92:95], v[44:47], v[140:143]\n"
      "s_waitcnt lgkmcnt(5)\n"
      "v_mfma_f32_16x16x32_bf16 v[128:131], a[96:99], v[48:51], v[128:131]\n"
      "v_mfma_f32_16x16x32_bf16 v[132:135], a[100:103], v[48:51], v[132:135]\n"
      "v_mfma_f32_16x16x32_bf16 v[136:139], a[104:107], v[48:51], v[136:139]\n"
      "v_mfma_f32_16x16x32_bf16 v[140:143], a[108:111], v[48:51], v[140:143]\n"
      "s_waitcnt lgkmcnt(4)\n"
      "v_mfma_f32_16x16x32_bf16 v[128:131], a[112:115], v[52:55], v[128:131]\n"
      "v_mfma_f32_16x16x32_bf16 v[132:135], a[116:119], v[52:55], v[132:135]\n"
      "v_mfma_f32_16x16x32_bf16 v[136:139], a[120:123], v[52:55], v[136:139]\n"
      "v_mfma_f32_16x16x32_bf16 v[140:143], a[124:127], v[52:55], v[140:143]\n"
      "s_waitcnt lgkmcnt(3)\n"
      "v_mfma_f32_16x16x32_bf16 v[144:147], a[64:67], v[56:59], 0\n"
      "v_mfma_f32_16x16x32_bf16 v[148:151], a[68:71], v[56:59], 0\n"
      "v_mfma_f32_16x16x32_bf16 v[152:155], a[72:75], v[56:59], 0\n"
      "v_mfma_f32_16x16x32_bf16 v[156:159], a[76:79], v[56:59], 0\n"
      "s_waitcnt lgkmcnt(2)\n"
      "v_mfma_f32_16x16x32_bf16 v[144:147], a[80:83], v[60:63], v[144:147]\n"
      "v_mfma_f32_16x16x32_bf16 v[148:151], a[84:87], v[60:63], v[148:151]\n"
      "v_mfma_f32_16x16x32_bf16 v[152:155], a[88:91], v[60:63], v[152:155]\n"
      "v_mfma_f32_16x16x32_bf16 v[156:159], a[92:95], v[60:63], v[156:159]\n"
      "s_waitcnt lgkmcnt(1)\n"
      "v_mfma_f32_16x16x32_bf16 v[144:147], a[96:99], v[10:13], v[144:147]\n"
      "v_mfma_f32_16x16x32_bf16 v[148:151], a[100:103], v[10:13], v[148:151]\n"
      "v_mfma_f32_16x16x32_bf16 v[152:155], a[104:107], v[10:13], v[152:155]\n"
      "v_mfma_f32_16x16x32_bf16 v[156:159], a[108:111], v[10:13], v[156:159]\n"
      "s_waitcnt lgkmcnt(0)\n"
      "v_mfma_f32_16x16x32_bf16 v[144:147], a[112:115], v[16:19], v[144:147]\n"
      "v_mfma_f32_16x16x32_bf16 v[148:151], a[116:119], v[16:19], v[148:151]\n"
      "v_mfma_f32_16x16x32_bf16 v[152:155], a[120:123], v[16:19], v[152:155]\n"
      "v_mfma_f32_16x16x32_bf16 v[156:159], a[124:127], v[16:19], v[156:159]\n"
      ".Lr_skip1_%=:\n"
      "s_bitcmp0_b32 s43, 2\n"
      "s_cbranch_scc1 .Lr_skip2_%=\n"
      "s_bitset0_b32 s40, 2\n"
      "s_cmp_eq_u32 s40, 0\n"
      "s_cbranch_scc1 .Lr_last2_%=\n"
      "ds_read_b128 v[24:27], v21 offset:24576\n"
      "ds_read_b128 v[28:31], v21 offset:25600\n"
      "ds_read_b128 v[32:35], v21 offset:26624\n"
      "ds_read_b128 v[36:39], v21 offset:27648\n"
      "ds_read_b128 v[40:43], v21 offset:28672\n"
      "ds_read_b128 v[44:47], v21 offset:29696\n"
      "ds_read_b128 v[48:51], v21 offset:30720\n"
      "ds_read_b128 v[52:55], v21 offset:31744\n"
      "ds_read_b128 v[56:59], v21 offset:32768\n"
      "ds_read_b128 v[60:63], v21 offset:33792\n"
      "ds_read_b128 v[10:13], v21 offset:34816\n"
      "ds_read_b128 v[16:19], v21 offset:35840\n"
      "s_waitcnt lgkmcnt(11)\n"
      "v_mfma_f32_16x16x32_bf16 v[160:163], a[128:131], v[24:27], 0\n"
      "v_mfma_f32_16x16x32_bf16 v[164:167], a[132:135], v[24:27], 0\n"
      "v_mfma_f32_16x16x32_bf16 v[168:171], a[136:139], v[24:27], 0\n"
      "v_mfma_f32_16x16x32_bf16 v[172:175], a[140:143], v[24:27], 0\n"
      "s_waitcnt lgkmcnt(10)\n"
      "v_mfma_f32_16x16x32_bf16 v[160:163], a[144:147], v[28:31], v[160:163]\n"
      "v_mfma_f32_16x16x32_bf16 v[164:167], a[148:151], v[28:31], v[164:167]\n"
      "v_mfma_f32_16x16x32_bf16 v[168:171], a[152:155], v[28:31], v[168:171]\n"
      "v_mfma_f32_16x16x32_bf16 v[172:175], a[156:159], v[28:31], v[172:175]\n"
      "s_waitcnt lgkmcnt(9)\n"
      "v_mfma_f32_16x16x32_bf16 v[160:163], a[160:163], v[32:35], v[160:163]\n"
      "v_mfma_f32_16x16x32_bf16 v[164:167], a[164:167], v[32:35], v[164:167]\n"
      "v_mfma_f32_16x16x32_bf16 v[168:171], a[168:171], v[32:35], v[168:171]\n"
      "v_mfma_f32_16x16x32_bf16 v[172:175], a[172:175], v[32:35], v[172:175]\n"
      "s_waitcnt lgkmcnt(8)\n"
      "v_mfma_f32_16x16x32_bf16 v[160:163], a[176:179], v[36:39], v[160:163]\n"
      "v_mfma_f32_16x16x32_bf16 v[164:167], a[180:183], v[36:39], v[164:167]\n"
      "v_mfma_f32_16x16x32_bf16 v[168:171], a[184:187], v[36:39], v[168:171]\n"
      "v_mfma_f32_16x16x32_bf16 v[172:175], a[188:191], v[36:39], v[172:175]\n"
      "s_waitcnt lgkmcnt(7)\n"
      "v_mfma_f32_16x16x32_bf16 v[176:179], a[128:131], v[40:43], 0\n"
      "v_mfma_f32_16x16x32_bf16 v[180:183], a[132:135], v[40:43], 0\n"
      "v_mfma_f32_16x16x32_bf16 v[184:187], a[136:139], v[40:43], 0\n"
      "v_mfma_f32_16x16x32_bf16 v[188:191], a[140:143], v[40:43], 0\n"
      "s_waitcnt lgkmcnt(6)\n"
      "v_mfma_f32_16x16x32_bf16 v[176:179], a[144:147], v[44:47], v[176:179]\n"
      "v_mfma_f32_16x16x32_bf16 v[180:183], a[148:151], v[44:47], v[180:183]\n"
      "v_mfma_f32_16x16x32_bf16 v[184:187], a[152:155], v[44:47], v[184:187]\n"
      "v_mfma_f32_16x16x32_bf16 v[188:191], a[156:159], v[44:47], v[188:191]\n"
      "s_waitcnt lgkmcnt(5)\n"
      "v_mfma_f32_16x16x32_bf16 v[176:179], a[160:163], v[48:51], v[176:179]\n"
      "v_mfma_f32_16x16x32_bf16 v[180:183], a[164:167], v[48:51], v[180:183]\n"
      "v_mfma_f32_16x16x32_bf16 v[184:187], a[168:171], v[48:51], v[184:187]\n"
      "v_mfma_f32_16x16x32_bf16 v[188:191], a[172:175], v[48:51], v[188:191]\n"
      "s_waitcnt lgkmcnt(4)\n"
      "v_mfma_f32_16x16x32_bf16 v[176:179], a[176:179], v[52:55], v[176:179]\n"
      "v_mfma_f32_16x16x32_bf16 v[180:183], a[180:183], v[52:55], v[180:183]\n"
      "v_mfma_f32_16x16x32_bf16 v[184:187], a[184:187], v[52:55], v[184:187]\n"
      "v_mfma_f32_16x16x32_bf16 v[188:191], a[188:191], v[52:55], v[188:191]\n"
      "s_waitcnt lgkmcnt(3)\n"
      "v_mfma_f32_16x16x32_bf16 v[192:195], a[128:131], v[56:59], 0\n"
      "v_mfma_f32_16x16x32_bf16 v[196:199], a[132:135], v[56:59], 0\n"
      "v_mfma_f32_16x16x32_bf16 v[200:203], a[136:139], v[56:59], 0\n"
      "v_mfma_f32_16x16x32_bf16 v[204:207], a[140:143], v[56:59], 0\n"
      "s_waitcnt lgkmcnt(2)\n"
      "v_mfma_f32_16x16x32_bf16 v[192:195], a[144:147], v[60:63], v[192:195]\n"
      "v_mfma_f32_16x16x32_bf16 v[196:199], a[148:151], v[60:63], v[196:199]\n"
      "v_mfma_f32_16x16x32_bf16 v[200:203], a[152:155], v[60:63], v[200:203]\n"
      "v_mfma_f32_16x16x32_bf16 v[204:207], a[156:159], v[60:63], v[204:207]\n"
      "s_waitcnt lgkmcnt(1)\n"
      "v_mfma_f32_16x16x32_bf16 v[192:195], a[160:163], v[10:13], v[192:195]\n"
      "v_mfma_f32_16x16x32_bf16 v[196:199], a[164:167], v[10:13], v[196:199]\n"
      "v_mfma_f32_16x16x32_bf16 v[200:203], a[168:171], v[10:13], v[200:203]\n"
      "v_mfma_f32_16x16x32_bf16 v[204:207], a[172:175], v[10:13], v[204:207]\n"
      "s_waitcnt lgkmcnt(0)\n"
      "v_mfma_f32_16x16x32_bf16 v[192:195], a[176:179], v[16:19], v[192:195]\n"
      "v_mfma_f32_16x16x32_bf16 v[196:199], a[180:183], v[16:19], v[196:199]\n"
      "v_mfma_f32_16x16x32_bf16 v[200:203], a[184:187], v[16:19], v[200:203]\n"
      "v_mfma_f32_16x16x32_bf16 v[204:207], a[188:191], v[16:19], v[204:207]\n"
      ".Lr_skip2_%=:\n"
      "s_bitcmp0_b32 s43, 3\n"
      "s_cbranch_scc1 .Lr_skip3_%=\n"
      "s_bitset0_b32 s40, 3\n"
      "s_cmp_eq_u32 s40, 0\n"
      "s_cbranch_scc1 .Lr_last3_%=\n"
      "ds_read_b128 v[24:27], v21 offset:36864\n"
      "ds_read_b128 v[28:31], v21 offset:37888\n"
      "ds_read_b128 v[32:35], v21 offset:38912\n"
      "ds_read_b128 v[36:39], v21 offset:39936\n"
      "ds_read_b128 v[40:43], v21 offset:40960\n"
      "ds_read_b128 v[44:47], v21 offset:41984\n"
      "ds_read_b128 v[48:51], v21 offset:43008\n"
      "ds_read_b128 v[52:55], v21 offset:44032\n"
      "ds_read_b128 v[56:59], v21 offset:45056\n"
      "ds_read_b128 v[60:63], v21 offset:46080\n"
      "ds_read_b128 v[10:13], v21 offset:47104\n"
      "ds_read_b128 v[16:19], v21 offset:48128\n"
      "s_waitcnt lgkmcnt(11)\n"
      "v_mfma_f32_16x16x32_bf16 v[208:211], a[192:195], v[24:27], 0\n"
      "v_mfma_f32_16x16x32_bf16 v[212:215], a[196:199], v[24:27], 0\n"
      "v_mfma_f32_16x16x32_bf16 v[216:219], a[200:203], v[24:27], 0\n"
      "v_mfma_f32_16x16x32_bf16 v[220:223], a[204:207], v[24:27], 0\n"
      "s_waitcnt lgkmcnt(10)\n"
      "v_mfma_f32_16x16x32_bf16 v[208:211], a[208:211], v[28:31], v[208:211]\n"
      "v_mfma_f32_16x16x32_bf16 v[212:215], a[212:215], v[28:31], v[212:215]\n"
      "v_mfma_f32_16x16x32_bf16 v[216:219], a[216:219], v[28:31], v[216:219]\n"
      "v_mfma_f32_16x16x32_bf16 v[220:223], a[220:223], v[28:31], v[220:223]\n"
      "s_waitcnt lgkmcnt(9)\n"
      "v_mfma_f32_16x16x32_bf16 v[208:211], a[224:227], v[32:35], v[208:211]\n"
      "v_mfma_f32_16x16x32_bf16 v[212:215], a[228:231], v[32:35], v[212:215]\n"
      "v_mfma_f32_16x16x32_bf16 v[216:219], a[232:235], v[32:35], v[216:219]\n"
      "v_mfma_f32_16x16x32_bf16 v[220:223], a[236:239], v[32:35], v[220:223]\n"
      "s_waitcnt lgkmcnt(8)\n"
      "v_mfma_f32_16x16x32_bf16 v[208:211], a[240:243], v[36:39], v[208:211]\n"
      "v_mfma_f32_16x16x32_bf16 v[212:215], a[244:247], v[36:39], v[212:215]\n"
      "v_mfma_f32_16x16x32_bf16 v[216:219], a[248:251], v[36:39], v[216:219]\n"
      "v_mfma_f32_16x16x32_bf16 v[220:223], a[252:255], v[36:39], v[220:223]\n"
      "s_waitcnt lgkmcnt(7)\n"
      "v_mfma_f32_16x16x32_bf16 v[224:227], a[192:195], v[40:43], 0\n"
      "v_mfma_f32_16x16x32_bf16 v[228:231], a[196:199], v[40:43], 0\n"
      "v_mfma_f32_16x16x32_bf16 v[232:235], a[200:203], v[40:43], 0\n"
      "v_mfma_f32_16x16x32_bf16 v[236:239], a[204:207], v[40:43], 0\n"
      "s_waitcnt lgkmcnt(6)\n"
      "v_mfma_f32_16x16x32_bf16 v[224:227], a[208:211], v[44:47], v[224:227]\n"
      "v_mfma_f32_16x16x32_bf16 v[228:231], a[212:215], v[44:47], v[228:231]\n"
      "v_mfma_f32_16x16x32_bf16 v[232:235], a[216:219], v[44:47], v[232:235]\n"
      "v_mfma_f32_16x16x32_bf16 v[236:239], a[220:223], v[44:47], v[236:239]\n"
      "s_waitcnt lgkmcnt(5)\n"
      "v_mfma_f32_16x16x32_bf16 v[224:227], a[224:227], v[48:51], v[224:227]\n"
      "v_mfma_f32_16x16x32_bf16 v[228:231], a[228:231], v[48:51], v[228:231]\n"
      "v_mfma_f32_16x16x32_bf16 v[232:235], a[232:235], v[48:51], v[232:235]\n"
      "v_mfma_f32_16x16x32_bf16 v[236:239], a[236:239], v[48:51], v[236:239]\n"
      "s_waitcnt lgkmcnt(4)\n"
      "v_mfma_f32_16x16x32_bf16 v[224:227], a[240:243], v[52:55], v[224:227]\n"
      "v_mfma_f32_16x16x32_bf16 v[228:231], a[244:247], v[52:55], v[228:231]\n"
      "v_mfma_f32_16x16x32_bf16 v[232:235], a[248:251], v[52:55], v[232:235]\n"
      "v_mfma_f32_16x16x32_bf16 v[236:239], a[252:255], v[52:55], v[236:239]\n"
      "s_waitcnt lgkmcnt(3)\n"
      "v_mfma_f32_16x16x32_bf16 v[240:243], a[192:195], v[56:59], 0\n"
      "v_mfma_f32_16x16x32_bf16 v[244:247], a[196:199], v[56:59], 0\n"
      "v_mfma_f32_16x16x32_bf16 v[248:251], a[200:203], v[56:59], 0\n"
      "v_mfma_f32_16x16x32_bf16 v[252:255], a[204:207], v[56:59], 0\n"
      "s_waitcnt lgkmcnt(2)\n"
      "v_mfma_f32_16x16x32_bf16 v[240:243], a[208:211], v[60:63], v[240:243]\n"
      "v_mfma_f32_16x16x32_bf16 v[244:247], a[212:215], v[60:63], v[244:247]\n"
      "v_mfma_f32_16x16x32_bf16 v[248:251], a[216:219], v[60:63], v[248:251]\n"
      "v_mfma_f32_16x16x32_bf16 v[252:255], a[220:223], v[60:63], v[252:255]\n"
      "s_waitcnt lgkmcnt(1)\n"
      "v_mfma_f32_16x16x32_bf16 v[240:243], a[224:227], v[10:13], v[240:243]\n"
      "v_mfma_f32_16x16x32_bf16 v[244:247], a[228:231], v[10:13], v[244:247]\n"
      "v_mfma_f32_16x16x32_bf16 v[248:251], a[232:235], v[10:13], v[248:251]\n"
      "v_mfma_f32_16x16x32_bf16 v[252:255], a[236:239], v[10:13], v[252:255]\n"
      "s_waitcnt lgkmcnt(0)\n"
      "v_mfma_f32_16x16x32_bf16 v[240:243], a[240:243], v[16:19], v[240:243]\n"
      "v_mfma_f32_16x16x32_bf16 v[244:247], a[244:247], v[16:19], v[244:247]\n"
      "v_mfma_f32_16x16x32_bf16 v[248:251], a[248:251], v[16:19], v[248:251]\n"
      "v_mfma_f32_16x16x32_bf16 v[252:255], a[252:255], v[16:19], v[252:255]\n"
      ".Lr_skip3_%=:\n"
      "s_branch .Lround_%=\n"
      ".Lr_last0_%=:\n"
      "v_and_b32 v6, 7, v1\n"
      "v_min_u32 v6, 6, v6\n"
      "v_mul_u32_u24 v6, 3840, v6\n"
      "ds_read_b128 v[24:27], v21 offset:0\n"
      "ds_read_b128 v[28:31], v21 offset:1024\n"
      "ds_read_b128 v[32:35], v21 offset:2048\n"
      "ds_read_b128 v[36:39], v21 offset:3072\n"
      "ds_read_b128 v[40:43], v21 offset:4096\n"
      "ds_read_b128 v[44:47], v21 offset:5120\n"
      "ds_read_b128 v[48:51], v21 offset:6144\n"
      "ds_read_b128 v[52:55], v21 offset:7168\n"
      "ds_read_b128 v[56:59], v21 offset:8192\n"
      "ds_read_b128 v[60:63], v21 offset:9216\n"
      "ds_read_b128 v[10:13], v21 offset:10240\n"
      "ds_read_b128 v[16:19], v21 offset:11264\n"
      "s_waitcnt lgkmcnt(11)\n"
      "v_mfma_f32_16x16x32_bf16 v[64:67], a[0:3], v[24:27], 0\n"
      "v_mfma_f32_16x16x32_bf16 v[68:71], a[4:7], v[24:27], 0\n"
      "v_mfma_f32_16x16x32_bf16 v[72:75], a[8:11], v[24:27], 0\n"
      "v_mfma_f32_16x16x32_bf16 v[76:79], a[12:15], v[24:27], 0\n"
      "s_waitcnt lgkmcnt(10)\n"
      "v_mfma_f32_16x16x32_bf16 v[64:67], a[16:19], v[28:31], v[64:67]\n"
      "v_mfma_f32_16x16x32_bf16 v[68:71], a[20:23], v[28:31], v[68:71]\n"
      "v_mfma_f32_16x16x32_bf16 v[72:75], a[24:27], v[28:31], v[72:75]\n"
      "v_mfma_f32_16x16x32_bf16 v[76:79], a[28:31], v[28:31], v[76:79]\n"
      "s_waitcnt lgkmcnt(9)\n"
      "v_mfma_f32_16x16x32_bf16 v[64:67], a[32:35], v[32:35], v[64:67]\n"
      "v_mfma_f32_16x16x32_bf16 v[68:71], a[36:39], v[32:35], v[68:71]\n"
      "v_mfma_f32_16x16x32_bf16 v[72:75], a[40:43], v[32:35], v[72:75]\n"
      "v_mfma_f32_16x16x32_bf16 v[76:79], a[44:47], v[32:35], v[76:79]\n"
      "s_waitcnt lgkmcnt(8)\n"
      "v_mfma_f32_16x16x32_bf16 v[64:67], a[48:51], v[36:39], v[64:67]\n"
      "v_mfma_f32_16x16x32_bf16 v[68:71], a[52:55], v[36:39], v[68:71]\n"
      "v_mfma_f32_16x16x32_bf16 v[72:75], a[56:59], v[36:39], v[72:75]\n"
      "v_mfma_f32_16x16x32_bf16 v[76:79], a[60:63], v[36:39], v[76:79]\n"
      "s_waitcnt lgkmcnt(7)\n"
      "v_mfma_f32_16x16x32_bf16 v[80:83], a[0:3], v[40:43], 0\n"
      "v_mfma_f32_16x16x32_bf16 v[84:87], a[4:7], v[40:43], 0\n"
      "v_mfma_f32_16x16x32_bf16 v[88:91], a[8:11], v[40:43], 0\n"
      "v_mfma_f32_16x16x32_bf16 v[92:95], a[12:15], v[40:43], 0\n"
      "s_waitcnt lgkmcnt(6)\n"
      "v_mfma_f32_16x16x32_bf16 v[80:83], a[16:19], v[44:47], v[80:83]\n"
      "v_mfma_f32_16x16x32_bf16 v[84:87], a[20:23], v[44:47], v[84:87]\n"
      "v_mfma_f32_16x16x32_bf16 v[88:91], a[24:27], v[44:47], v[88:91]\n"
      "v_mfma_f32_16x16x32_bf16 v[92:95], a[28:31], v[44:47], v[92:95]\n"
      "v_pk_add_f32 v[64:65], v[64:65], v[112:113]\n"
      "v_pk_add_f32 v[66:67], v[66:67], v[114:115]\n"
      "v_pk_add_f32 v[64:65], v[64:65], v[160:161]\n"
      "v_pk_add_f32 v[66:67], v[66:67], v[162:163]\n"
      "v_pk_add_f32 v[64:65], v[64:65], v[208:209]\n"
      "v_pk_add_f32 v[66:67], v[66:67], v[210:211]\n"
      "v_cvt_pk_bf16_f32 v112, v64, v65\n"
      "v_cvt_pk_bf16_f32 v113, v66, v67\n"
      "s_waitcnt lgkmcnt(5)\n"
      "v_mfma_f32_16x16x32_bf16 v[80:83], a[32:35], v[48:51], v[80:83]\n"
      "v_mfma_f32_16x16x32_bf16 v[84:87], a[36:39], v[48:51], v[84:87]\n"
      "v_mfma_f32_16x16x32_bf16 v[88:91], a[40:43], v[48:51], v[88:91]\n"
      "v_mfma_f32_16x16x32_bf16 v[92:95], a[44:47], v[48:51], v[92:95]\n"
      "v_pk_add_f32 v[68:69], v[68:69], v[116:117]\n"
      "v_pk_add_f32 v[70:71], v[70:71], v[118:119]\n"
      "v_pk_add_f32 v[68:69], v[68:69], v[164:165]\n"
      "v_pk_add_f32 v[70:71], v[70:71], v[166:167]\n"
      "v_pk_add_f32 v[68:69], v[68:69], v[212:213]\n"
      "v_pk_add_f32 v[70:71], v[70:71], v[214:215]\n"
      "v_cvt_pk_bf16_f32 v114, v68, v69\n"
      "v_cvt_pk_bf16_f32 v115, v70, v71\n"
      "v_bitop3_b32 v116, v112, v113, v5 bitop3:0x96\n"
      "v_bitop3_b32 v116, v116, v114, v115 bitop3:0x96\n"
      "buffer_store_dwordx4 v[112:115], v20, s[52:55], s45 offen offset:0 sc1\n"
      "buffer_store_dword v116, v22, s[52:55], s45 offen offset:1024 sc1\n"
      "s_waitcnt lgkmcnt(4)\n"
      "v_mfma_f32_16x16x32_bf16 v[80:83], a[48:51], v[52:55], v[80:83]\n"
      "v_mfma_f32_16x16x32_bf16 v[84:87], a[52:55], v[52:55], v[84:87]\n"
      "v_mfma_f32_16x16x32_bf16 v[88:91], a[56:59], v[52:55], v[88:91]\n"
      "v_mfma_f32_16x16x32_bf16 v[92:95], a[60:63], v[52:55], v[92:95]\n"
      "v_pk_add_f32 v[72:73], v[72:73], v[120:121]\n"
      "v_pk_add_f32 v[74:75], v[74:75], v[122:123]\n"
      "v_pk_add_f32 v[72:73], v[72:73], v[168:169]\n"
      "v_pk_add_f32 v[74:75], v[74:75], v[170:171]\n"
      "v_pk_add_f32 v[72:73], v[72:73], v[216:217]\n"
      "v_pk_add_f32 v[74:75], v[74:75], v[218:219]\n"
      "v_cvt_pk_bf16_f32 v120, v72, v73\n"
      "v_cvt_pk_bf16_f32 v121, v74, v75\n"
      "s_waitcnt lgkmcnt(3)\n"
      "v_mfma_f32_16x16x32_bf16 v[96:99], a[0:3], v[56:59], 0\n"
      "v_mfma_f32_16x16x32_bf16 v[100:103], a[4:7], v[56:59], 0\n"
      "v_mfma_f32_16x16x32_bf16 v[104:107], a[8:11], v[56:59], 0\n"
      "v_mfma_f32_16x16x32_bf16 v[108:111], a[12:15], v[56:59], 0\n"
      "v_pk_add_f32 v[76:77], v[76:77], v[124:125]\n"
      "v_pk_add_f32 v[78:79], v[78:79], v[126:127]\n"
      "v_pk_add_f32 v[76:77], v[76:77], v[172:173]\n"
      "v_pk_add_f32 v[78:79], v[78:79], v[174:175]\n"
      "v_pk_add_f32 v[76:77], v[76:77], v[220:221]\n"
      "v_pk_add_f32 v[78:79], v[78:79], v[222:223]\n"
      "v_cvt_pk_bf16_f32 v122, v76, v77\n"
      "v_cvt_pk_bf16_f32 v123, v78, v79\n"
      "v_bitop3_b32 v124, v120, v121, v5 bitop3:0x96\n"
      "v_bitop3_b32 v124, v124, v122, v123 bitop3:0x96\n"
      "buffer_store_dwordx4 v[120:123], v4, s[52:55], s47 offen offset:0 sc1\n"
      "buffer_store_dword v124, v23, s[52:55], s47 offen offset:1024 sc1\n"
      "s_waitcnt lgkmcnt(2)\n"
      "v_mfma_f32_16x16x32_bf16 v[96:99], a[16:19], v[60:63], v[96:99]\n"
      "v_mfma_f32_16x16x32_bf16 v[100:103], a[20:23], v[60:63], v[100:103]\n"
      "v_mfma_f32_16x16x32_bf16 v[104:107], a[24:27], v[60:63], v[104:107]\n"
      "v_mfma_f32_16x16x32_bf16 v[108:111], a[28:31], v[60:63], v[108:111]\n"
      "v_pk_add_f32 v[80:81], v[80:81], v[128:129]\n"
      "v_pk_add_f32 v[82:83], v[82:83], v[130:131]\n"
      "v_pk_add_f32 v[80:81], v[80:81], v[176:177]\n"
      "v_pk_add_f32 v[82:83], v[82:83], v[178:179]\n"
      "v_pk_add_f32 v[80:81], v[80:81], v[224:225]\n"
      "v_pk_add_f32 v[82:83], v[82:83], v[226:227]\n"
      "v_cvt_pk_bf16_f32 v128, v80, v81\n"
      "v_cvt_pk_bf16_f32 v129, v82, v83\n"
      "s_waitcnt lgkmcnt(1)\n"
      "v_mfma_f32_16x16x32_bf16 v[96:99], a[32:35], v[10:13], v[96:99]\n"
      "v_mfma_f32_16x16x32_bf16 v[100:103], a[36:39], v[10:13], v[100:103]\n"
      "v_mfma_f32_16x16x32_bf16 v[104:107], a[40:43], v[10:13], v[104:107]\n"
      "v_mfma_f32_16x16x32_bf16 v[108:111], a[44:47], v[10:13], v[108:111]\n"
      "v_pk_add_f32 v[84:85], v[84:85], v[132:133]\n"
      "v_pk_add_f32 v[86:87], v[86:87], v[134:135]\n"
      "v_pk_add_f32 v[84:85], v[84:85], v[180:181]\n"
      "v_pk_add_f32 v[86:87], v[86:87], v[182:183]\n"
      "v_pk_add_f32 v[84:85], v[84:85], v[228:229]\n"
      "v_pk_add_f32 v[86:87], v[86:87], v[230:231]\n"
      "v_cvt_pk_bf16_f32 v130, v84, v85\n"
      "v_cvt_pk_bf16_f32 v131, v86, v87\n"
      "v_bitop3_b32 v132, v128, v129, v5 bitop3:0x96\n"
      "v_bitop3_b32 v132, v132, v130, v131 bitop3:0x96\n"
      "buffer_store_dwordx4 v[128:131], v20, s[52:55], s45 offen offset:1280 sc1\n"
      "buffer_store_dword v132, v22, s[52:55], s45 offen offset:2304 sc1\n"
      "s_waitcnt lgkmcnt(0)\n"
      "v_mfma_f32_16x16x32_bf16 v[96:99], a[48:51], v[16:19], v[96:99]\n"
      "v_mfma_f32_16x16x32_bf16 v[100:103], a[52:55], v[16:19], v[100:103]\n"
      "v_mfma_f32_16x16x32_bf16 v[104:107], a[56:59], v[16:19], v[104:107]\n"
      "v_mfma_f32_16x16x32_bf16 v[108:111], a[60:63], v[16:19], v[108:111]\n"
      "v_pk_add_f32 v[88:89], v[88:89], v[136:137]\n"
      "v_pk_add_f32 v[90:91], v[90:91], v[138:139]\n"
      "v_pk_add_f32 v[88:89], v[88:89], v[184:185]\n"
      "v_pk_add_f32 v[90:91], v[90:91], v[186:187]\n"
      "v_pk_add_f32 v[88:89], v[88:89], v[232:233]\n"
      "v_pk_add_f32 v[90:91], v[90:91], v[234:235]\n"
      "v_cvt_pk_bf16_f32 v136, v88, v89\n"
      "v_cvt_pk_bf16_f32 v137, v90, v91\n"
      "v_pk_add_f32 v[92:93], v[92:93], v[140:141]\n"
      "v_pk_add_f32 v[94:95], v[94:95], v[142:143]\n"
      "v_pk_add_f32 v[92:93], v[92:93], v[188:189]\n"
      "v_pk_add_f32 v[94:95], v[94:95], v[190:191]\n"
      "v_pk_add_f32 v[92:93], v[92:93], v[236:237]\n"
      "v_pk_add_f32 v[94:95], v[94:95], v[238:239]\n"
      "v_cvt_pk_bf16_f32 v138, v92, v93\n"
      "v_cvt_pk_bf16_f32 v139, v94, v95\n"
      "v_bitop3_b32 v140, v136, v137, v5 bitop3:0x96\n"
      "v_bitop3_b32 v140, v140, v138, v139 bitop3:0x96\n"
      "buffer_store_dwordx4 v[136:139], v4, s[52:55], s47 offen offset:1280 sc1\n"
      "buffer_store_dword v140, v23, s[52:55], s47 offen offset:2304 sc1\n"
      "v_pk_add_f32 v[96:97], v[96:97], v[144:145]\n"
      "v_pk_add_f32 v[98:99], v[98:99], v[146:147]\n"
      "v_pk_add_f32 v[96:97], v[96:97], v[192:193]\n"
      "v_pk_add_f32 v[98:99], v[98:99], v[194:195]\n"
      "v_pk_add_f32 v[96:97], v[96:97], v[240:241]\n"
      "v_pk_add_f32 v[98:99], v[98:99], v[242:243]\n"
      "v_cvt_pk_bf16_f32 v144, v96, v97\n"
      "v_cvt_pk_bf16_f32 v145, v98, v99\n"
      "v_pk_add_f32 v[100:101], v[100:101], v[148:149]\n"
      "v_pk_add_f32 v[102:103], v[102:103], v[150:151]\n"
      "v_pk_add_f32 v[100:101], v[100:101], v[196:197]\n"
      "v_pk_add_f32 v[102:103], v[102:103], v[198:199]\n"
      "v_pk_add_f32 v[100:101], v[100:101], v[244:245]\n"
      "v_pk_add_f32 v[102:103], v[102:103], v[246:247]\n"
      "v_cvt_pk_bf16_f32 v146, v100, v101\n"
      "v_cvt_pk_bf16_f32 v147, v102, v103\n"
      "v_bitop3_b32 v148, v144, v145, v5 bitop3:0x96\n"
      "v_bitop3_b32 v148, v148, v146, v147 bitop3:0x96\n"
      "buffer_store_dwordx4 v[144:147], v20, s[52:55], s45 offen offset:2560 sc1\n"
      "buffer_store_dword v148, v22, s[52:55], s45 offen offset:3584 sc1\n"
      "v_pk_add_f32 v[104:105], v[104:105], v[152:153]\n"
      "v_pk_add_f32 v[106:107], v[106:107], v[154:155]\n"
      "v_pk_add_f32 v[104:105], v[104:105], v[200:201]\n"
      "v_pk_add_f32 v[106:107], v[106:107], v[202:203]\n"
      "v_pk_add_f32 v[104:105], v[104:105], v[248:249]\n"
      "v_pk_add_f32 v[106:107], v[106:107], v[250:251]\n"
      "v_cvt_pk_bf16_f32 v152, v104, v105\n"
      "v_cvt_pk_bf16_f32 v153, v106, v107\n"
      "v_pk_add_f32 v[108:109], v[108:109], v[156:157]\n"
      "v_pk_add_f32 v[110:111], v[110:111], v[158:159]\n"
      "v_pk_add_f32 v[108:109], v[108:109], v[204:205]\n"
      "v_pk_add_f32 v[110:111], v[110:111], v[206:207]\n"
      "v_pk_add_f32 v[108:109], v[108:109], v[252:253]\n"
      "v_pk_add_f32 v[110:111], v[110:111], v[254:255]\n"
      "v_cvt_pk_bf16_f32 v154, v108, v109\n"
      "v_cvt_pk_bf16_f32 v155, v110, v111\n"
      "v_bitop3_b32 v156, v152, v153, v5 bitop3:0x96\n"
      "v_bitop3_b32 v156, v156, v154, v155 bitop3:0x96\n"
      "buffer_store_dwordx4 v[152:155], v4, s[52:55], s47 offen offset:2560 sc1\n"
      "buffer_store_dword v156, v23, s[52:55], s47 offen offset:3584 sc1\n"
      "s_branch .Lsent_%=\n"
      ".Lr_last1_%=:\n"
      "v_and_b32 v6, 7, v1\n"
      "v_min_u32 v6, 6, v6\n"
      "v_mul_u32_u24 v6, 3840, v6\n"
      "ds_read_b128 v[24:27], v21 offset:12288\n"
      "ds_read_b128 v[28:31], v21 offset:13312\n"
      "ds_read_b128 v[32:35], v21 offset:14336\n"
      "ds_read_b128 v[36:39], v21 offset:15360\n"
      "ds_read_b128 v[40:43], v21 offset:16384\n"
      "ds_read_b128 v[44:47], v21 offset:17408\n"
      "ds_read_b128 v[48:51], v21 offset:18432\n"
      "ds_read_b128 v[52:55], v21 offset:19456\n"
      "ds_read_b128 v[56:59], v21 offset:20480\n"
      "ds_read_b128 v[60:63], v21 offset:21504\n"
      "ds_read_b128 v[10:13], v21 offset:22528\n"
      "ds_read_b128 v[16:19], v21 offset:23552\n"
      "s_waitcnt lgkmcnt(11)\n"
      "v_mfma_f32_16x16x32_bf16 v[112:115], a[64:67], v[24:27], 0\n"
      "v_mfma_f32_16x16x32_bf16 v[116:119], a[68:71], v[24:27], 0\n"
      "v_mfma_f32_16x16x32_bf16 v[120:123], a[72:75], v[24:27], 0\n"
      "v_mfma_f32_16x16x32_bf16 v[124:127], a[76:79], v[24:27], 0\n"
      "s_waitcnt lgkmcnt(10)\n"
      "v_mfma_f32_16x16x32_bf16 v[112:115], a[80:83], v[28:31], v[112:115]\n"
      "v_mfma_f32_16x16x32_bf16 v[116:119], a[84:87], v[28:31], v[116:119]\n"
      "v_mfma_f32_16x16x32_bf16 v[120:123], a[88:91], v[28:31], v[120:123]\n"
      "v_mfma_f32_16x16x32_bf16 v[124:127], a[92:95], v[28:31], v[124:127]\n"
      "s_waitcnt lgkmcnt(9)\n"
      "v_mfma_f32_16x16x32_bf16 v[112:115], a[96:99], v[32:35], v[112:115]\n"
      "v_mfma_f32_16x16x32_bf16 v[116:119], a[100:103], v[32:35], v[116:119]\n"
      "v_mfma_f32_16x16x32_bf16 v[120:123], a[104:107], v[32:35], v[120:123]\n"
      "v_mfma_f32_16x16x32_bf16 v[124:127], a[108:111], v[32:35], v[124:127]\n"
      "s_waitcnt lgkmcnt(8)\n"
      "v_mfma_f32_16x16x32_bf16 v[112:115], a[112:115], v[36:39], v[112:115]\n"
      "v_mfma_f32_16x16x32_bf16 v[116:119], a[116:119], v[36:39], v[116:119]\n"
      "v_mfma_f32_16x16x32_bf16 v[120:123], a[120:123], v[36:39], v[120:123]\n"
      "v_mfma_f32_16x16x32_bf16 v[124:127], a[124:127], v[36:39], v[124:127]\n"
      "s_waitcnt lgkmcnt(7)\n"
      "v_mfma_f32_16x16x32_bf16 v[128:131], a[64:67], v[40:43], 0\n"
      "v_mfma_f32_16x16x32_bf16 v[132:135], a[68:71], v[40:43], 0\n"
      "v_mfma_f32_16x16x32_bf16 v[136:139], a[72:75], v[40:43], 0\n"
      "v_mfma_f32_16x16x32_bf16 v[140:143], a[76:79], v[40:43], 0\n"
      "s_waitcnt lgkmcnt(6)\n"
      "v_mfma_f32_16x16x32_bf16 v[128:131], a[80:83], v[44:47], v[128:131]\n"
      "v_mfma_f32_16x16x32_bf16 v[132:135], a[84:87], v[44:47], v[132:135]\n"
      "v_mfma_f32_16x16x32_bf16 v[136:139], a[88:91], v[44:47], v[136:139]\n"
      "v_mfma_f32_16x16x32_bf16 v[140:143], a[92:95], v[44:47], v[140:143]\n"
      "v_pk_add_f32 v[64:65], v[64:65], v[112:113]\n"
      "v_pk_add_f32 v[66:67], v[66:67], v[114:115]\n"
      "v_pk_add_f32 v[64:65], v[64:65], v[160:161]\n"
      "v_pk_add_f32 v[66:67], v[66:67], v[162:163]\n"
      "v_pk_add_f32 v[64:65], v[64:65], v[208:209]\n"
      "v_pk_add_f32 v[66:67], v[66:67], v[210:211]\n"
      "v_cvt_pk_bf16_f32 v112, v64, v65\n"
      "v_cvt_pk_bf16_f32 v113, v66, v67\n"
      "s_waitcnt lgkmcnt(5)\n"
      "v_mfma_f32_16x16x32_bf16 v[128:131], a[96:99], v[48:51], v[128:131]\n"
      "v_mfma_f32_16x16x32_bf16 v[132:135], a[100:103], v[48:51], v[132:135]\n"
      "v_mfma_f32_16x16x32_bf16 v[136:139], a[104:107], v[48:51], v[136:139]\n"
      "v_mfma_f32_16x16x32_bf16 v[140:143], a[108:111], v[48:51], v[140:143]\n"
      "v_pk_add_f32 v[68:69], v[68:69], v[116:117]\n"
      "v_pk_add_f32 v[70:71], v[70:71], v[118:119]\n"
      "v_pk_add_f32 v[68:69], v[68:69], v[164:165]\n"
      "v_pk_add_f32 v[70:71], v[70:71], v[166:167]\n"
      "v_pk_add_f32 v[68:69], v[68:69], v[212:213]\n"
      "v_pk_add_f32 v[70:71], v[70:71], v[214:215]\n"
      "v_cvt_pk_bf16_f32 v114, v68, v69\n"
      "v_cvt_pk_bf16_f32 v115, v70, v71\n"
      "v_bitop3_b32 v116, v112, v113, v5 bitop3:0x96\n"
      "v_bitop3_b32 v116, v116, v114, v115 bitop3:0x96\n"
      "buffer_store_dwordx4 v[112:115], v20, s[52:55], s45 offen offset:0 sc1\n"
      "buffer_store_dword v116, v22, s[52:55], s45 offen offset:1024 sc1\n"
      "s_waitcnt lgkmcnt(4)\n"
      "v_mfma_f32_16x16x32_bf16 v[128:131], a[112:115], v[52:55], v[128:131]\n"
      "v_mfma_f32_16x16x32_bf16 v[132:135], a[116:119], v[52:55], v[132:135]\n"
      "v_mfma_f32_16x16x32_bf16 v[136:139], a[120:123], v[52:55], v[136:139]\n"
      "v_mfma_f32_16x16x32_bf16 v[140:143], a[124:127], v[52:55], v[140:143]\n"
      "v_pk_add_f32 v[72:73], v[72:73], v[120:121]\n"
      "v_pk_add_f32 v[74:75], v[74:75], v[122:123]\n"
      "v_pk_add_f32 v[72:73], v[72:73], v[168:169]\n"
      "v_pk_add_f32 v[74:75], v[74:75], v[170:171]\n"
      "v_pk_add_f32 v[72:73], v[72:73], v[216:217]\n"
      "v_pk_add_f32 v[74:75], v[74:75], v[218:219]\n"
      "v_cvt_pk_bf16_f32 v120, v72, v73\n"
      "v_cvt_pk_bf16_f32 v121, v74, v75\n"
      "s_waitcnt lgkmcnt(3)\n"
      "v_mfma_f32_16x16x32_bf16 v[144:147], a[64:67], v[56:59], 0\n"
      "v_mfma_f32_16x16x32_bf16 v[148:151], a[68:71], v[56:59], 0\n"
      "v_mfma_f32_16x16x32_bf16 v[152:155], a[72:75], v[56:59], 0\n"
      "v_mfma_f32_16x16x32_bf16 v[156:159], a[76:79], v[56:59], 0\n"
      "v_pk_add_f32 v[76:77], v[76:77], v[124:125]\n"
      "v_pk_add_f32 v[78:79], v[78:79], v[126:127]\n"
      "v_pk_add_f32 v[76:77], v[76:77], v[172:173]\n"
      "v_pk_add_f32 v[78:79], v[78:79], v[174:175]\n"
      "v_pk_add_f32 v[76:77], v[76:77], v[220:221]\n"
      "v_pk_add_f32 v[78:79], v[78:79], v[222:223]\n"
      "v_cvt_pk_bf16_f32 v122, v76, v77\n"
      "v_cvt_pk_bf16_f32 v123, v78, v79\n"
      "v_bitop3_b32 v124, v120, v121, v5 bitop3:0x96\n"
      "v_bitop3_b32 v124, v124, v122, v123 bitop3:0x96\n"
      "buffer_store_dwordx4 v[120:123], v4, s[52:55], s47 offen offset:0 sc1\n"
      "buffer_store_dword v124, v23, s[52:55], s47 offen offset:1024 sc1\n"
      "s_waitcnt lgkmcnt(2)\n"
      "v_mfma_f32_16x16x32_bf16 v[144:147], a[80:83], v[60:63], v[144:147]\n"
      "v_mfma_f32_16x16x32_bf16 v[148:151], a[84:87], v[60:63], v[148:151]\n"
      "v_mfma_f32_16x16x32_bf16 v[152:155], a[88:91], v[60:63], v[152:155]\n"
      "v_mfma_f32_16x16x32_bf16 v[156:159], a[92:95], v[60:63], v[156:159]\n"
      "v_pk_add_f32 v[80:81], v[80:81], v[128:129]\n"
      "v_pk_add_f32 v[82:83], v[82:83], v[130:131]\n"
      "v_pk_add_f32 v[80:81], v[80:81], v[176:177]\n"
      "v_pk_add_f32 v[82:83], v[82:83], v[178:179]\n"
      "v_pk_add_f32 v[80:81], v[80:81], v[224:225]\n"
      "v_pk_add_f32 v[82:83], v[82:83], v[226:227]\n"
      "v_cvt_pk_bf16_f32 v128, v80, v81\n"
      "v_cvt_pk_bf16_f32 v129, v82, v83\n"
      "s_waitcnt lgkmcnt(1)\n"
      "v_mfma_f32_16x16x32_bf16 v[144:147], a[96:99], v[10:13], v[144:147]\n"
      "v_mfma_f32_16x16x32_bf16 v[148:151], a[100:103], v[10:13], v[148:151]\n"
      "v_mfma_f32_16x16x32_bf16 v[152:155], a[104:107], v[10:13], v[152:155]\n"
      "v_mfma_f32_16x16x32_bf16 v[156:159], a[108:111], v[10:13], v[156:159]\n"
      "v_pk_add_f32 v[84:85], v[84:85], v[132:133]\n"
      "v_pk_add_f32 v[86:87], v[86:87], v[134:135]\n"
      "v_pk_add_f32 v[84:85], v[84:85], v[180:181]\n"
      "v_pk_add_f32 v[86:87], v[86:87], v[182:183]\n"
      "v_pk_add_f32 v[84:85], v[84:85], v[228:229]\n"
      "v_pk_add_f32 v[86:87], v[86:87], v[230:231]\n"
      "v_cvt_pk_bf16_f32 v130, v84, v85\n"
      "v_cvt_pk_bf16_f32 v131, v86, v87\n"
      "v_bitop3_b32 v132, v128, v129, v5 bitop3:0x96\n"
      "v_bitop3_b32 v132, v132, v130, v131 bitop3:0x96\n"
      "buffer_store_dwordx4 v[128:131], v20, s[52:55], s45 offen offset:1280 sc1\n"
      "buffer_store_dword v132, v22, s[52:55], s45 offen offset:2304 sc1\n"
      "s_waitcnt lgkmcnt(0)\n"
      "v_mfma_f32_16x16x32_bf16 v[144:147], a[112:115], v[16:19], v[144:147]\n"
      "v_mfma_f32_16x16x32_bf16 v[148:151], a[116:119], v[16:19], v[148:151]\n"
      "v_mfma_f32_16x16x32_bf16 v[152:155], a[120:123], v[16:19], v[152:155]\n"
      "v_mfma_f32_16x16x32_bf16 v[156:159], a[124:127], v[16:19], v[156:159]\n"
      "v_pk_add_f32 v[88:89], v[88:89], v[136:137]\n"
      "v_pk_add_f32 v[90:91], v[90:91], v[138:139]\n"
      "v_pk_add_f32 v[88:89], v[88:89], v[184:185]\n"
      "v_pk_add_f32 v[90:91], v[90:91], v[186:187]\n"
      "v_pk_add_f32 v[88:89], v[88:89], v[232:233]\n"
      "v_pk_add_f32 v[90:91], v[90:91], v[234:235]\n"
      "v_cvt_pk_bf16_f32 v136, v88, v89\n"
      "v_cvt_pk_bf16_f32 v137, v90, v91\n"
      "v_pk_add_f32 v[92:93], v[92:93], v[140:141]\n"
      "v_pk_add_f32 v[94:95], v[94:95], v[142:143]\n"
      "v_pk_add_f32 v[92:93], v[92:93], v[188:189]\n"
      "v_pk_add_f32 v[94:95], v[94:95], v[190:191]\n"
      "v_pk_add_f32 v[92:93], v[92:93], v[236:237]\n"
      "v_pk_add_f32 v[94:95], v[94:95], v[238:239]\n"
      "v_cvt_pk_bf16_f32 v138, v92, v93\n"
      "v_cvt_pk_bf16_f32 v139, v94, v95\n"
      "v_bitop3_b32 v140, v136, v137, v5 bitop3:0x96\n"
      "v_bitop3_b32 v140, v140, v138, v139 bitop3:0x96\n"
      "buffer_store_dwordx4 v[136:139], v4, s[52:55], s47 offen offset:1280 sc1\n"
      "buffer_store_dword v140, v23, s[52:55], s47 offen offset:2304 sc1\n"
      "v_pk_add_f32 v[96:97], v[96:97], v[144:145]\n"
      "v_pk_add_f32 v[98:99], v[98:99], v[146:147]\n"
      "v_pk_add_f32 v[96:97], v[96:97], v[192:193]\n"
      "v_pk_add_f32 v[98:99], v[98:99], v[194:195]\n"
      "v_pk_add_f32 v[96:97], v[96:97], v[240:241]\n"
      "v_pk_add_f32 v[98:99], v[98:99], v[242:243]\n"
      "v_cvt_pk_bf16_f32 v144, v96, v97\n"
      "v_cvt_pk_bf16_f32 v145, v98, v99\n"
      "v_pk_add_f32 v[100:101], v[100:101], v[148:149]\n"
      "v_pk_add_f32 v[102:103], v[102:103], v[150:151]\n"
      "v_pk_add_f32 v[100:101], v[100:101], v[196:197]\n"
      "v_pk_add_f32 v[102:103], v[102:103], v[198:199]\n"
      "v_pk_add_f32 v[100:101], v[100:101], v[244:245]\n"
      "v_pk_add_f32 v[102:103], v[102:103], v[246:247]\n"
      "v_cvt_pk_bf16_f32 v146, v100, v101\n"
      "v_cvt_pk_bf16_f32 v147, v102, v103\n"
      "v_bitop3_b32 v148, v144, v145, v5 bitop3:0x96\n"
      "v_bitop3_b32 v148, v148, v146, v147 bitop3:0x96\n"
      "buffer_store_dwordx4 v[144:147], v20, s[52:55], s45 offen offset:2560 sc1\n"
      "buffer_store_dword v148, v22, s[52:55], s45 offen offset:3584 sc1\n"
      "v_pk_add_f32 v[104:105], v[104:105], v[152:153]\n"
      "v_pk_add_f32 v[106:107], v[106:107], v[154:155]\n"
      "v_pk_add_f32 v[104:105], v[104:105], v[200:201]\n"
      "v_pk_add_f32 v[106:107], v[106:107], v[202:203]\n"
      "v_pk_add_f32 v[104:105], v[104:105], v[248:249]\n"
      "v_pk_add_f32 v[106:107], v[106:107], v[250:251]\n"
      "v_cvt_pk_bf16_f32 v152, v104, v105\n"
      "v_cvt_pk_bf16_f32 v153, v106, v107\n"
      "v_pk_add_f32 v[108:109], v[108:109], v[156:157]\n"
      "v_pk_add_f32 v[110:111], v[110:111], v[158:159]\n"
      "v_pk_add_f32 v[108:109], v[108:109], v[204:205]\n"
      "v_pk_add_f32 v[110:111], v[110:111], v[206:207]\n"
      "v_pk_add_f32 v[108:109], v[108:109], v[252:253]\n"
      "v_pk_add_f32 v[110:111], v[110:111], v[254:255]\n"
      "v_cvt_pk_bf16_f32 v154, v108, v109\n"
      "v_cvt_pk_bf16_f32 v155, v110, v111\n"
      "v_bitop3_b32 v156, v152, v153, v5 bitop3:0x96\n"
      "v_bitop3_b32 v156, v156, v154, v155 bitop3:0x96\n"
      "buffer_store_dwordx4 v[152:155], v4, s[52:55], s47 offen offset:2560 sc1\n"
      "buffer_store_dword v156, v23, s[52:55], s47 offen offset:3584 sc1\n"
      "s_branch .Lsent_%=\n"
      ".Lr_last2_%=:\n"
      "v_and_b32 v6, 7, v1\n"
      "v_min_u32 v6, 6, v6\n"
      "v_mul_u32_u24 v6, 3840, v6\n"
      "ds_read_b128 v[24:27], v21 offset:24576\n"
      "ds_read_b128 v[28:31], v21 offset:25600\n"
      "ds_read_b128 v[32:35], v21 offset:26624\n"
      "ds_read_b128 v[36:39], v21 offset:27648\n"
      "ds_read_b128 v[40:43], v21 offset:28672\n"
      "ds_read_b128 v[44:47], v21 offset:29696\n"
      "ds_read_b128 v[48:51], v21 offset:30720\n"
      "ds_read_b128 v[52:55], v21 offset:31744\n"
      "ds_read_b128 v[56:59], v21 offset:32768\n"
      "ds_read_b128 v[60:63], v21 offset:33792\n"
      "ds_read_b128 v[10:13], v21 offset:34816\n"
      "ds_read_b128 v[16:19], v21 offset:35840\n"
      "s_waitcnt lgkmcnt(11)\n"
      "v_mfma_f32_16x16x32_bf16 v[160:163], a[128:131], v[24:27], 0\n"
      "v_mfma_f32_16x16x32_bf16 v[164:167], a[132:135], v[24:27], 0\n"
      "v_mfma_f32_16x16x32_bf16 v[168:171], a[136:139], v[24:27], 0\n"
      "v_mfma_f32_16x16x32_bf16 v[172:175], a[140:143], v[24:27], 0\n"
      "s_waitcnt lgkmcnt(10)\n"
      "v_mfma_f32_16x16x32_bf16 v[160:163], a[144:147], v[28:31], v[160:163]\n"
      "v_mfma_f32_16x16x32_bf16 v[164:167], a[148:151], v[28:31], v[164:167]\n"
      "v_mfma_f32_16x16x32_bf16 v[168:171], a[152:155], v[28:31], v[168:171]\n"
      "v_mfma_f32_16x16x32_bf16 v[172:175], a[156:159], v[28:31], v[172:175]\n"
      "s_waitcnt lgkmcnt(9)\n"
      "v_mfma_f32_16x16x32_bf16 v[160:163], a[160:163], v[32:35], v[160:163]\n"
      "v_mfma_f32_16x16x32_bf16 v[164:167], a[164:167], v[32:35], v[164:167]\n"
      "v_mfma_f32_16x16x32_bf16 v[168:171], a[168:171], v[32:35], v[168:171]\n"
      "v_mfma_f32_16x16x32_bf16 v[172:175], a[172:175], v[32:35], v[172:175]\n"
      "s_waitcnt lgkmcnt(8)\n"
      "v_mfma_f32_16x16x32_bf16 v[160:163], a[176:179], v[36:39], v[160:163]\n"
      "v_mfma_f32_16x16x32_bf16 v[164:167], a[180:183], v[36:39], v[164:167]\n"
      "v_mfma_f32_16x16x32_bf16 v[168:171], a[184:187], v[36:39], v[168:171]\n"
      "v_mfma_f32_16x16x32_bf16 v[172:175], a[188:191], v[36:39], v[172:175]\n"
      "s_waitcnt lgkmcnt(7)\n"
      "v_mfma_f32_16x16x32_bf16 v[176:179], a[128:131], v[40:43], 0\n"
      "v_mfma_f32_16x16x32_bf16 v[180:183], a[132:135], v[40:43], 0\n"
      "v_mfma_f32_16x16x32_bf16 v[184:187], a[136:139], v[40:43], 0\n"
      "v_mfma_f32_16x16x32_bf16 v[188:191], a[140:143], v[40:43], 0\n"
      "s_waitcnt lgkmcnt(6)\n"
      "v_mfma_f32_16x16x32_bf16 v[176:179], a[144:147], v[44:47], v[176:179]\n"
      "v_mfma_f32_16x16x32_bf16 v[180:183], a[148:151], v[44:47], v[180:183]\n"
      "v_mfma_f32_16x16x32_bf16 v[184:187], a[152:155], v[44:47], v[184:187]\n"
      "v_mfma_f32_16x16x32_bf16 v[188:191], a[156:159], v[44:47], v[188:191]\n"
      "v_pk_add_f32 v[64:65], v[64:65], v[112:113]\n"
      "v_pk_add_f32 v[66:67], v[66:67], v[114:115]\n"
      "v_pk_add_f32 v[64:65], v[64:65], v[160:161]\n"
      "v_pk_add_f32 v[66:67], v[66:67], v[162:163]\n"
      "v_pk_add_f32 v[64:65], v[64:65], v[208:209]\n"
      "v_pk_add_f32 v[66:67], v[66:67], v[210:211]\n"
      "v_cvt_pk_bf16_f32 v112, v64, v65\n"
      "v_cvt_pk_bf16_f32 v113, v66, v67\n"
      "s_waitcnt lgkmcnt(5)\n"
      "v_mfma_f32_16x16x32_bf16 v[176:179], a[160:163], v[48:51], v[176:179]\n"
      "v_mfma_f32_16x16x32_bf16 v[180:183], a[164:167], v[48:51], v[180:183]\n"
      "v_mfma_f32_16x16x32_bf16 v[184:187], a[168:171], v[48:51], v[184:187]\n"
      "v_mfma_f32_16x16x32_bf16 v[188:191], a[172:175], v[48:51], v[188:191]\n"
      "v_pk_add_f32 v[68:69], v[68:69], v[116:117]\n"
      "v_pk_add_f32 v[70:71], v[70:71], v[118:119]\n"
      "v_pk_add_f32 v[68:69], v[68:69], v[164:165]\n"
      "v_pk_add_f32 v[70:71], v[70:71], v[166:167]\n"
      "v_pk_add_f32 v[68:69], v[68:69], v[212:213]\n"
      "v_pk_add_f32 v[70:71], v[70:71], v[214:215]\n"
      "v_cvt_pk_bf16_f32 v114, v68, v69\n"
      "v_cvt_pk_bf16_f32 v115, v70, v71\n"
      "v_bitop3_b32 v116, v112, v113, v5 bitop3:0x96\n"
      "v_bitop3_b32 v116, v116, v114, v115 bitop3:0x96\n"
      "buffer_store_dwordx4 v[112:115], v20, s[52:55], s45 offen offset:0 sc1\n"
      "buffer_store_dword v116, v22, s[52:55], s45 offen offset:1024 sc1\n"
      "s_waitcnt lgkmcnt(4)\n"
      "v_mfma_f32_16x16x32_bf16 v[176:179], a[176:179], v[52:55], v[176:179]\n"
      "v_mfma_f32_16x16x32_bf16 v[180:183], a[180:183], v[52:55], v[180:183]\n"
      "v_mfma_f32_16x16x32_bf16 v[184:187], a[184:187], v[52:55], v[184:187]\n"
      "v_mfma_f32_16x16x32_bf16 v[188:191], a[188:191], v[52:55], v[188:191]\n"
      "v_pk_add_f32 v[72:73], v[72:73], v[120:121]\n"
      "v_pk_add_f32 v[74:75], v[74:75], v[122:123]\n"
      "v_pk_add_f32 v[72:73], v[72:73], v[168:169]\n"
      "v_pk_add_f32 v[74:75], v[74:75], v[170:171]\n"
      "v_pk_add_f32 v[72:73], v[72:73], v[216:217]\n"
      "v_pk_add_f32 v[74:75], v[74:75], v[218:219]\n"
      "v_cvt_pk_bf16_f32 v120, v72, v73\n"
      "v_cvt_pk_bf16_f32 v121, v74, v75\n"
      "s_waitcnt lgkmcnt(3)\n"
      "v_mfma_f32_16x16x32_bf16 v[192:195], a[128:131], v[56:59], 0\n"
      "v_mfma_f32_16x16x32_bf16 v[196:199], a[132:135], v[56:59], 0\n"
      "v_mfma_f32_16x16x32_bf16 v[200:203], a[136:139], v[56:59], 0\n"
      "v_mfma_f32_16x16x32_bf16 v[204:207], a[140:143], v[56:59], 0\n"
      "v_pk_add_f32 v[76:77], v[76:77], v[124:125]\n"
      "v_pk_add_f32 v[78:79], v[78:79], v[126:127]\n"
      "v_pk_add_f32 v[76:77], v[76:77], v[172:173]\n"
      "v_pk_add_f32 v[78:79], v[78:79], v[174:175]\n"
      "v_pk_add_f32 v[76:77], v[76:77], v[220:221]\n"
      "v_pk_add_f32 v[78:79], v[78:79], v[222:223]\n"
      "v_cvt_pk_bf16_f32 v122, v76, v77\n"
      "v_cvt_pk_bf16_f32 v123, v78, v79\n"
      "v_bitop3_b32 v124, v120, v121, v5 bitop3:0x96\n"
      "v_bitop3_b32 v124, v124, v122, v123 bitop3:0x96\n"
      "buffer_store_dwordx4 v[120:123], v4, s[52:55], s47 offen offset:0 sc1\n"
      "buffer_store_dword v124, v23, s[52:55], s47 offen offset:1024 sc1\n"
      "s_waitcnt lgkmcnt(2)\n"
      "v_mfma_f32_16x16x32_bf16 v[192:195], a[144:147], v[60:63], v[192:195]\n"
      "v_mfma_f32_16x16x32_bf16 v[196:199], a[148:151], v[60:63], v[196:199]\n"
      "v_mfma_f32_16x16x32_bf16 v[200:203], a[152:155], v[60:63], v[200:203]\n"
      "v_mfma_f32_16x16x32_bf16 v[204:207], a[156:159], v[60:63], v[204:207]\n"
      "v_pk_add_f32 v[80:81], v[80:81], v[128:129]\n"
      "v_pk_add_f32 v[82:83], v[82:83], v[130:131]\n"
      "v_pk_add_f32 v[80:81], v[80:81], v[176:177]\n"
      "v_pk_add_f32 v[82:83], v[82:83], v[178:179]\n"
      "v_pk_add_f32 v[80:81], v[80:81], v[224:225]\n"
      "v_pk_add_f32 v[82:83], v[82:83], v[226:227]\n"
      "v_cvt_pk_bf16_f32 v128, v80, v81\n"
      "v_cvt_pk_bf16_f32 v129, v82, v83\n"
      "s_waitcnt lgkmcnt(1)\n"
      "v_mfma_f32_16x16x32_bf16 v[192:195], a[160:163], v[10:13], v[192:195]\n"
      "v_mfma_f32_16x16x32_bf16 v[196:199], a[164:167], v[10:13], v[196:199]\n"
      "v_mfma_f32_16x16x32_bf16 v[200:203], a[168:171], v[10:13], v[200:203]\n"
      "v_mfma_f32_16x16x32_bf16 v[204:207], a[172:175], v[10:13], v[204:207]\n"
      "v_pk_add_f32 v[84:85], v[84:85], v[132:133]\n"
      "v_pk_add_f32 v[86:87], v[86:87], v[134:135]\n"
      "v_pk_add_f32 v[84:85], v[84:85], v[180:181]\n"
      "v_pk_add_f32 v[86:87], v[86:87], v[182:183]\n"
      "v_pk_add_f32 v[84:85], v[84:85], v[228:229]\n"
      "v_pk_add_f32 v[86:87], v[86:87], v[230:231]\n"
      "v_cvt_pk_bf16_f32 v130, v84, v85\n"
      "v_cvt_pk_bf16_f32 v131, v86, v87\n"
      "v_bitop3_b32 v132, v128, v129, v5 bitop3:0x96\n"
      "v_bitop3_b32 v132, v132, v130, v131 bitop3:0x96\n"
      "buffer_store_dwordx4 v[128:131], v20, s[52:55], s45 offen offset:1280 sc1\n"
      "buffer_store_dword v132, v22, s[52:55], s45 offen offset:2304 sc1\n"
      "s_waitcnt lgkmcnt(0)\n"
      "v_mfma_f32_16x16x32_bf16 v[192:195], a[176:179], v[16:19], v[192:195]\n"
      "v_mfma_f32_16x16x32_bf16 v[196:199], a[180:183], v[16:19], v[196:199]\n"
      "v_mfma_f32_16x16x32_bf16 v[200:203], a[184:187], v[16:19], v[200:203]\n"
      "v_mfma_f32_16x16x32_bf16 v[204:207], a[188:191], v[16:19], v[204:207]\n"
      "v_pk_add_f32 v[88:89], v[88:89], v[136:137]\n"
      "v_pk_add_f32 v[90:91], v[90:91], v[138:139]\n"
      "v_pk_add_f32 v[88:89], v[88:89], v[184:185]\n"
      "v_pk_add_f32 v[90:91], v[90:91], v[186:187]\n"
      "v_pk_add_f32 v[88:89], v[88:89], v[232:233]\n"
      "v_pk_add_f32 v[90:91], v[90:91], v[234:235]\n"
      "v_cvt_pk_bf16_f32 v136, v88, v89\n"
      "v_cvt_pk_bf16_f32 v137, v90, v91\n"
      "v_pk_add_f32 v[92:93], v[92:93], v[140:141]\n"
      "v_pk_add_f32 v[94:95], v[94:95], v[142:143]\n"
      "v_pk_add_f32 v[92:93], v[92:93], v[188:189]\n"
      "v_pk_add_f32 v[94:95], v[94:95], v[190:191]\n"
      "v_pk_add_f32 v[92:93], v[92:93], v[236:237]\n"
      "v_pk_add_f32 v[94:95], v[94:95], v[238:239]\n"
      "v_cvt_pk_bf16_f32 v138, v92, v93\n"
      "v_cvt_pk_bf16_f32 v139, v94, v95\n"
      "v_bitop3_b32 v140, v136, v137, v5 bitop3:0x96\n"
      "v_bitop3_b32 v140, v140, v138, v139 bitop3:0x96\n"
      "buffer_store_dwordx4 v[136:139], v4, s[52:55], s47 offen offset:1280 sc1\n"
      "buffer_store_dword v140, v23, s[52:55], s47 offen offset:2304 sc1\n"
      "v_pk_add_f32 v[96:97], v[96:97], v[144:145]\n"
      "v_pk_add_f32 v[98:99], v[98:99], v[146:147]\n"
      "v_pk_add_f32 v[96:97], v[96:97], v[192:193]\n"
      "v_pk_add_f32 v[98:99], v[98:99], v[194:195]\n"
      "v_pk_add_f32 v[96:97], v[96:97], v[240:241]\n"
      "v_pk_add_f32 v[98:99], v[98:99], v[242:243]\n"
      "v_cvt_pk_bf16_f32 v144, v96, v97\n"
      "v_cvt_pk_bf16_f32 v145, v98, v99\n"
      "v_pk_add_f32 v[100:101], v[100:101], v[148:149]\n"
      "v_pk_add_f32 v[102:103], v[102:103], v[150:151]\n"
      "v_pk_add_f32 v[100:101], v[100:101], v[196:197]\n"
      "v_pk_add_f32 v[102:103], v[102:103], v[198:199]\n"
      "v_pk_add_f32 v[100:101], v[100:101], v[244:245]\n"
      "v_pk_add_f32 v[102:103], v[102:103], v[246:247]\n"
      "v_cvt_pk_bf16_f32 v146, v100, v101\n"
      "v_cvt_pk_bf16_f32 v147, v102, v103\n"
      "v_bitop3_b32 v148, v144, v145, v5 bitop3:0x96\n"
      "v_bitop3_b32 v148, v148, v146, v147 bitop3:0x96\n"
      "buffer_store_dwordx4 v[144:147], v20, s[52:55], s45 offen offset:2560 sc1\n"
      "buffer_store_dword v148, v22, s[52:55], s45 offen offset:3584 sc1\n"
      "v_pk_add_f32 v[104:105], v[104:105], v[152:153]\n"
      "v_pk_add_f32 v[106:107], v[106:107], v[154:155]\n"
      "v_pk_add_f32 v[104:105], v[104:105], v[200:201]\n"
      "v_pk_add_f32 v[106:107], v[106:107], v[202:203]\n"
      "v_pk_add_f32 v[104:105], v[104:105], v[248:249]\n"
      "v_pk_add_f32 v[106:107], v[106:107], v[250:251]\n"
      "v_cvt_pk_bf16_f32 v152, v104, v105\n"
      "v_cvt_pk_bf16_f32 v153, v106, v107\n"
      "v_pk_add_f32 v[108:109], v[108:109], v[156:157]\n"
      "v_pk_add_f32 v[110:111], v[110:111], v[158:159]\n"
      "v_pk_add_f32 v[108:109], v[108:109], v[204:205]\n"
      "v_pk_add_f32 v[110:111], v[110:111], v[206:207]\n"
      "v_pk_add_f32 v[108:109], v[108:109], v[252:253]\n"
      "v_pk_add_f32 v[110:111], v[110:111], v[254:255]\n"
      "v_cvt_pk_bf16_f32 v154, v108, v109\n"
      "v_cvt_pk_bf16_f32 v155, v110, v111\n"
      "v_bitop3_b32 v156, v152, v153, v5 bitop3:0x96\n"
      "v_bitop3_b32 v156, v156, v154, v155 bitop3:0x96\n"
      "buffer_store_dwordx4 v[152:155], v4, s[52:55], s47 offen offset:2560 sc1\n"
      "buffer_store_dword v156, v23, s[52:55], s47 offen offset:3584 sc1\n"
      "s_branch .Lsent_%=\n"
      ".Lr_last3_%=:\n"
      "v_and_b32 v6, 7, v1\n"
      "v_min_u32 v6, 6, v6\n"
      "v_mul_u32_u24 v6, 3840, v6\n"
      "ds_read_b128 v[24:27], v21 offset:36864\n"
      "ds_read_b128 v[28:31], v21 offset:37888\n"
      "ds_read_b128 v[32:35], v21 offset:38912\n"
      "ds_read_b128 v[36:39], v21 offset:39936\n"
      "ds_read_b128 v[40:43], v21 offset:40960\n"
      "ds_read_b128 v[44:47], v21 offset:41984\n"
      "ds_read_b128 v[48:51], v21 offset:43008\n"
      "ds_read_b128 v[52:55], v21 offset:44032\n"
      "ds_read_b128 v[56:59], v21 offset:45056\n"
      "ds_read_b128 v[60:63], v21 offset:46080\n"
      "ds_read_b128 v[10:13], v21 offset:47104\n"
      "ds_read_b128 v[16:19], v21 offset:48128\n"
      "s_waitcnt lgkmcnt(11)\n"
      "v_mfma_f32_16x16x32_bf16 v[208:211], a[192:195], v[24:27], 0\n"
      "v_mfma_f32_16x16x32_bf16 v[212:215], a[196:199], v[24:27], 0\n"
      "v_mfma_f32_16x16x32_bf16 v[216:219], a[200:203], v[24:27], 0\n"
      "v_mfma_f32_16x16x32_bf16 v[220:223], a[204:207], v[24:27], 0\n"
      "s_waitcnt lgkmcnt(10)\n"
      "v_mfma_f32_16x16x32_bf16 v[208:211], a[208:211], v[28:31], v[208:211]\n"
      "v_mfma_f32_16x16x32_bf16 v[212:215], a[212:215], v[28:31], v[212:215]\n"
      "v_mfma_f32_16x16x32_bf16 v[216:219], a[216:219], v[28:31], v[216:219]\n"
      "v_mfma_f32_16x16x32_bf16 v[220:223], a[220:223], v[28:31], v[220:223]\n"
      "s_waitcnt lgkmcnt(9)\n"
      "v_mfma_f32_16x16x32_bf16 v[208:211], a[224:227], v[32:35], v[208:211]\n"
      "v_mfma_f32_16x16x32_bf16 v[212:215], a[228:231], v[32:35], v[212:215]\n"
      "v_mfma_f32_16x16x32_bf16 v[216:219], a[232:235], v[32:35], v[216:219]\n"
      "v_mfma_f32_16x16x32_bf16 v[220:223], a[236:239], v[32:35], v[220:223]\n"
      "s_waitcnt lgkmcnt(8)\n"
      "v_mfma_f32_16x16x32_bf16 v[208:211], a[240:243], v[36:39], v[208:211]\n"
      "v_mfma_f32_16x16x32_bf16 v[212:215], a[244:247], v[36:39], v[212:215]\n"
      "v_mfma_f32_16x16x32_bf16 v[216:219], a[248:251], v[36:39], v[216:219]\n"
      "v_mfma_f32_16x16x32_bf16 v[220:223], a[252:255], v[36:39], v[220:223]\n"
      "s_waitcnt lgkmcnt(7)\n"
      "v_mfma_f32_16x16x32_bf16 v[224:227], a[192:195], v[40:43], 0\n"
      "v_mfma_f32_16x16x32_bf16 v[228:231], a[196:199], v[40:43], 0\n"
      "v_mfma_f32_16x16x32_bf16 v[232:235], a[200:203], v[40:43], 0\n"
      "v_mfma_f32_16x16x32_bf16 v[236:239], a[204:207], v[40:43], 0\n"
      "s_waitcnt lgkmcnt(6)\n"
      "v_mfma_f32_16x16x32_bf16 v[224:227], a[208:211], v[44:47], v[224:227]\n"
      "v_mfma_f32_16x16x32_bf16 v[228:231], a[212:215], v[44:47], v[228:231]\n"
      "v_mfma_f32_16x16x32_bf16 v[232:235], a[216:219], v[44:47], v[232:235]\n"
      "v_mfma_f32_16x16x32_bf16 v[236:239], a[220:223], v[44:47], v[236:239]\n"
      "v_pk_add_f32 v[64:65], v[64:65], v[112:113]\n"
      "v_pk_add_f32 v[66:67], v[66:67], v[114:115]\n"
      "v_pk_add_f32 v[64:65], v[64:65], v[160:161]\n"
      "v_pk_add_f32 v[66:67], v[66:67], v[162:163]\n"
      "v_pk_add_f32 v[64:65], v[64:65], v[208:209]\n"
      "v_pk_add_f32 v[66:67], v[66:67], v[210:211]\n"
      "v_cvt_pk_bf16_f32 v112, v64, v65\n"
      "v_cvt_pk_bf16_f32 v113, v66, v67\n"
      "s_waitcnt lgkmcnt(5)\n"
      "v_mfma_f32_16x16x32_bf16 v[224:227], a[224:227], v[48:51], v[224:227]\n"
      "v_mfma_f32_16x16x32_bf16 v[228:231], a[228:231], v[48:51], v[228:231]\n"
      "v_mfma_f32_16x16x32_bf16 v[232:235], a[232:235], v[48:51], v[232:235]\n"
      "v_mfma_f32_16x16x32_bf16 v[236:239], a[236:239], v[48:51], v[236:239]\n"
      "v_pk_add_f32 v[68:69], v[68:69], v[116:117]\n"
      "v_pk_add_f32 v[70:71], v[70:71], v[118:119]\n"
      "v_pk_add_f32 v[68:69], v[68:69], v[164:165]\n"
      "v_pk_add_f32 v[70:71], v[70:71], v[166:167]\n"
      "v_pk_add_f32 v[68:69], v[68:69], v[212:213]\n"
      "v_pk_add_f32 v[70:71], v[70:71], v[214:215]\n"
      "v_cvt_pk_bf16_f32 v114, v68, v69\n"
      "v_cvt_pk_bf16_f32 v115, v70, v71\n"
      "v_bitop3_b32 v116, v112, v113, v5 bitop3:0x96\n"
      "v_bitop3_b32 v116, v116, v114, v115 bitop3:0x96\n"
      "buffer_store_dwordx4 v[112:115], v20, s[52:55], s45 offen offset:0 sc1\n"
      "buffer_store_dword v116, v22, s[52:55], s45 offen offset:1024 sc1\n"
      "s_waitcnt lgkmcnt(4)\n"
      "v_mfma_f32_16x16x32_bf16 v[224:227], a[240:243], v[52:55], v[224:227]\n"
      "v_mfma_f32_16x16x32_bf16 v[228:231], a[244:247], v[52:55], v[228:231]\n"
      "v_mfma_f32_16x16x32_bf16 v[232:235], a[248:251], v[52:55], v[232:235]\n"
      "v_mfma_f32_16x16x32_bf16 v[236:239], a[252:255], v[52:55], v[236:239]\n"
      "v_pk_add_f32 v[72:73], v[72:73], v[120:121]\n"
      "v_pk_add_f32 v[74:75], v[74:75], v[122:123]\n"
      "v_pk_add_f32 v[72:73], v[72:73], v[168:169]\n"
      "v_pk_add_f32 v[74:75], v[74:75], v[170:171]\n"
      "v_pk_add_f32 v[72:73], v[72:73], v[216:217]\n"
      "v_pk_add_f32 v[74:75], v[74:75], v[218:219]\n"
      "v_cvt_pk_bf16_f32 v120, v72, v73\n"
      "v_cvt_pk_bf16_f32 v121, v74, v75\n"
      "s_waitcnt lgkmcnt(3)\n"
      "v_mfma_f32_16x16x32_bf16 v[240:243], a[192:195], v[56:59], 0\n"
      "v_mfma_f32_16x16x32_bf16 v[244:247], a[196:199], v[56:59], 0\n"
      "v_mfma_f32_16x16x32_bf16 v[248:251], a[200:203], v[56:59], 0\n"
      "v_mfma_f32_16x16x32_bf16 v[252:255], a[204:207], v[56:59], 0\n"
      "v_pk_add_f32 v[76:77], v[76:77], v[124:125]\n"
      "v_pk_add_f32 v[78:79], v[78:79], v[126:127]\n"
      "v_pk_add_f32 v[76:77], v[76:77], v[172:173]\n"
      "v_pk_add_f32 v[78:79], v[78:79], v[174:175]\n"
      "v_pk_add_f32 v[76:77], v[76:77], v[220:221]\n"
      "v_pk_add_f32 v[78:79], v[78:79], v[222:223]\n"
      "v_cvt_pk_bf16_f32 v122, v76, v77\n"
      "v_cvt_pk_bf16_f32 v123, v78, v79\n"
      "v_bitop3_b32 v124, v120, v121, v5 bitop3:0x96\n"
      "v_bitop3_b32 v124, v124, v122, v123 bitop3:0x96\n"
      "buffer_store_dwordx4 v[120:123], v4, s[52:55], s47 offen offset:0 sc1\n"
      "buffer_store_dword v124, v23, s[52:55], s47 offen offset:1024 sc1\n"
      "s_waitcnt lgkmcnt(2)\n"
      "v_mfma_f32_16x16x32_bf16 v[240:243], a[208:211], v[60:63], v[240:243]\n"
      "v_mfma_f32_16x16x32_bf16 v[244:247], a[212:215], v[60:63], v[244:247]\n"
      "v_mfma_f32_16x16x32_bf16 v[248:251], a[216:219], v[60:63], v[248:251]\n"
      "v_mfma_f32_16x16x32_bf16 v[252:255], a[220:223], v[60:63], v[252:255]\n"
      "v_pk_add_f32 v[80:81], v[80:81], v[128:129]\n"
      "v_pk_add_f32 v[82:83], v[82:83], v[130:131]\n"
      "v_pk_add_f32 v[80:81], v[80:81], v[176:177]\n"
      "v_pk_add_f32 v[82:83], v[82:83], v[178:179]\n"
      "v_pk_add_f32 v[80:81], v[80:81], v[224:225]\n"
      "v_pk_add_f32 v[82:83], v[82:83], v[226:227]\n"
      "v_cvt_pk_bf16_f32 v128, v80, v81\n"
      "v_cvt_pk_bf16_f32 v129, v82, v83\n"
      "s_waitcnt lgkmcnt(1)\n"
      "v_mfma_f32_16x16x32_bf16 v[240:243], a[224:227], v[10:13], v[240:243]\n"
      "v_mfma_f32_16x16x32_bf16 v[244:247], a[228:231], v[10:13], v[244:247]\n"
      "v_mfma_f32_16x16x32_bf16 v[248:251], a[232:235], v[10:13], v[248:251]\n"
      "v_mfma_f32_16x16x32_bf16 v[252:255], a[236:239], v[10:13], v[252:255]\n"
      "v_pk_add_f32 v[84:85], v[84:85], v[132:133]\n"
      "v_pk_add_f32 v[86:87], v[86:87], v[134:135]\n"
      "v_pk_add_f32 v[84:85], v[84:85], v[180:181]\n"
      "v_pk_add_f32 v[86:87], v[86:87], v[182:183]\n"
      "v_pk_add_f32 v[84:85], v[84:85], v[228:229]\n"
      "v_pk_add_f32 v[86:87], v[86:87], v[230:231]\n"
      "v_cvt_pk_bf16_f32 v130, v84, v85\n"
      "v_cvt_pk_bf16_f32 v131, v86, v87\n"
      "v_bitop3_b32 v132, v128, v129, v5 bitop3:0x96\n"
      "v_bitop3_b32 v132, v132, v130, v131 bitop3:0x96\n"
      "buffer_store_dwordx4 v[128:131], v20, s[52:55], s45 offen offset:1280 sc1\n"
      "buffer_store_dword v132, v22, s[52:55], s45 offen offset:2304 sc1\n"
      "s_waitcnt lgkmcnt(0)\n"
      "v_mfma_f32_16x16x32_bf16 v[240:243], a[240:243], v[16:19], v[240:243]\n"
      "v_mfma_f32_16x16x32_bf16 v[244:247], a[244:247], v[16:19], v[244:247]\n"
      "v_mfma_f32_16x16x32_bf16 v[248:251], a[248:251], v[16:19], v[248:251]\n"
      "v_mfma_f32_16x16x32_bf16 v[252:255], a[252:255], v[16:19], v[252:255]\n"
      "v_pk_add_f32 v[88:89], v[88:89], v[136:137]\n"
      "v_pk_add_f32 v[90:91], v[90:91], v[138:139]\n"
      "v_pk_add_f32 v[88:89], v[88:89], v[184:185]\n"
      "v_pk_add_f32 v[90:91], v[90:91], v[186:187]\n"
      "v_pk_add_f32 v[88:89], v[88:89], v[232:233]\n"
      "v_pk_add_f32 v[90:91], v[90:91], v[234:235]\n"
      "v_cvt_pk_bf16_f32 v136, v88, v89\n"
      "v_cvt_pk_bf16_f32 v137, v90, v91\n"
      "v_pk_add_f32 v[92:93], v[92:93], v[140:141]\n"
      "v_pk_add_f32 v[94:95], v[94:95], v[142:143]\n"
      "v_pk_add_f32 v[92:93], v[92:93], v[188:189]\n"
      "v_pk_add_f32 v[94:95], v[94:95], v[190:191]\n"
      "v_pk_add_f32 v[92:93], v[92:93], v[236:237]\n"
      "v_pk_add_f32 v[94:95], v[94:95], v[238:239]\n"
      "v_cvt_pk_bf16_f32 v138, v92, v93\n"
      "v_cvt_pk_bf16_f32 v139, v94, v95\n"
      "v_bitop3_b32 v140, v136, v137, v5 bitop3:0x96\n"
      "v_bitop3_b32 v140, v140, v138, v139 bitop3:0x96\n"
      "buffer_store_dwordx4 v[136:139], v4, s[52:55], s47 offen offset:1280 sc1\n"
      "buffer_store_dword v140, v23, s[52:55], s47 offen offset:2304 sc1\n"
      "v_pk_add_f32 v[96:97], v[96:97], v[144:145]\n"
      "v_pk_add_f32 v[98:99], v[98:99], v[146:147]\n"
      "v_pk_add_f32 v[96:97], v[96:97], v[192:193]\n"
      "v_pk_add_f32 v[98:99], v[98:99], v[194:195]\n"
      "v_pk_add_f32 v[96:97], v[96:97], v[240:241]\n"
      "v_pk_add_f32 v[98:99], v[98:99], v[242:243]\n"
      "v_cvt_pk_bf16_f32 v144, v96, v97\n"
      "v_cvt_pk_bf16_f32 v145, v98, v99\n"
      "v_pk_add_f32 v[100:101], v[100:101], v[148:149]\n"
      "v_pk_add_f32 v[102:103], v[102:103], v[150:151]\n"
      "v_pk_add_f32 v[100:101], v[100:101], v[196:197]\n"
      "v_pk_add_f32 v[102:103], v[102:103], v[198:199]\n"
      "v_pk_add_f32 v[100:101], v[100:101], v[244:245]\n"
      "v_pk_add_f32 v[102:103], v[102:103], v[246:247]\n"
      "v_cvt_pk_bf16_f32 v146, v100, v101\n"
      "v_cvt_pk_bf16_f32 v147, v102, v103\n"
      "v_bitop3_b32 v148, v144, v145, v5 bitop3:0x96\n"
      "v_bitop3_b32 v148, v148, v146, v147 bitop3:0x96\n"
      "buffer_store_dwordx4 v[144:147], v20, s[52:55], s45 offen offset:2560 sc1\n"
      "buffer_store_dword v148, v22, s[52:55], s45 offen offset:3584 sc1\n"
      "v_pk_add_f32 v[104:105], v[104:105], v[152:153]\n"
      "v_pk_add_f32 v[106:107], v[106:107], v[154:155]\n"
      "v_pk_add_f32 v[104:105], v[104:105], v[200:201]\n"
      "v_pk_add_f32 v[106:107], v[106:107], v[202:203]\n"
      "v_pk_add_f32 v[104:105], v[104:105], v[248:249]\n"
      "v_pk_add_f32 v[106:107], v[106:107], v[250:251]\n"
      "v_cvt_pk_bf16_f32 v152, v104, v105\n"
      "v_cvt_pk_bf16_f32 v153, v106, v107\n"
      "v_pk_add_f32 v[108:109], v[108:109], v[156:157]\n"
      "v_pk_add_f32 v[110:111], v[110:111], v[158:159]\n"
      "v_pk_add_f32 v[108:109], v[108:109], v[204:205]\n"
      "v_pk_add_f32 v[110:111], v[110:111], v[206:207]\n"
      "v_pk_add_f32 v[108:109], v[108:109], v[252:253]\n"
      "v_pk_add_f32 v[110:111], v[110:111], v[254:255]\n"
      "v_cvt_pk_bf16_f32 v154, v108, v109\n"
      "v_cvt_pk_bf16_f32 v155, v110, v111\n"
      "v_bitop3_b32 v156, v152, v153, v5 bitop3:0x96\n"
      "v_bitop3_b32 v156, v156, v154, v155 bitop3:0x96\n"
      "buffer_store_dwordx4 v[152:155], v4, s[52:55], s47 offen offset:2560 sc1\n"
      "buffer_store_dword v156, v23, s[52:55], s47 offen offset:3584 sc1\n"
      "s_branch .Lsent_%=\n"
      ".Lsent_%=:\n"
      "s_lshr_b32 s29, s21, 1\n"
      "s_cmp_lg_u32 s29, s20\n"
      "s_cbranch_scc1 .Lkept_%=\n"
      "s_bitcmp1_b32 s21, 0\n"
      "s_cbranch_scc1 .Lkeep1_%=\n"
      ".Lkeep0_%=:\n"
      "ds_write_b128 v21, v[64:67] offset:50176\n"
      "ds_write_b128 v21, v[68:71] offset:51200\n"
      "ds_write_b128 v21, v[80:83] offset:52224\n"
      "ds_write_b128 v21, v[84:87] offset:53248\n"
      "ds_write_b128 v21, v[96:99] offset:54272\n"
      "ds_write_b128 v21, v[100:103] offset:55296\n"
      "s_branch .Lkept_%=\n"
      ".Lkeep1_%=:\n"
      "ds_write_b128 v21, v[72:75] offset:50176\n"
      "ds_write_b128 v21, v[76:79] offset:51200\n"
      "ds_write_b128 v21, v[88:91] offset:52224\n"
      "ds_write_b128 v21, v[92:95] offset:53248\n"
      "ds_write_b128 v21, v[104:107] offset:54272\n"
      "ds_write_b128 v21, v[108:111] offset:55296\n"
      ".Lkept_%=:\n"
      "s_waitcnt lgkmcnt(0)\n"
      "s_barrier\n"
      "s_cmp_eq_u32 s20, 3\n"
      "s_cbranch_scc1 .Lend_%=\n"
      "s_lshl_b32 s31, s20, 11\n"
      "s_mov_b32 s68, 0\n"
      "s_add_u32 s31, s31, 50176\n"
      "v_add_u32 v9, s31, v21\n"
      "ds_read_b128 v[104:107], v9\n"
      "ds_read_b128 v[108:111], v9 offset:1024\n"
      "s_mul_i32 s29, s23, 96\n"
      "s_mul_i32 s30, s26, 48\n"
      "s_add_u32 s29, s29, s30\n"
      "s_lshl_b32 s30, s20, 4\n"
      "s_add_u32 s29, s29, s30\n"
      "s_lshl_b32 s29, s29, 10\n"
      "s_lshl_b32 s30, s27, 9\n"
      "s_add_u32 s29, s29, s30\n"
      "s_lshl_b32 s30, s21, 6\n"
      "s_add_u32 s31, s29, s30\n"
      "v_and_b32 v8, 15, v1\n"
      "v_lshrrev_b32 v9, 4, v1\n"
      "v_lshlrev_b32 v9, 4, v9\n"
      "v_lshl_add_u32 v8, v8, 10, v9\n"
      "s_waitcnt lgkmcnt(0)\n"
      ".Lpoll2_%=:\n"
      "buffer_load_dwordx4 v[112:115], v6, s[52:55], s48 offen offset:1008 sc1\n"
      "buffer_load_dword v7, v6, s[52:55], s48 offen offset:1276 sc1\n"
      "s_waitcnt vmcnt(0)\n"
      "v_bitop3_b32 v7, v7, v112, v113 bitop3:0x96\n"
      "v_bitop3_b32 v7, v7, v114, v115 bitop3:0x96\n"
      "v_cmp_eq_u32 vcc, v5, v7\n"
      "s_add_u32 s68, s68, 1\n"
      "s_cmp_gt_u32 s68, 0x20000\n"
      "s_cbranch_scc1 .Lpoll2_done_%=\n"
      "s_cmp_lg_u32 vcc_lo, -1\n"
      "s_cbranch_scc1 .Lpoll2_%=\n"
      ".Lpoll2_done_%=:\n"
      "s_mov_b32 s30, s48\n"
      "v_lshrrev_b32 v9, 2, v21\n"
      "buffer_load_dwordx4 v[32:35], v21, s[52:55], s30 offen sc1\n"
      "buffer_load_dword v36, v9, s[52:55], s30 offen offset:1024 sc1\n"
      "s_add_u32 s30, s30, 3840\n"
      "buffer_load_dwordx4 v[40:43], v21, s[52:55], s30 offen sc1\n"
      "buffer_load_dword v44, v9, s[52:55], s30 offen offset:1024 sc1\n"
      "s_add_u32 s30, s30, 3840\n"
      "buffer_load_dwordx4 v[48:51], v21, s[52:55], s30 offen sc1\n"
      "buffer_load_dword v52, v9, s[52:55], s30 offen offset:1024 sc1\n"
      "s_add_u32 s30, s30, 3840\n"
      "buffer_load_dwordx4 v[56:59], v21, s[52:55], s30 offen sc1\n"
      "buffer_load_dword v60, v9, s[52:55], s30 offen offset:1024 sc1\n"
      "s_add_u32 s30, s30, 3840\n"
      "buffer_load_dwordx4 v[64:67], v21, s[52:55], s30 offen sc1\n"
      "buffer_load_dword v68, v9, s[52:55], s30 offen offset:1024 sc1\n"
      "s_add_u32 s30, s30, 3840\n"
      "buffer_load_dwordx4 v[72:75], v21, s[52:55], s30 offen sc1\n"
      "buffer_load_dword v76, v9, s[52:55], s30 offen offset:1024 sc1\n"
      "s_add_u32 s30, s30, 3840\n"
      "buffer_load_dwordx4 v[80:83], v21, s[52:55], s30 offen sc1\n"
      "buffer_load_dword v84, v9, s[52:55], s30 offen offset:1024 sc1\n"
      "v_mov_b32 v7, 0\n"
      "s_waitcnt vmcnt(12)\n"
      "v_bitop3_b32 v36, v36, v32, v33 bitop3:0x96\n"
      "v_bitop3_b32 v36, v36, v34, v35 bitop3:0x96\n"
      "v_bitop3_b32 v7, v7, v36, v5 bitop3:0xf6\n"
      "v_and_b32 v39, 0xffff0000, v35\n"
      "v_lshlrev_b32 v38, 16, v35\n"
      "v_and_b32 v37, 0xffff0000, v34\n"
      "v_lshlrev_b32 v36, 16, v34\n"
      "v_and_b32 v35, 0xffff0000, v33\n"
      "v_lshlrev_b32 v34, 16, v33\n"
      "v_and_b32 v33, 0xffff0000, v32\n"
      "v_lshlrev_b32 v32, 16, v32\n"
      "v_pk_add_f32 v[88:89], v[104:105], v[32:33]\n"
      "v_pk_add_f32 v[90:91], v[106:107], v[34:35]\n"
      "v_pk_add_f32 v[92:93], v[108:109], v[36:37]\n"
      "v_pk_add_f32 v[94:95], v[110:111], v[38:39]\n"
      "s_waitcnt vmcnt(10)\n"
      "v_bitop3_b32 v44, v44, v40, v41 bitop3:0x96\n"
      "v_bitop3_b32 v44, v44, v42, v43 bitop3:0x96\n"
      "v_bitop3_b32 v7, v7, v44, v5 bitop3:0xf6\n"
      "v_and_b32 v47, 0xffff0000, v43\n"
      "v_lshlrev_b32 v46, 16, v43\n"
      "v_and_b32 v45, 0xffff0000, v42\n"
      "v_lshlrev_b32 v44, 16, v42\n"
      "v_and_b32 v43, 0xffff0000, v41\n"
      "v_lshlrev_b32 v42, 16, v41\n"
      "v_and_b32 v41, 0xffff0000, v40\n"
      "v_lshlrev_b32 v40, 16, v40\n"
      "v_pk_add_f32 v[88:89], v[88:89], v[40:41]\n"
      "v_pk_add_f32 v[90:91], v[90:91], v[42:43]\n"
      "v_pk_add_f32 v[92:93], v[92:93], v[44:45]\n"
      "v_pk_add_f32 v[94:95], v[94:95], v[46:47]\n"
      "s_waitcnt vmcnt(8)\n"
      "v_bitop3_b32 v52, v52, v48, v49 bitop3:0x96\n"
      "v_bitop3_b32 v52, v52, v50, v51 bitop3:0x96\n"
      "v_bitop3_b32 v7, v7, v52, v5 bitop3:0xf6\n"
      "v_and_b32 v55, 0xffff0000, v51\n"
      "v_lshlrev_b32 v54, 16, v51\n"
      "v_and_b32 v53, 0xffff0000, v50\n"
      "v_lshlrev_b32 v52, 16, v50\n"
      "v_and_b32 v51, 0xffff0000, v49\n"
      "v_lshlrev_b32 v50, 16, v49\n"
      "v_and_b32 v49, 0xffff0000, v48\n"
      "v_lshlrev_b32 v48, 16, v48\n"
      "v_pk_add_f32 v[88:89], v[88:89], v[48:49]\n"
      "v_pk_add_f32 v[90:91], v[90:91], v[50:51]\n"
      "v_pk_add_f32 v[92:93], v[92:93], v[52:53]\n"
      "v_pk_add_f32 v[94:95], v[94:95], v[54:55]\n"
      "s_waitcnt vmcnt(6)\n"
      "v_bitop3_b32 v60, v60, v56, v57 bitop3:0x96\n"
      "v_bitop3_b32 v60, v60, v58, v59 bitop3:0x96\n"
      "v_bitop3_b32 v7, v7, v60, v5 bitop3:0xf6\n"
      "v_and_b32 v63, 0xffff0000, v59\n"
      "v_lshlrev_b32 v62, 16, v59\n"
      "v_and_b32 v61, 0xffff0000, v58\n"
      "v_lshlrev_b32 v60, 16, v58\n"
      "v_and_b32 v59, 0xffff0000, v57\n"
      "v_lshlrev_b32 v58, 16, v57\n"
      "v_and_b32 v57, 0xffff0000, v56\n"
      "v_lshlrev_b32 v56, 16, v56\n"
      "v_pk_add_f32 v[88:89], v[88:89], v[56:57]\n"
      "v_pk_add_f32 v[90:91], v[90:91], v[58:59]\n"
      "v_pk_add_f32 v[92:93], v[92:93], v[60:61]\n"
      "v_pk_add_f32 v[94:95], v[94:95], v[62:63]\n"
      "s_waitcnt vmcnt(4)\n"
      "v_bitop3_b32 v68, v68, v64, v65 bitop3:0x96\n"
      "v_bitop3_b32 v68, v68, v66, v67 bitop3:0x96\n"
      "v_bitop3_b32 v7, v7, v68, v5 bitop3:0xf6\n"
      "v_and_b32 v71, 0xffff0000, v67\n"
      "v_lshlrev_b32 v70, 16, v67\n"
      "v_and_b32 v69, 0xffff0000, v66\n"
      "v_lshlrev_b32 v68, 16, v66\n"
      "v_and_b32 v67, 0xffff0000, v65\n"
      "v_lshlrev_b32 v66, 16, v65\n"
      "v_and_b32 v65, 0xffff0000, v64\n"
      "v_lshlrev_b32 v64, 16, v64\n"
      "v_pk_add_f32 v[88:89], v[88:89], v[64:65]\n"
      "v_pk_add_f32 v[90:91], v[90:91], v[66:67]\n"
      "v_pk_add_f32 v[92:93], v[92:93], v[68:69]\n"
      "v_pk_add_f32 v[94:95], v[94:95], v[70:71]\n"
      "s_waitcnt vmcnt(2)\n"
      "v_bitop3_b32 v76, v76, v72, v73 bitop3:0x96\n"
      "v_bitop3_b32 v76, v76, v74, v75 bitop3:0x96\n"
      "v_bitop3_b32 v7, v7, v76, v5 bitop3:0xf6\n"
      "v_and_b32 v79, 0xffff0000, v75\n"
      "v_lshlrev_b32 v78, 16, v75\n"
      "v_and_b32 v77, 0xffff0000, v74\n"
      "v_lshlrev_b32 v76, 16, v74\n"
      "v_and_b32 v75, 0xffff0000, v73\n"
      "v_lshlrev_b32 v74, 16, v73\n"
      "v_and_b32 v73, 0xffff0000, v72\n"
      "v_lshlrev_b32 v72, 16, v72\n"
      "v_pk_add_f32 v[88:89], v[88:89], v[72:73]\n"
      "v_pk_add_f32 v[90:91], v[90:91], v[74:75]\n"
      "v_pk_add_f32 v[92:93], v[92:93], v[76:77]\n"
      "v_pk_add_f32 v[94:95], v[94:95], v[78:79]\n"
      "s_waitcnt vmcnt(0)\n"
      "v_bitop3_b32 v84, v84, v80, v81 bitop3:0x96\n"
      "v_bitop3_b32 v84, v84, v82, v83 bitop3:0x96\n"
      "v_bitop3_b32 v7, v7, v84, v5 bitop3:0xf6\n"
      "v_and_b32 v87, 0xffff0000, v83\n"
      "v_lshlrev_b32 v86, 16, v83\n"
      "v_and_b32 v85, 0xffff0000, v82\n"
      "v_lshlrev_b32 v84, 16, v82\n"
      "v_and_b32 v83, 0xffff0000, v81\n"
      "v_lshlrev_b32 v82, 16, v81\n"
      "v_and_b32 v81, 0xffff0000, v80\n"
      "v_lshlrev_b32 v80, 16, v80\n"
      "v_pk_add_f32 v[88:89], v[88:89], v[80:81]\n"
      "v_pk_add_f32 v[90:91], v[90:91], v[82:83]\n"
      "v_pk_add_f32 v[92:93], v[92:93], v[84:85]\n"
      "v_pk_add_f32 v[94:95], v[94:95], v[86:87]\n"
      "v_cmp_ne_u32 vcc, 0, v7\n"
      "s_add_u32 s68, s68, 1\n"
      "s_cmp_gt_u32 s68, 0x20000\n"
      "s_cbranch_scc1 .Lverified_%=\n"
      "s_cbranch_vccnz .Lpoll2_done_%=\n"
      ".Lverified_%=:\n"
      "v_cvt_pk_bf16_f32 v100, v88, v89\n"
      "v_cvt_pk_bf16_f32 v101, v90, v91\n"
      "v_cvt_pk_bf16_f32 v102, v92, v93\n"
      "v_cvt_pk_bf16_f32 v103, v94, v95\n"
      "buffer_store_dwordx4 v[100:103], v8, s[56:59], s31 offen\n"
      "s_or_b32 s29, s21, s22\n"
      "s_or_b32 s29, s29, s20\n"
      "s_cmp_lg_u32 s29, 0\n"
      "s_cbranch_scc1 .Lnoepoch_%=\n"
      "s_lshl_b32 s29, s23, 7\n"
      "v_mov_b32 v2, s60\n"
      "v_mov_b32 v3, s61\n"
      "v_mov_b32 v4, 0\n"
      "s_mov_b64 exec, 1\n"
      "buffer_store_dwordx2 v[2:3], v4, s[52:55], s29 offen sc1\n"
      "s_mov_b64 exec, -1\n"
      ".Lnoepoch_%=:\n"
      "s_branch .Lend_%=\n"
      ".Lrare0_%=:\n"
      "s_nop 11\n"
      "v_max3_f32 v2, v160, v161, v162\n"
      "v_max3_f32 v3, v163, v164, v165\n"
      "v_max3_f32 v2, v2, v166, v167\n"
      "v_max3_f32 v3, v3, v168, v169\n"
      "v_max3_f32 v2, v2, v170, v171\n"
      "v_max3_f32 v3, v3, v172, v173\n"
      "v_max3_f32 v2, v2, v174, v175\n"
      "v_max_f32 v2, v2, v3\n"
      "v_mov_b32 v3, v2\n"
      "s_nop 1\n"
      "v_permlane32_swap_b32 v2, v3\n"
      "v_max_f32 v2, v2, v3\n"
      "v_max_f32 v2, 0, v2\n"
      "v_sub_f32 v3, 0, v2\n"
      "v_exp_f32 v4, v3\n"
      "v_exp_f32 v5, v3\n"
      "v_sub_f32 v208, v208, v2\n"
      "v_sub_f32 v160, v160, v2\n"
      "v_sub_f32 v176, v176, v2\n"
      "v_sub_f32 v209, v209, v2\n"
      "v_sub_f32 v161, v161, v2\n"
      "v_sub_f32 v177, v177, v2\n"
      "v_sub_f32 v210, v210, v2\n"
      "v_sub_f32 v162, v162, v2\n"
      "v_sub_f32 v178, v178, v2\n"
      "v_sub_f32 v211, v211, v2\n"
      "v_sub_f32 v163, v163, v2\n"
      "v_sub_f32 v179, v179, v2\n"
      "v_sub_f32 v212, v212, v2\n"
      "v_sub_f32 v164, v164, v2\n"
      "v_sub_f32 v180, v180, v2\n"
      "v_sub_f32 v213, v213, v2\n"
      "v_sub_f32 v165, v165, v2\n"
      "v_sub_f32 v181, v181, v2\n"
      "v_sub_f32 v214, v214, v2\n"
      "v_sub_f32 v166, v166, v2\n"
      "v_sub_f32 v182, v182, v2\n"
      "v_sub_f32 v215, v215, v2\n"
      "v_sub_f32 v167, v167, v2\n"
      "v_sub_f32 v183, v183, v2\n"
      "v_sub_f32 v216, v216, v2\n"
      "v_sub_f32 v168, v168, v2\n"
      "v_sub_f32 v184, v184, v2\n"
      "v_sub_f32 v217, v217, v2\n"
      "v_sub_f32 v169, v169, v2\n"
      "v_sub_f32 v185, v185, v2\n"
      "v_sub_f32 v218, v218, v2\n"
      "v_sub_f32 v170, v170, v2\n"
      "v_sub_f32 v186, v186, v2\n"
      "v_sub_f32 v219, v219, v2\n"
      "v_sub_f32 v171, v171, v2\n"
      "v_sub_f32 v187, v187, v2\n"
      "v_sub_f32 v220, v220, v2\n"
      "v_sub_f32 v172, v172, v2\n"
      "v_sub_f32 v188, v188, v2\n"
      "v_sub_f32 v221, v221, v2\n"
      "v_sub_f32 v173, v173, v2\n"
      "v_sub_f32 v189, v189, v2\n"
      "v_sub_f32 v222, v222, v2\n"
      "v_sub_f32 v174, v174, v2\n"
      "v_sub_f32 v190, v190, v2\n"
      "v_sub_f32 v223, v223, v2\n"
      "v_sub_f32 v175, v175, v2\n"
      "v_sub_f32 v191, v191, v2\n"
      "v_pk_mul_f32 v[64:65], v[64:65], v[4:5]\n"
      "v_pk_mul_f32 v[66:67], v[66:67], v[4:5]\n"
      "v_pk_mul_f32 v[68:69], v[68:69], v[4:5]\n"
      "v_pk_mul_f32 v[70:71], v[70:71], v[4:5]\n"
      "v_pk_mul_f32 v[72:73], v[72:73], v[4:5]\n"
      "v_pk_mul_f32 v[74:75], v[74:75], v[4:5]\n"
      "v_pk_mul_f32 v[76:77], v[76:77], v[4:5]\n"
      "v_pk_mul_f32 v[78:79], v[78:79], v[4:5]\n"
      "v_pk_mul_f32 v[80:81], v[80:81], v[4:5]\n"
      "v_pk_mul_f32 v[82:83], v[82:83], v[4:5]\n"
      "v_pk_mul_f32 v[84:85], v[84:85], v[4:5]\n"
      "v_pk_mul_f32 v[86:87], v[86:87], v[4:5]\n"
      "v_pk_mul_f32 v[88:89], v[88:89], v[4:5]\n"
      "v_pk_mul_f32 v[90:91], v[90:91], v[4:5]\n"
      "v_pk_mul_f32 v[92:93], v[92:93], v[4:5]\n"
      "v_pk_mul_f32 v[94:95], v[94:95], v[4:5]\n"
      "v_pk_mul_f32 v[96:97], v[96:97], v[4:5]\n"
      "v_pk_mul_f32 v[98:99], v[98:99], v[4:5]\n"
      "v_pk_mul_f32 v[100:101], v[100:101], v[4:5]\n"
      "v_pk_mul_f32 v[102:103], v[102:103], v[4:5]\n"
      "v_pk_mul_f32 v[104:105], v[104:105], v[4:5]\n"
      "v_pk_mul_f32 v[106:107], v[106:107], v[4:5]\n"
      "v_pk_mul_f32 v[108:109], v[108:109], v[4:5]\n"
      "v_pk_mul_f32 v[110:111], v[110:111], v[4:5]\n"
      "v_pk_mul_f32 v[112:113], v[112:113], v[4:5]\n"
      "v_pk_mul_f32 v[114:115], v[114:115], v[4:5]\n"
      "v_pk_mul_f32 v[116:117], v[116:117], v[4:5]\n"
      "v_pk_mul_f32 v[118:119], v[118:119], v[4:5]\n"
      "v_pk_mul_f32 v[120:121], v[120:121], v[4:5]\n"
      "v_pk_mul_f32 v[122:123], v[122:123], v[4:5]\n"
      "v_pk_mul_f32 v[124:125], v[124:125], v[4:5]\n"
      "v_pk_mul_f32 v[126:127], v[126:127], v[4:5]\n"
      "v_mul_f32 v16, v16, v4\n"
      "v_exp_f32 v192, v160\n"
      "v_exp_f32 v193, v161\n"
      "v_exp_f32 v194, v162\n"
      "v_exp_f32 v195, v163\n"
      "v_exp_f32 v196, v164\n"
      "v_exp_f32 v197, v165\n"
      "v_exp_f32 v198, v166\n"
      "v_exp_f32 v199, v167\n"
      "v_exp_f32 v200, v168\n"
      "v_exp_f32 v201, v169\n"
      "v_exp_f32 v202, v170\n"
      "v_exp_f32 v203, v171\n"
      "v_exp_f32 v204, v172\n"
      "v_exp_f32 v205, v173\n"
      "v_exp_f32 v206, v174\n"
      "v_exp_f32 v207, v175\n"
      "v_add_f32 v17, v192, v194\n"
      "v_add_f32 v18, v193, v195\n"
      "v_add_f32 v17, v17, v196\n"
      "v_add_f32 v18, v18, v197\n"
      "v_add_f32 v17, v17, v198\n"
      "v_add_f32 v18, v18, v199\n"
      "v_add_f32 v17, v17, v200\n"
      "v_add_f32 v18, v18, v201\n"
      "v_add_f32 v17, v17, v202\n"
      "v_add_f32 v18, v18, v203\n"
      "v_add_f32 v17, v17, v204\n"
      "v_add_f32 v18, v18, v205\n"
      "v_add_f32 v17, v17, v206\n"
      "v_add_f32 v18, v18, v207\n"
      "v_add_f32 v17, v17, v18\n"
      "v_cvt_pk_bf16_f32 v24, v192, v193\n"
      "v_cvt_pk_bf16_f32 v25, v194, v195\n"
      "v_cvt_pk_bf16_f32 v26, v196, v197\n"
      "v_cvt_pk_bf16_f32 v27, v198, v199\n"
      "v_cvt_pk_bf16_f32 v28, v200, v201\n"
      "v_cvt_pk_bf16_f32 v29, v202, v203\n"
      "v_cvt_pk_bf16_f32 v30, v204, v205\n"
      "v_cvt_pk_bf16_f32 v31, v206, v207\n"
      "s_nop 1\n"
      "s_setpc_b64 s[62:63]\n"
      ".Lrare1_%=:\n"
      "s_nop 11\n"
      "v_max3_f32 v2, v176, v177, v178\n"
      "v_max3_f32 v3, v179, v180, v181\n"
      "v_max3_f32 v2, v2, v182, v183\n"
      "v_max3_f32 v3, v3, v184, v185\n"
      "v_max3_f32 v2, v2, v186, v187\n"
      "v_max3_f32 v3, v3, v188, v189\n"
      "v_max3_f32 v2, v2, v190, v191\n"
      "v_max_f32 v2, v2, v3\n"
      "v_mov_b32 v3, v2\n"
      "s_nop 1\n"
      "v_permlane32_swap_b32 v2, v3\n"
      "v_max_f32 v2, v2, v3\n"
      "v_max_f32 v2, 0, v2\n"
      "v_sub_f32 v3, 0, v2\n"
      "v_exp_f32 v4, v3\n"
      "v_exp_f32 v5, v3\n"
      "v_sub_f32 v208, v208, v2\n"
      "v_sub_f32 v176, v176, v2\n"
      "v_sub_f32 v160, v160, v2\n"
      "v_sub_f32 v209, v209, v2\n"
      "v_sub_f32 v177, v177, v2\n"
      "v_sub_f32 v161, v161, v2\n"
      "v_sub_f32 v210, v210, v2\n"
      "v_sub_f32 v178, v178, v2\n"
      "v_sub_f32 v162, v162, v2\n"
      "v_sub_f32 v211, v211, v2\n"
      "v_sub_f32 v179, v179, v2\n"
      "v_sub_f32 v163, v163, v2\n"
      "v_sub_f32 v212, v212, v2\n"
      "v_sub_f32 v180, v180, v2\n"
      "v_sub_f32 v164, v164, v2\n"
      "v_sub_f32 v213, v213, v2\n"
      "v_sub_f32 v181, v181, v2\n"
      "v_sub_f32 v165, v165, v2\n"
      "v_sub_f32 v214, v214, v2\n"
      "v_sub_f32 v182, v182, v2\n"
      "v_sub_f32 v166, v166, v2\n"
      "v_sub_f32 v215, v215, v2\n"
      "v_sub_f32 v183, v183, v2\n"
      "v_sub_f32 v167, v167, v2\n"
      "v_sub_f32 v216, v216, v2\n"
      "v_sub_f32 v184, v184, v2\n"
      "v_sub_f32 v168, v168, v2\n"
      "v_sub_f32 v217, v217, v2\n"
      "v_sub_f32 v185, v185, v2\n"
      "v_sub_f32 v169, v169, v2\n"
      "v_sub_f32 v218, v218, v2\n"
      "v_sub_f32 v186, v186, v2\n"
      "v_sub_f32 v170, v170, v2\n"
      "v_sub_f32 v219, v219, v2\n"
      "v_sub_f32 v187, v187, v2\n"
      "v_sub_f32 v171, v171, v2\n"
      "v_sub_f32 v220, v220, v2\n"
      "v_sub_f32 v188, v188, v2\n"
      "v_sub_f32 v172, v172, v2\n"
      "v_sub_f32 v221, v221, v2\n"
      "v_sub_f32 v189, v189, v2\n"
      "v_sub_f32 v173, v173, v2\n"
      "v_sub_f32 v222, v222, v2\n"
      "v_sub_f32 v190, v190, v2\n"
      "v_sub_f32 v174, v174, v2\n"
      "v_sub_f32 v223, v223, v2\n"
      "v_sub_f32 v191, v191, v2\n"
      "v_sub_f32 v175, v175, v2\n"
      "v_pk_mul_f32 v[64:65], v[64:65], v[4:5]\n"
      "v_pk_mul_f32 v[66:67], v[66:67], v[4:5]\n"
      "v_pk_mul_f32 v[68:69], v[68:69], v[4:5]\n"
      "v_pk_mul_f32 v[70:71], v[70:71], v[4:5]\n"
      "v_pk_mul_f32 v[72:73], v[72:73], v[4:5]\n"
      "v_pk_mul_f32 v[74:75], v[74:75], v[4:5]\n"
      "v_pk_mul_f32 v[76:77], v[76:77], v[4:5]\n"
      "v_pk_mul_f32 v[78:79], v[78:79], v[4:5]\n"
      "v_pk_mul_f32 v[80:81], v[80:81], v[4:5]\n"
      "v_pk_mul_f32 v[82:83], v[82:83], v[4:5]\n"
      "v_pk_mul_f32 v[84:85], v[84:85], v[4:5]\n"
      "v_pk_mul_f32 v[86:87], v[86:87], v[4:5]\n"
      "v_pk_mul_f32 v[88:89], v[88:89], v[4:5]\n"
      "v_pk_mul_f32 v[90:91], v[90:91], v[4:5]\n"
      "v_pk_mul_f32 v[92:93], v[92:93], v[4:5]\n"
      "v_pk_mul_f32 v[94:95], v[94:95], v[4:5]\n"
      "v_pk_mul_f32 v[96:97], v[96:97], v[4:5]\n"
      "v_pk_mul_f32 v[98:99], v[98:99], v[4:5]\n"
      "v_pk_mul_f32 v[100:101], v[100:101], v[4:5]\n"
      "v_pk_mul_f32 v[102:103], v[102:103], v[4:5]\n"
      "v_pk_mul_f32 v[104:105], v[104:105], v[4:5]\n"
      "v_pk_mul_f32 v[106:107], v[106:107], v[4:5]\n"
      "v_pk_mul_f32 v[108:109], v[108:109], v[4:5]\n"
      "v_pk_mul_f32 v[110:111], v[110:111], v[4:5]\n"
      "v_pk_mul_f32 v[112:113], v[112:113], v[4:5]\n"
      "v_pk_mul_f32 v[114:115], v[114:115], v[4:5]\n"
      "v_pk_mul_f32 v[116:117], v[116:117], v[4:5]\n"
      "v_pk_mul_f32 v[118:119], v[118:119], v[4:5]\n"
      "v_pk_mul_f32 v[120:121], v[120:121], v[4:5]\n"
      "v_pk_mul_f32 v[122:123], v[122:123], v[4:5]\n"
      "v_pk_mul_f32 v[124:125], v[124:125], v[4:5]\n"
      "v_pk_mul_f32 v[126:127], v[126:127], v[4:5]\n"
      "v_mul_f32 v16, v16, v4\n"
      "v_exp_f32 v192, v176\n"
      "v_exp_f32 v193, v177\n"
      "v_exp_f32 v194, v178\n"
      "v_exp_f32 v195, v179\n"
      "v_exp_f32 v196, v180\n"
      "v_exp_f32 v197, v181\n"
      "v_exp_f32 v198, v182\n"
      "v_exp_f32 v199, v183\n"
      "v_exp_f32 v200, v184\n"
      "v_exp_f32 v201, v185\n"
      "v_exp_f32 v202, v186\n"
      "v_exp_f32 v203, v187\n"
      "v_exp_f32 v204, v188\n"
      "v_exp_f32 v205, v189\n"
      "v_exp_f32 v206, v190\n"
      "v_exp_f32 v207, v191\n"
      "v_add_f32 v17, v192, v194\n"
      "v_add_f32 v18, v193, v195\n"
      "v_add_f32 v17, v17, v196\n"
      "v_add_f32 v18, v18, v197\n"
      "v_add_f32 v17, v17, v198\n"
      "v_add_f32 v18, v18, v199\n"
      "v_add_f32 v17, v17, v200\n"
      "v_add_f32 v18, v18, v201\n"
      "v_add_f32 v17, v17, v202\n"
      "v_add_f32 v18, v18, v203\n"
      "v_add_f32 v17, v17, v204\n"
      "v_add_f32 v18, v18, v205\n"
      "v_add_f32 v17, v17, v206\n"
      "v_add_f32 v18, v18, v207\n"
      "v_add_f32 v17, v17, v18\n"
      "v_cvt_pk_bf16_f32 v24, v192, v193\n"
      "v_cvt_pk_bf16_f32 v25, v194, v195\n"
      "v_cvt_pk_bf16_f32 v26, v196, v197\n"
      "v_cvt_pk_bf16_f32 v27, v198, v199\n"
      "v_cvt_pk_bf16_f32 v28, v200, v201\n"
      "v_cvt_pk_bf16_f32 v29, v202, v203\n"
      "v_cvt_pk_bf16_f32 v30, v204, v205\n"
      "v_cvt_pk_bf16_f32 v31, v206, v207\n"
      "s_nop 1\n"
      "s_setpc_b64 s[62:63]\n"
      ".Lend_%=:\n"
      :
      : "s"(__builtin_amdgcn_kernarg_segment_ptr()), "s"((uint32_t)blockIdx.x), "v"((uint32_t)threadIdx.x),
        "s"((uint32_t)(size_t)lds)
      : "v1", "v2", "v3", "v4", "v5", "v6", "v7", "v8", "v9", "v10", "v11", "v12", "v13", "v14", "v15", "v16",
        "v17", "v18", "v19", "v20", "v21", "v22", "v23", "v24", "v25", "v26", "v27", "v28", "v29", "v30", "v31", "v32",
        "v33", "v34", "v35", "v36", "v37", "v38", "v39", "v40", "v41", "v42", "v43", "v44", "v45", "v46", "v47", "v48",
        "v49", "v50", "v51", "v52", "v53", "v54", "v55", "v56", "v57", "v58", "v59", "v60", "v61", "v62", "v63", "v64",
        "v65", "v66", "v67", "v68", "v69", "v70", "v71", "v72", "v73", "v74", "v75", "v76", "v77", "v78", "v79", "v80",
        "v81", "v82", "v83", "v84", "v85", "v86", "v87", "v88", "v89", "v90", "v91", "v92", "v93", "v94", "v95", "v96",
        "v97", "v98", "v99", "v100", "v101", "v102", "v103", "v104", "v105", "v106", "v107", "v108", "v109", "v110", "v111", "v112",
        "v113", "v114", "v115", "v116", "v117", "v118", "v119", "v120", "v121", "v122", "v123", "v124", "v125", "v126", "v127", "v128",
        "v129", "v130", "v131", "v132", "v133", "v134", "v135", "v136", "v137", "v138", "v139", "v140", "v141", "v142", "v143", "v144",
        "v145", "v146", "v147", "v148", "v149", "v150", "v151", "v152", "v153", "v154", "v155", "v156", "v157", "v158", "v159", "v160",
        "v161", "v162", "v163", "v164", "v165", "v166", "v167", "v168", "v169", "v170", "v171", "v172", "v173", "v174", "v175", "v176",
        "v177", "v178", "v179", "v180", "v181", "v182", "v183", "v184", "v185", "v186", "v187", "v188", "v189", "v190", "v191", "v192",
        "v193", "v194", "v195", "v196", "v197", "v198", "v199", "v200", "v201", "v202", "v203", "v204", "v205", "v206", "v207", "v208",
        "v209", "v210", "v211", "v212", "v213", "v214", "v215", "v216", "v217", "v218", "v219", "v220", "v221", "v222", "v223", "v224",
        "v225", "v226", "v227", "v228", "v229", "v230", "v231", "v232", "v233", "v234", "v235", "v236", "v237", "v238", "v239", "v240",
        "v241", "v242", "v243", "v244", "v245", "v246", "v247", "v248", "v249", "v250", "v251", "v252", "v253", "v254", "v255", "a0",
        "a1", "a2", "a3", "a4", "a5", "a6", "a7", "a8", "a9", "a10", "a11", "a12", "a13", "a14", "a15", "a16",
        "a17", "a18", "a19", "a20", "a21", "a22", "a23", "a24", "a25", "a26", "a27", "a28", "a29", "a30", "a31", "a32",
        "a33", "a34", "a35", "a36", "a37", "a38", "a39", "a40", "a41", "a42", "a43", "a44", "a45", "a46", "a47", "a48",
        "a49", "a50", "a51", "a52", "a53", "a54", "a55", "a56", "a57", "a58", "a59", "a60", "a61", "a62", "a63", "a64",
        "a65", "a66", "a67", "a68", "a69", "a70", "a71", "a72", "a73", "a74", "a75", "a76", "a77", "a78", "a79", "a80",
        "a81", "a82", "a83", "a84", "a85", "a86", "a87", "a88", "a89", "a90", "a91", "a92", "a93", "a94", "a95", "a96",
        "a97", "a98", "a99", "a100", "a101", "a102", "a103", "a104", "a105", "a106", "a107", "a108", "a109", "a110", "a111", "a112",
        "a113", "a114", "a115", "a116", "a117", "a118", "a119", "a120", "a121", "a122", "a123", "a124", "a125", "a126", "a127", "a128",
        "a129", "a130", "a131", "a132", "a133", "a134", "a135", "a136", "a137", "a138", "a139", "a140", "a141", "a142", "a143", "a144",
        "a145", "a146", "a147", "a148", "a149", "a150", "a151", "a152", "a153", "a154", "a155", "a156", "a157", "a158", "a159", "a160",
        "a161", "a162", "a163", "a164", "a165", "a166", "a167", "a168", "a169", "a170", "a171", "a172", "a173", "a174", "a175", "a176",
        "a177", "a178", "a179", "a180", "a181", "a182", "a183", "a184", "a185", "a186", "a187", "a188", "a189", "a190", "a191", "a192",
        "a193", "a194", "a195", "a196", "a197", "a198", "a199", "a200", "a201", "a202", "a203", "a204", "a205", "a206", "a207", "a208",
        "a209", "a210", "a211", "a212", "a213", "a214", "a215", "a216", "a217", "a218", "a219", "a220", "a221", "a222", "a223", "a224",
        "a225", "a226", "a227", "a228", "a229", "a230", "a231", "a232", "a233", "a234", "a235", "a236", "a237", "a238", "a239", "a240",
        "a241", "a242", "a243", "a244", "a245", "a246", "a247", "a248", "a249", "a250", "a251", "a252", "a253", "a254", "a255", "s4",
        "s5", "s6", "s7", "s8", "s9", "s10", "s11", "s12", "s13", "s14", "s15", "s16", "s17", "s18", "s19", "s20",
        "s21", "s22", "s23", "s24", "s25", "s26", "s27", "s28", "s29", "s30", "s31", "s35", "s36", "s37", "s38", "s39",
        "s40", "s41", "s42", "s43", "s44", "s45", "s46", "s47", "s48", "s49", "s50", "s51", "s52", "s53", "s54", "s55",
        "s56", "s57", "s58", "s59", "s60", "s61", "s62", "s63", "s64", "s65", "s66", "s67", "s68", "s69", "s70", "s71",
        "s72", "s73", "s74", "s75", "s76", "s77", "s78", "s79", "s80", "s81", "s82", "s83", "s84", "s85", "s86", "s87",
        "s88", "s89", "s90", "s91", "s92", "s93", "s94", "s95", "s96", "s97", "s98", "s99", "vcc", "memory");
}

extern "C" void kernel_launch(void* const* d_in, const int* in_sizes, int n_in, void* d_out, int out_size, void* d_ws,
                              size_t ws_size, hipStream_t stream) {
  mha_block_cached_kernel<<<256, 256, 0, stream>>>(d_in[0], d_in[1], d_in[2], d_in[3], d_in[4], d_out, d_ws);
}
```
